```python
import torch

def _softmax(x, axis=-1):
    return torch.softmax(x, dim=axis)
def _gelu(x):
    return torch.nn.functional.gelu(x, approximate="tanh")
def _silu(x):
    return torch.nn.functional.silu(x)
def _sigmoid(x):
    return torch.sigmoid(x)
def _layernorm(x, axis=-1, eps=1e-5):
    m = x.mean(dim=axis, keepdim=True)
    v = x.var(dim=axis, keepdim=True, unbiased=False)
    return (x - m) / torch.sqrt(v + eps)
def _scatter_add(tgt, idx, src, axis=0):
    return tgt.clone().index_add_(axis, idx, src)
def _segment_sum(src, seg, n):
    out = torch.zeros((n,) + src.shape[1:], dtype=src.dtype)
    return out.index_add_(0, seg, src)
def _pad(x, pw):
    flat = tuple(v for pair in reversed(pw) for v in pair)
    return torch.nn.functional.pad(x, flat)

kernel_name = 'sequential_preLN__mha_block__mixer_token_mlp__prompt'

def setup_inputs():

    _w = (lambda a: (lambda t: t.to(torch.bfloat16) if t.is_floating_point() else t)(torch.as_tensor(a)))
    def _A_setup():
        B, S, D, H = 1, 2048, 448, 16
        return {
            "x": _w((lambda *s: torch.randn(*s, dtype=torch.bfloat16) * 0.05)(B, S, D)),
            "wqkv": _w((lambda *s: torch.randn(*s, dtype=torch.bfloat16) * 0.05)(D, 3 * D)),
            "wo": _w((lambda *s: torch.randn(*s, dtype=torch.bfloat16) * 0.05)(D, D)),
        }
    _A_in = _A_setup()

    def _B_setup():
        B, S, D, F = 1, 2048, 448, 1792
        return {
            "x": _w((lambda *s: torch.randn(*s, dtype=torch.bfloat16) * 0.05)(B, S, D)),
            "w1": _w((lambda *s: torch.randn(*s, dtype=torch.bfloat16) * 0.8)(S, F) * (S ** -0.5)),
            "b1": _w((lambda *s: torch.randn(*s, dtype=torch.bfloat16) * 12.8)(F) * 0.02),
            "w2": _w((lambda *s: torch.randn(*s, dtype=torch.bfloat16) * 0.2)(F, S) * (F ** -0.5)),
            "b2": _w((lambda *s: torch.randn(*s, dtype=torch.bfloat16) * 1.6)(S) * 0.02),
        }
    _B_in = _B_setup()

    return {
        "A_x": _A_in["x"],
        "A_wqkv": _A_in["wqkv"],
        "A_wo": _A_in["wo"],
        "B_x": _B_in["x"],
        "B_w1": _B_in["w1"],
        "B_b1": _B_in["b1"],
        "B_w2": _B_in["w2"],
        "B_b2": _B_in["b2"],
    }

def reference(A_x, A_wqkv, A_wo, B_x, B_w1, B_b1, B_w2, B_b2):
    def _A_ref(x, wqkv, wo):
        B, S, D = x.shape
        H = 16
        Dh = D // H
        qkv = (lambda x, *d: x.permute(*d))((x @ wqkv).reshape(B, S, 3, H, Dh), 2, 0, 3, 1, 4)
        q, k, v = qkv[0], qkv[1], qkv[2]
        scores = torch.einsum("bhqd,bhkd->bhqk", q, k) * (Dh ** -0.5)
        p = _softmax(scores, axis=-1)
        o = torch.einsum("bhqk,bhkd->bhqd", p, v)
        return (lambda x, *d: x.permute(*d))(o, 0, 2, 1, 3).reshape(B, S, D) @ wo

    def _B_ref(x, w1, b1, w2, b2):
        B, S, D = x.shape
        h = _layernorm(x, axis=-1)
        u = torch.einsum("bsd,sf->bdf", h, w1) + b1
        u = _gelu(u)
        y = torch.einsum("bdf,fs->bds", u, w2) + b2
        return (lambda x, *d: x.permute(*d))(y, 0, 2, 1)

    x = A_x
    h = x + _A_ref(_layernorm(x, axis=-1), A_wqkv, A_wo)
    h2 = h + _B_ref(_layernorm(h, axis=-1), B_w1, B_b1, B_w2, B_b2)
    return h2
```

```cpp
#include <hip/hip_runtime.h>
#include <hip/hip_bf16.h>
typedef __hip_bfloat16 bf16;
constexpr int S=2048, D=448, H=16, DH=28, F=1792;
__device__ inline float b2f(bf16 x){return __bfloat162float(x);}
__device__ inline float wsum(float v){ for(int o=32;o>0;o>>=1) v+=__shfl_xor(v,o); return v;}
__global__ void ln_k(const bf16* x, const float* xf, bf16* out, int twice){
  int row=blockIdx.x, l=threadIdx.x; float v[7];
  for(int i=0;i<7;i++) v[i]= xf? xf[row*D+l+64*i] : b2f(x[row*D+l+64*i]);
  for(int t=0;t<=twice;t++){
    float s=0; for(int i=0;i<7;i++) s+=v[i]; s=wsum(s)/D;
    float q=0; for(int i=0;i<7;i++){v[i]-=s; q+=v[i]*v[i];} q=wsum(q)/D;
    float r=1.0f/sqrtf(q+1e-5f); for(int i=0;i<7;i++) v[i]*=r;
  }
  for(int i=0;i<7;i++) out[row*D+l+64*i]=__float2bfloat16(v[i]);
}
__global__ void qkv_k(const bf16* xn,const bf16* w, bf16* qkv){
  int n=blockIdx.x*64+threadIdx.x; int row=blockIdx.y; float a=0;
  for(int k=0;k<D;k++) a+=b2f(xn[row*D+k])*b2f(w[k*3*D+n]);
  qkv[row*3*D+n]=__float2bfloat16(a);
}
__global__ void attn_k(const bf16* qkv, bf16* o){
  int row=blockIdx.x*64+threadIdx.x; int h=blockIdx.y; float q[DH], acc[DH];
  for(int d=0;d<DH;d++){q[d]=b2f(qkv[row*3*D+h*DH+d])*0.18898223650461362f; acc[d]=0;}
  float m=-1e30f,l=0;
  for(int j=0;j<S;j++){ float s=0; for(int d=0;d<DH;d++) s+=q[d]*b2f(qkv[j*3*D+D+h*DH+d]);
    float mn=fmaxf(m,s); float al=__expf(m-mn), p=__expf(s-mn); l=l*al+p;
    for(int d=0;d<DH;d++) acc[d]=acc[d]*al+p*b2f(qkv[j*3*D+2*D+h*DH+d]); m=mn; }
  for(int d=0;d<DH;d++) o[row*D+h*DH+d]=__float2bfloat16(acc[d]/l);
}
__global__ void proj_k(const bf16* o,const bf16* w,const bf16* x,float* h){
  int n=blockIdx.x*64+threadIdx.x; int row=blockIdx.y; float a=0;
  for(int k=0;k<D;k++) a+=b2f(o[row*D+k])*b2f(w[k*D+n]);
  h[row*D+n]=a+b2f(x[row*D+n]);
}
__global__ void g1_k(const bf16* hn,const bf16* w1,const bf16* b1,bf16* u){
  int f=blockIdx.x*64+threadIdx.x; int d=blockIdx.y; float a=0;
  for(int s=0;s<S;s++) a+=b2f(hn[s*D+d])*b2f(w1[s*F+f]);
  a+=b2f(b1[f]); float t=0.7978845608028654f*(a+0.044715f*a*a*a); a=0.5f*a*(1.f+tanhf(t));
  u[d*F+f]=__float2bfloat16(a);
}
__global__ void g2_k(const bf16* u,const bf16* w2,const bf16* b2,const float* h,bf16* out){
  int s=blockIdx.x*64+threadIdx.x; int d=blockIdx.y; float a=0;
  for(int f=0;f<F;f++) a+=b2f(u[d*F+f])*b2f(w2[f*S+s]);
  out[s*D+d]=__float2bfloat16(a+b2f(b2[s])+h[s*D+d]);
}
extern "C" void kernel_launch(void* const* d_in, const int* in_sizes, int n_in, void* d_out, int out_size, void* d_ws, size_t ws_size, hipStream_t stream){
  const bf16* x=(const bf16*)d_in[0]; const bf16* wqkv=(const bf16*)d_in[1]; const bf16* wo=(const bf16*)d_in[2];
  const bf16* w1=(const bf16*)d_in[4]; const bf16* b1=(const bf16*)d_in[5]; const bf16* w2=(const bf16*)d_in[6]; const bf16* b2=(const bf16*)d_in[7];
  char* ws=(char*)d_ws; bf16* xn=(bf16*)ws; bf16* qkv=(bf16*)(ws+(4<<20)); bf16* o=(bf16*)(ws+(12<<20)); float* h=(float*)(ws+(16<<20)); bf16* hn=(bf16*)(ws+(24<<20)); bf16* u=(bf16*)(ws+(28<<20));
  ln_k<<<S,64,0,stream>>>(x,nullptr,xn,0);
  qkv_k<<<dim3(3*D/64,S),64,0,stream>>>(xn,wqkv,qkv);
  attn_k<<<dim3(S/64,H),64,0,stream>>>(qkv,o);
  proj_k<<<dim3(D/64,S),64,0,stream>>>(o,wo,x,h);
  ln_k<<<S,64,0,stream>>>(nullptr,h,hn,1);
  g1_k<<<dim3(F/64,D),64,0,stream>>>(hn,w1,b1,u);
  g2_k<<<dim3(S/64,D),64,0,stream>>>(u,w2,b2,h,(bf16*)d_out);
}
```

```cpp
#include <hip/hip_runtime.h>
#include <hip/hip_bf16.h>
#include <utility>

typedef unsigned u32;
typedef unsigned long long u64;
typedef float f32x16 __attribute__((ext_vector_type(16)));
typedef float f32x4 __attribute__((ext_vector_type(4)));
typedef float f32x2 __attribute__((ext_vector_type(2)));
typedef __bf16 bf16x8 __attribute__((ext_vector_type(8)));
typedef __bf16 bf16x2 __attribute__((ext_vector_type(2)));
typedef short s16x4 __attribute__((ext_vector_type(4)));
typedef u32 u32x4 __attribute__((ext_vector_type(4)));
typedef u32 u32x2 __attribute__((ext_vector_type(2)));
typedef __amdgpu_buffer_rsrc_t rsrc_t;
#define LAS __attribute__((address_space(3)))
#define DI __device__ __forceinline__
#define SBAR() __builtin_amdgcn_sched_barrier(0)
#define MFMA(a, b, c) __builtin_amdgcn_mfma_f32_32x32x16_bf16((a), (b), (c), 0, 0, 0)

constexpr int SEQ = 2048, DM = 448, FF = 1792;
constexpr u32 WS_F1 = 4096;
constexpr u32 WS_F2 = WS_F1 + 131072;
constexpr u32 WS_F3 = WS_F2 + 131072;
constexpr u32 WS_F4 = WS_F3 + 131072;
constexpr u32 WS_ST = WS_F4 + 131072;
constexpr u32 WS_KV = 1u << 20;
constexpr u32 WS_OF = 5u << 20;
constexpr u32 WS_HN = 7u << 20;
constexpr u32 WS_HR = 9u << 20;
constexpr u32 WS_UF = 11u << 20;
constexpr u32 WS_SIZE = 13u << 20;
constexpr u32 TAGX = 0x9E3779B9u;
constexpr int SC1 = 16;
constexpr u32 LDS_BYTES = 163840;
constexpr u32 L_SLOT = 28672;
constexpr u32 L_CS = 143360;
constexpr u32 L_RS = L_CS + 512;
constexpr u32 L_R = 0;
constexpr u32 L_Z = 114688;
constexpr u32 SPIN_CAP = 1u << 20;

#define TS(i) do {} while (0)

template <int... I, class F> DI void static_for_impl(std::integer_sequence<int, I...>, F&& f) { (f(std::integral_constant<int, I>{}), ...); }
template <int N, class F> DI void static_for(F&& f) { static_for_impl(std::make_integer_sequence<int, N>{}, f); }
#define INL __attribute__((always_inline))
DI u32 rfl(u32 x) { return __builtin_amdgcn_readfirstlane(x); }
DI u32 cvtpk(float lo, float hi) { f32x2 v = {lo, hi}; return __builtin_bit_cast(u32, __builtin_convertvector(v, bf16x2)); }
DI float dot2(u32 a, u32 b, float c) { return __builtin_amdgcn_fdot2_f32_bf16(__builtin_bit_cast(bf16x2, a), __builtin_bit_cast(bf16x2, b), c, false); }
DI bf16x8 asbf(u32x4 v) { return __builtin_bit_cast(bf16x8, v); }
DI u32x4 asu(bf16x8 v) { return __builtin_bit_cast(u32x4, v); }
DI float halves_sum(float v) { auto r = __builtin_amdgcn_permlane32_swap(__float_as_uint(v), __float_as_uint(v), false, false); return __uint_as_float(r[0]) + __uint_as_float(r[1]); }
DI float halves_max(float v) { auto r = __builtin_amdgcn_permlane32_swap(__float_as_uint(v), __float_as_uint(v), false, false); return fmaxf(__uint_as_float(r[0]), __uint_as_float(r[1])); }
DI rsrc_t mkrsrc(const void* p, u32 bytes) { return __builtin_amdgcn_make_buffer_rsrc((void*)p, 0, bytes, 0x00020000); }
DI bf16x8 lds_frag(u32 a) { return *(const LAS bf16x8*)a; }
DI f32x4 lds_f4(u32 a) { return *(const LAS f32x4*)a; }
DI bf16x8 lds_tr2(u32 a0, u32 a1) {
  s16x4 lo = __builtin_amdgcn_ds_read_tr16_b64_v4i16((LAS s16x4*)a0), hi = __builtin_amdgcn_ds_read_tr16_b64_v4i16((LAS s16x4*)a1);
  return __builtin_bit_cast(bf16x8, __builtin_shufflevector(lo, hi, 0, 1, 2, 3, 4, 5, 6, 7));
}
DI void mfma_v0(f32x16& d, bf16x8 a, bf16x8 b) { asm volatile("v_mfma_f32_32x32x16_bf16 %0, %1, %2, 0" : "=&v"(d) : "v"(a), "v"(b)); }
DI void mfma_v1(f32x16& d, bf16x8 a, bf16x8 b) { asm volatile("v_mfma_f32_32x32x16_bf16 %0, %1, %2, %0" : "+v"(d) : "v"(a), "v"(b)); }
DI void dma16(rsrc_t r, u32 voff, u32 soff, u32 m0v) {
  asm volatile("s_mov_b32 m0, %3\n\ts_nop 0\n\tbuffer_load_dwordx4 %0, %1, %2 offen lds" :: "v"(voff), "s"(r), "s"(soff), "s"(m0v) : "memory");
}
DI void dma16x4_sc1(rsrc_t r, u32 voff, u32 soff, u32 m0v) {
  asm volatile("s_mov_b32 m0, %3\n\ts_nop 0\n\tbuffer_load_dwordx4 %0, %1, %2 offen sc1 lds\n\tbuffer_load_dwordx4 %0, %1, %2 offen offset:1024 sc1 lds\n\t"
               "buffer_load_dwordx4 %0, %1, %2 offen offset:2048 sc1 lds\n\tbuffer_load_dwordx4 %0, %1, %2 offen offset:3072 sc1 lds" :: "v"(voff), "s"(r), "s"(soff), "s"(m0v) : "memory");
}
#define WAIT_VM(n) asm volatile("s_waitcnt vmcnt(%0)" :: "i"(n) : "memory")
#define BARRIER() asm volatile("s_waitcnt lgkmcnt(0)\n\ts_barrier" ::: "memory")
#define WAIT_VM_BAR(n) asm volatile("s_waitcnt vmcnt(%0) lgkmcnt(0)\n\ts_barrier" :: "i"(n) : "memory")
DI void set_flag(rsrc_t ws, u32 off, u32 tag, u32 lane) {
  u32x2 f = {tag, tag ^ TAGX};
  if (lane == 0) __builtin_amdgcn_raw_buffer_store_b64(f, ws, off, 0, SC1);
}
DI void wait_flags(rsrc_t ws, u32 voff, u32 tag) {
  for (u32 spin = 0; spin < SPIN_CAP; ++spin) {
    u32x2 f = __builtin_amdgcn_raw_buffer_load_b64(ws, voff, 0, SC1);
    if (__all(f.x == tag && f.y == (tag ^ TAGX))) break;
    __builtin_amdgcn_s_sleep(1);
    asm volatile("" ::: "memory");
  }
  asm volatile("" ::: "memory");
}
DI bf16x8 gld_sc1(rsrc_t ws, u32 voff, u32 soff) { return asbf(__builtin_amdgcn_raw_buffer_load_b128(ws, voff, soff, SC1)); }
DI u32x4 pack8(const f32x16& v, int s) { return u32x4{cvtpk(v[8 * s], v[8 * s + 1]), cvtpk(v[8 * s + 2], v[8 * s + 3]), cvtpk(v[8 * s + 4], v[8 * s + 5]), cvtpk(v[8 * s + 6], v[8 * s + 7])}; }
DI float bflo(u32 v) { return __uint_as_float(v << 16); }
DI float bfhi(u32 v) { return __uint_as_float(v & 0xffff0000u); }
DI f32x16 reduce4(const f32x16 (&acc)[4], u32 lz, u32 w, u32 lane) {
#pragma unroll
  for (int a = 1; a < 4; ++a) {
    const u32 base = lz + (((w ^ a) * 3 + (a - 1)) << 12) + lane * 16;
#pragma unroll
    for (int q = 0; q < 4; ++q) *(LAS f32x4*)(base + q * 1024) = f32x4{acc[a][4 * q], acc[a][4 * q + 1], acc[a][4 * q + 2], acc[a][4 * q + 3]};
  }
  BARRIER();
  f32x16 r = acc[0];
  const u32 rb = lz + w * 12288 + lane * 16;
#pragma unroll
  for (int a = 0; a < 3; ++a)
#pragma unroll
    for (int q = 0; q < 4; ++q) { const f32x4 p = lds_f4(rb + a * 4096 + q * 1024); r[4 * q] += p[0]; r[4 * q + 1] += p[1]; r[4 * q + 2] += p[2]; r[4 * q + 3] += p[3]; }
  return r;
}

#define KVD 3
#define HND 10

__global__ void __launch_bounds__(256, 1)
fused_block(const u32* __restrict__ x, const u32* __restrict__ wqkv, const u32* __restrict__ wo, const u32* __restrict__ w1, const u32* __restrict__ b1,
            const u32* __restrict__ w2, const u32* __restrict__ b2, unsigned short* __restrict__ out, char* __restrict__ wsp) {
  extern __shared__ __attribute__((aligned(16))) char smem[];
  const u32 L0 = (u32)(size_t)(LAS char*)smem;
  const u32 lane = threadIdx.x & 63, w = rfl(threadIdx.x >> 6);
  const u32 r32 = lane & 31, hh = lane >> 5;
  const u32 id = blockIdx.x, xg = id & 7, slot = id >> 3;
  TS(0);
  const rsrc_t rws = mkrsrc(wsp, WS_SIZE), rx = mkrsrc(x, SEQ * DM * 2), rwq = mkrsrc(wqkv, DM * 3 * DM * 2);
  const u32 epoch = __builtin_amdgcn_raw_buffer_load_b32(rws, 0, 0, SC1);
  const u32 tq = (lane & 15) >> 2, tcol = ((lane >> 4) & 1) * 32 + (lane & 3) * 8;

  const u32 h = 4 * (xg >> 1) + (slot & 3), rb = (xg & 1) * 8 + (slot >> 2);
  u32x4 kf0[2], vf0[2], qf0[2];
  {
    const u32 xrow = lane >> 3;
    const u32 vx0 = (rb * 128 + w * 32 + xrow) * 896 + (((lane & 7) ^ (xrow >> 1)) << 4);
    const u32 vx1 = (rb * 128 + w * 32 + 8 + xrow) * 896 + (((lane & 7) ^ (4 + (xrow >> 1))) << 4);
    const u32 vw = (lane >> 2) * 2688 + (lane & 3) * 16 + h * 56;
    u32 sw[3], mw[3];
#pragma unroll
    for (int i = 0; i < 3; ++i) { const u32 pc = 3 * w + i; sw[i] = (pc & 3) * 16 * 2688 + (pc >> 2) * 896; mw[i] = L0 + 16384 + pc * 1024; }
    const u32 mx = L0 + w * 4096;
    auto issue = [&](int c) INL {
      const u32 so = (c % 5) * L_SLOT;
      dma16(rx, vx0, c * 128, mx + so); dma16(rx, vx1, c * 128, mx + so + 1024);
      dma16(rx, vx0, c * 128 + 16 * 896, mx + so + 2048); dma16(rx, vx1, c * 128 + 16 * 896, mx + so + 3072);
#pragma unroll
      for (int i = 0; i < 3; ++i) dma16(rwq, vw, sw[i] + c * 64 * 2688, mw[i] + so);
    };
#pragma unroll
    for (int c = 0; c < 5; ++c) issue(c);
    TS(1);
    u32 xa[4];
#pragma unroll
    for (int i = 0; i < 4; ++i) xa[i] = L0 + w * 4096 + r32 * 128 + (((2 * i + hh) ^ ((r32 >> 1) & 7)) << 4);
    const u32 wa = L0 + 16384 + (8 * hh + tq) * 64 + tcol, wc = wa + (w == 3 ? 0 : w) * 4096;
    f32x16 aq = {}, ak = {}, av = {};
    float sum = 0.f, sq = 0.f, cs = 0.f;
#pragma unroll
    for (int c = 0; c < 7; ++c) {
      if (c == 0) WAIT_VM_BAR(28); else if (c <= 3) WAIT_VM_BAR(21); else if (c == 4) WAIT_VM_BAR(14); else if (c == 5) WAIT_VM_BAR(7); else WAIT_VM_BAR(0);
      if (c == 0) TS(2);
      if (c == 1 || c == 2) issue(c + 4);
      const u32 so = (c % 5) * L_SLOT;
#pragma unroll
      for (int i = 0; i < 4; ++i) {
        const bf16x8 xf = lds_frag(xa[i] + so);
        const u32 wo_ = so + i * 1024;
        const bf16x8 fk = lds_tr2(wa + wo_ + 4096, wa + wo_ + 4096 + 256), fv = lds_tr2(wa + wo_ + 8192, wa + wo_ + 8192 + 256), fq = lds_tr2(wa + wo_, wa + wo_ + 256);
        const bf16x8 fc = lds_tr2(wc + wo_, wc + wo_ + 256);
        ak = MFMA(fk, xf, ak); av = MFMA(xf, fv, av); aq = MFMA(fq, xf, aq);
        const u32x4 xu = asu(xf), cu = asu(fc);
#pragma unroll
        for (int e = 0; e < 4; ++e) { sum = dot2(xu[e], 0x3F803F80u, sum); sq = dot2(xu[e], xu[e], sq); cs = dot2(cu[e], 0x3F803F80u, cs); }
      }
    }
    TS(3);
    sum = halves_sum(sum); sq = halves_sum(sq); cs = halves_sum(cs);
    const float mean = sum * (1.f / DM), var = fmaxf(sq * (1.f / DM) - mean * mean, 0.f), ra = __builtin_amdgcn_rsqf(var + 1e-5f), rbm = ra * mean;
    if (w < 3 && hh == 0) *(LAS float*)(L0 + L_CS + w * 128 + r32 * 4) = cs;
    *(LAS float*)(L0 + L_RS + w * 256 + hh * 128 + r32 * 4) = hh ? rbm : ra;
    BARRIER();
    f32x16 t;
#pragma unroll
    for (int g = 0; g < 4; ++g) { const f32x4 c4 = lds_f4(L0 + L_CS + 128 + g * 32 + hh * 16);
#pragma unroll
      for (int e = 0; e < 4; ++e) t[4 * g + e] = ra * ak[4 * g + e] - rbm * c4[e]; }
    kf0[0] = pack8(t, 0); kf0[1] = pack8(t, 1);
    if (hh) { kf0[1][2] = 0x3F803F80u; kf0[1][3] = 0x00003F80u; }
    const u32 kvo = WS_KV + ((h * 64 + rb * 4 + w) << 12) + lane * 16;
    __builtin_amdgcn_raw_buffer_store_b128(kf0[0], rws, kvo, 0, SC1); __builtin_amdgcn_raw_buffer_store_b128(kf0[1], rws, kvo + 1024, 0, SC1);
    const float cv = *(const LAS float*)(L0 + L_CS + 256 + r32 * 4);
#pragma unroll
    for (int g = 0; g < 4; ++g) { const f32x4 a4 = lds_f4(L0 + L_RS + w * 256 + g * 32 + hh * 16), b4 = lds_f4(L0 + L_RS + w * 256 + 128 + g * 32 + hh * 16);
#pragma unroll
      for (int e = 0; e < 4; ++e) t[4 * g + e] = a4[e] * av[4 * g + e] - b4[e] * cv; }
    vf0[0] = pack8(t, 0); vf0[1] = pack8(t, 1);
    if (r32 >= 28) { const u32 pv = r32 == 28 ? 0x3F803F80u : 0u; vf0[0] = u32x4{pv, pv, pv, pv}; vf0[1] = vf0[0]; }
    __builtin_amdgcn_raw_buffer_store_b128(vf0[0], rws, kvo + 2048, 0, SC1); __builtin_amdgcn_raw_buffer_store_b128(vf0[1], rws, kvo + 3072, 0, SC1);
    const float qs = 1.4426950408889634f / 5.291502622129181f, qa = ra * qs, qb = rbm * qs;
#pragma unroll
    for (int g = 0; g < 4; ++g) { const f32x4 c4 = lds_f4(L0 + L_CS + g * 32 + hh * 16);
#pragma unroll
      for (int e = 0; e < 4; ++e) t[4 * g + e] = qa * aq[4 * g + e] - qb * c4[e]; }
    qf0[0] = pack8(t, 0); qf0[1] = pack8(t, 1);
    if (hh) { qf0[1][2] = 0u; qf0[1][3] = 0u; }
    *(LAS u32x4*)(L0 + L_Z + w * 2048 + lane * 16) = qf0[0]; *(LAS u32x4*)(L0 + L_Z + w * 2048 + 1024 + lane * 16) = qf0[1];
  }
  const u32 tag = rfl(epoch) + 1;
  TS(4);
  WAIT_VM(0);
  set_flag(rws, WS_F1 + (((h * 16 + rb) * 4 + w) << 7), tag, lane);
  BARRIER();
  TS(5);

  const u32 rt = xg * 8 + (slot >> 2), cj = slot & 3, dt3 = ((7 * cj) >> 1) + w;
  const u32 nd = 4 - (xg & 1), gi = (xg & 1) ? (slot * 43) >> 7 : slot >> 2, db = (xg & 1) * 4 + slot - gi * nd;
  const u32 fb = (xg >> 1) * 7 + (gi < 6 ? gi : 6), sb = (xg >> 1) * 8 + (gi < 7 ? gi : 7);
  const rsrc_t rw1 = mkrsrc(w1, SEQ * FF * 2), rw2 = mkrsrc(w2, FF * SEQ * 2), rwo = mkrsrc(wo, DM * DM * 2);
  const u32 wrow = lane >> 3, wpc = ((lane & 7) ^ ((wrow & 2) << 1)) << 4;
  const u32 vw1 = (w * 512 + wrow) * (FF * 2) + fb * 128 + wpc;
  const u32 vw2 = (w * 448 + wrow) * (SEQ * 2) + sb * 128 + wpc;
  const u32 ta0 = (4 * hh + tq) * 128 + (((w & 1) ^ (tq >> 1)) << 6) + tcol, ta1 = ta0 ^ 64;

  bf16x8 w1f[32][2];
  f32x16 ofin;
  {
    bf16x8 qf[4][2], kf[16][2], vf[16][2];
    qf[0][0] = asbf(qf0[0]); qf[0][1] = asbf(qf0[1]);
#pragma unroll
    for (int a = 1; a < 4; ++a) { qf[a][0] = lds_frag(L0 + L_Z + (w ^ a) * 2048 + lane * 16); qf[a][1] = lds_frag(L0 + L_Z + (w ^ a) * 2048 + 1024 + lane * 16); }
    kf[0][0] = asbf(kf0[0]); kf[0][1] = asbf(kf0[1]); vf[0][0] = asbf(vf0[0]); vf[0][1] = asbf(vf0[1]);
    BARRIER();
    const u32 kvb = WS_KV + ((h * 64 + w) << 12) + lane * 16;
    auto kvload = [&](int t) INL {
      const u32 so = ((rb + t) & 15) << 14;
      kf[t][0] = gld_sc1(rws, kvb, so); kf[t][1] = gld_sc1(rws, kvb + 1024, so); vf[t][0] = gld_sc1(rws, kvb + 2048, so); vf[t][1] = gld_sc1(rws, kvb + 3072, so);
    };
    const u32 mst = L0 + L_Z + w * 12288, mwo = L0 + L_R + w * L_SLOT;
    const u32 vwo = (lane >> 2) * 896 + dt3 * 64 + (lane & 3) * 16;
    auto w1dma = [&](int st) INL {
#pragma unroll
      for (int i = 0; i < 4; ++i) dma16(rw1, vw1, (st * 32 + i * 8) * (FF * 2), mst + (st % 3) * 4096 + i * 1024);
    };
    auto w1read = [&](int st) INL {
#pragma unroll
      for (int s = 0; s < 2; ++s) { const u32 o = mst + (st % 3) * 4096 + s * 2048;
        w1f[2 * st + s][0] = lds_tr2(ta0 + o, ta0 + o + 1024); w1f[2 * st + s][1] = lds_tr2(ta1 + o, ta1 + o + 1024); }
    };
    f32x16 o[4] = {}, sc[2];
    u32x4 plo, phi;
    const f32x16 zero = {};
    asm volatile("s_nop 1"); mfma_v0(sc[0], kf[0][0], qf[0][0]); mfma_v1(sc[0], kf[0][1], qf[0][1]); asm volatile("s_nop 15" : "+v"(sc[0]));
    wait_flags(rws, WS_F1 + (((h * 16 + (lane & 15)) * 4 + w) << 7), tag);
    TS(6);
#pragma unroll
    for (int t = 1; t <= KVD; ++t) kvload(t);
    SBAR();
    static_for<64>([&](auto U) INL {
      constexpr int u = U, t = u >> 2, a = u & 3, un = u + 1, tn = (un >> 2) & 15, an = un & 3, up = u > 0 ? u - 1 : 0, tp = up >> 2, ap = up & 3;
      f32x16& s = sc[u & 1]; f32x16& sn = sc[un & 1];
      if (a == 0 && t >= 1 && t + KVD < 16) kvload(t + KVD);
      if (a == 1) { asm volatile("s_waitcnt lgkmcnt(0)" ::: "memory"); w1dma(t);
        if (t >= 2) { dma16(rwo, vwo, (2 * t - 4) * 16 * 896, mwo + (2 * t - 4) * 1024); dma16(rwo, vwo, (2 * t - 3) * 16 * 896, mwo + (2 * t - 3) * 1024); } }
      if constexpr (a == 2 && t >= 2) {
        constexpr int younger = (t >= 4 ? 2 : 0) + ((t - 1 + KVD < 16) ? 4 : 0) + 4 + (t >= 3 ? 2 : 0) + ((t + KVD < 16) ? 4 : 0) + 6;
        WAIT_VM(younger);
        w1read(t - 2);
      }
      SBAR();
      if (u > 0) phi[3] = cvtpk(sn[14], sn[15]);
      SBAR();
      if (un < 64) mfma_v0(sn, kf[tn][0], qf[an][0]);
      SBAR();
      s[0] = __builtin_amdgcn_exp2f(s[0]); s[1] = __builtin_amdgcn_exp2f(s[1]); SBAR();
      s[2] = __builtin_amdgcn_exp2f(s[2]); s[3] = __builtin_amdgcn_exp2f(s[3]); plo[0] = cvtpk(s[0], s[1]); SBAR();
      if (u > 0) o[ap] = MFMA(vf[tp][1], asbf(phi), o[ap]);
      SBAR();
      s[4] = __builtin_amdgcn_exp2f(s[4]); s[5] = __builtin_amdgcn_exp2f(s[5]); plo[1] = cvtpk(s[2], s[3]); SBAR();
      s[6] = __builtin_amdgcn_exp2f(s[6]); s[7] = __builtin_amdgcn_exp2f(s[7]); plo[2] = cvtpk(s[4], s[5]); SBAR();
      if (un < 64) mfma_v1(sn, kf[tn][1], qf[an][1]);
      SBAR();
      s[8] = __builtin_amdgcn_exp2f(s[8]); s[9] = __builtin_amdgcn_exp2f(s[9]); plo[3] = cvtpk(s[6], s[7]); SBAR();
      s[10] = __builtin_amdgcn_exp2f(s[10]); s[11] = __builtin_amdgcn_exp2f(s[11]); phi[0] = cvtpk(s[8], s[9]); SBAR();
      o[a] = MFMA(vf[t][0], asbf(plo), o[a]);
      SBAR();
      s[12] = __builtin_amdgcn_exp2f(s[12]); s[13] = __builtin_amdgcn_exp2f(s[13]); phi[1] = cvtpk(s[10], s[11]); SBAR();
      s[14] = __builtin_amdgcn_exp2f(s[14]); s[15] = __builtin_amdgcn_exp2f(s[15]); phi[2] = cvtpk(s[12], s[13]); SBAR();
    });
    phi[3] = cvtpk(sc[1][14], sc[1][15]);
    o[3] = MFMA(vf[15][1], asbf(phi), o[3]);
    WAIT_VM(0);
    w1read(14); w1read(15);
    TS(7);
    BARRIER();
    ofin = reduce4(o, L0 + L_Z, w, lane);
    float l = halves_sum(hh ? ofin[12] : 0.f), mo = 0.f;
#pragma unroll
    for (int i = 0; i < 16; ++i) mo = fmaxf(mo, fabsf(ofin[i]));
    if (__builtin_expect(!__all(l > 0x1p-64f && l < 0x1p64f && mo < 0x1p127f), 0)) {
      const u32 kva = WS_KV + (h << 18) + lane * 16;
      float m = -3.0e38f;
#pragma unroll 1
      for (u32 t = 0; t < 64; ++t) {
        f32x16 s = MFMA(gld_sc1(rws, kva, t << 12), qf[0][0], zero); s = MFMA(gld_sc1(rws, kva + 1024, t << 12), qf[0][1], s);
#pragma unroll
        for (int i = 0; i < 16; ++i) m = fmaxf(m, s[i]);
      }
      m = -halves_max(m);
      const u32 m0 = cvtpk(m, 0.f) & 0xffffu; const float r0 = m - bflo(m0);
      const u32 m1 = cvtpk(r0, 0.f) & 0xffffu; const u32 m2 = cvtpk(r0 - bflo(m1), 0.f) & 0xffffu;
      u32x4 q1 = asu(qf[0][1]);
      if (hh) { q1[2] = m0 | (m1 << 16); q1[3] = m2; }
      ofin = zero;
#pragma unroll 1
      for (u32 t = 0; t < 64; ++t) {
        f32x16 s = MFMA(gld_sc1(rws, kva, t << 12), qf[0][0], zero); s = MFMA(gld_sc1(rws, kva + 1024, t << 12), asbf(q1), s);
#pragma unroll
        for (int i = 0; i < 16; ++i) s[i] = __builtin_amdgcn_exp2f(s[i]);
        ofin = MFMA(gld_sc1(rws, kva + 2048, t << 12), asbf(pack8(s, 0)), ofin); ofin = MFMA(gld_sc1(rws, kva + 3072, t << 12), asbf(pack8(s, 1)), ofin);
      }
      l = halves_sum(hh ? ofin[12] : 0.f);
    }
    const float rl = __builtin_amdgcn_rcpf(l);
#pragma unroll
    for (int i = 0; i < 16; ++i) ofin[i] *= rl;
    u32x4 o0 = pack8(ofin, 0), o1 = pack8(ofin, 1);
    if (hh) { o1[2] = 0u; o1[3] = 0u; }
    const u32 oo = WS_OF + (((rb * 4 + w) * 16 + h) << 11) + lane * 16;
    __builtin_amdgcn_raw_buffer_store_b128(o0, rws, oo, 0, SC1); __builtin_amdgcn_raw_buffer_store_b128(o1, rws, oo + 1024, 0, SC1);
  }
  TS(8);
  {
    bf16x8 wof[32];
    const u32 wob = L0 + L_R + w * L_SLOT + (4 * hh + tq) * 64 + tcol, wob2 = L0 + L_R + w * L_SLOT + tq * 64 + tcol;
#pragma unroll
    for (int k = 0; k < 32; ++k) { const u32 ro = ((k >> 1) * 28 + (k & 1) * 16) * 64; wof[k] = lds_tr2(wob + ro, ((k & 1) ? wob2 : wob) + ro + 512); }
    WAIT_VM(0);
    set_flag(rws, WS_F2 + (((rb * 4 + w) * 16 + h) << 7), tag, lane);
    float xr[16];
    const u32 vxr = (rt * 32 + 4 * hh) * 896 + (dt3 * 32 + r32) * 2;
#pragma unroll
    for (int i = 0; i < 16; ++i) xr[i] = bflo(__builtin_amdgcn_raw_buffer_load_b16(rx, vxr, ((i & 3) + 8 * (i >> 2)) * 896, 0));
    BARRIER();
    TS(9);
    wait_flags(rws, WS_F2 + ((rt * 16 + w * 4 + (lane & 3)) << 7), tag);
    TS(10);
    dma16x4_sc1(rws, lane * 16, WS_OF + (rt << 15) + (w << 13), L0 + L_Z + (w << 13));
    dma16x4_sc1(rws, lane * 16, WS_OF + (rt << 15) + (w << 13) + 4096, L0 + L_Z + (w << 13) + 4096);
#pragma unroll
    for (int i = 0; i < 28; ++i) dma16(rw2, vw2, i * 8 * (SEQ * 2), L0 + L_R + w * L_SLOT + i * 1024);
    WAIT_VM_BAR(28);
    TS(11);
    f32x16 hv = {};
#pragma unroll
    for (int k = 0; k < 32; ++k) hv = MFMA(lds_frag(L0 + L_Z + k * 1024 + lane * 16), wof[k], hv);
    const u32 col = dt3 * 32 + r32;
    const bool valid = col - cj * 112 < 112u;
    float a[32];
#pragma unroll
    for (int i = 0; i < 16; ++i) { hv[i] += xr[i]; const float z = valid ? hv[i] : 0.f; a[2 * i] = z; a[2 * i + 1] = z * z; }
#pragma unroll
    for (int i = 0; i < 16; ++i) { auto r = __builtin_amdgcn_permlane16_swap(__float_as_uint(a[i]), __float_as_uint(a[i + 16]), false, false); a[i] = __uint_as_float(r[0]) + __uint_as_float(r[1]); }
#define DPPADD(x, ctrl) ((x) + __uint_as_float(__builtin_amdgcn_update_dpp(0, __float_as_uint(x), ctrl, 0xf, 0xf, true)))
#pragma unroll
    for (int i = 0; i < 8; ++i) { const float p = DPPADD(a[i], 0x140), q = DPPADD(a[i + 8], 0x140); a[i] = (lane & 8) ? q : p; }
#pragma unroll
    for (int i = 0; i < 4; ++i) { const float p = DPPADD(a[i], 0x141), q = DPPADD(a[i + 4], 0x141); a[i] = (lane & 4) ? q : p; }
#pragma unroll
    for (int i = 0; i < 2; ++i) { const float p = DPPADD(a[i], 0x4e), q = DPPADD(a[i + 2], 0x4e); a[i] = (lane & 2) ? q : p; }
    { const float p = DPPADD(a[0], 0xb1), q = DPPADD(a[1], 0xb1); a[0] = (lane & 1) ? q : p; }
    const u32 lst = L0 + L_Z + 32768;
    *(LAS float*)(lst + lane * 16 + w * 4) = a[0];
    BARRIER();
    const f32x4 p4 = lds_f4(lst + lane * 16);
    const float part = (p4[0] + p4[1]) + (p4[2] + p4[3]);
    if (w == 0) __builtin_amdgcn_raw_buffer_store_b64(u32x2{__float_as_uint(part), tag}, rws, WS_ST + ((rt * 4 + cj) << 9) + lane * 8, 0, SC1);
    TS(12);
    float tot = 0.f;
    for (u32 spin = 0; spin < SPIN_CAP; ++spin) {
      u32x2 g[4];
#pragma unroll
      for (int j = 0; j < 4; ++j) g[j] = __builtin_amdgcn_raw_buffer_load_b64(rws, WS_ST + (rt << 11) + lane * 8, j * 512, SC1);
      tot = (__uint_as_float(g[0].x) + __uint_as_float(g[1].x)) + (__uint_as_float(g[2].x) + __uint_as_float(g[3].x));
      if (__all(g[0].y == tag && g[1].y == tag && g[2].y == tag && g[3].y == tag)) break;
      __builtin_amdgcn_s_sleep(1);
      asm volatile("" ::: "memory");
    }
    TS(13);
    const float oth = __uint_as_float(__builtin_amdgcn_update_dpp(0, __float_as_uint(tot), 0xb1, 0xf, 0xf, true));
    const float mean = ((lane & 1) ? oth : tot) * (1.f / DM), var = fmaxf(((lane & 1) ? tot : oth) * (1.f / DM) - mean * mean, 0.f);
    const float r1 = __builtin_amdgcn_rsqf(var + 1e-5f), rr = r1 * __builtin_amdgcn_rsqf(var * r1 * r1 + 1e-5f);
    const u32 lpv = lst + 1024 + w * 256 + hh * 128;
    *(LAS float*)(lpv + (lane & 1) * 64 + (r32 >> 1) * 4) = (lane & 1) ? mean * rr : rr;
    f32x16 hn;
#pragma unroll
    for (int g = 0; g < 4; ++g) { const f32x4 a4 = lds_f4(lpv + g * 16), b4 = lds_f4(lpv + 64 + g * 16);
#pragma unroll
      for (int e = 0; e < 4; ++e) hn[4 * g + e] = hv[4 * g + e] * a4[e] - b4[e]; }
    const u32 ho = valid ? ((dt3 * 128 + rt * 2) << 10) + lane * 16 : 0x80000000u;
    __builtin_amdgcn_raw_buffer_store_b128(pack8(hn, 0), rws, ho, WS_HN, SC1); __builtin_amdgcn_raw_buffer_store_b128(pack8(hn, 1), rws, ho, WS_HN + 1024, SC1);
    __builtin_amdgcn_raw_buffer_store_b128(pack8(hv, 0), rws, ho, WS_HR, SC1); __builtin_amdgcn_raw_buffer_store_b128(pack8(hv, 1), rws, ho, WS_HR + 1024, SC1);
    WAIT_VM(0);
    set_flag(rws, WS_F3 + ((rt * 16 + cj * 4 + w) << 7), tag, lane);
  }
  TS(14);
  if (gi < 8) {
    const u32 dtA = db * 2 + (w >> 1), dtB = db * 2 + 1 - (w >> 1);
    bf16x8 w2f[28][2];
    auto w2read = [&](int s) INL { const u32 o = L0 + L_R + w * L_SLOT + (s % 14) * 2048; w2f[s][0] = lds_tr2(ta0 + o, ta0 + o + 1024); w2f[s][1] = lds_tr2(ta1 + o, ta1 + o + 1024); };
    u32x2 bb[4];
    {
      const rsrc_t rb1 = mkrsrc(b1, FF * 2);
#pragma unroll
      for (int g = 0; g < 4; ++g) bb[g] = __builtin_amdgcn_raw_buffer_load_b64(rb1, (fb * 64 + (w & 1) * 32 + 4 * hh) * 2, g * 16, 0);
      const u32 g4 = lane >> 4, dtq = db * 2 + (g4 & 1);
      u32 pj = dtq + (dtq > 3) + (dtq > 10);
      if (g4 >= 2 && (dtq == 3 || dtq == 10)) pj += 1;
      wait_flags(rws, WS_F3 + (((w * 16 + (lane & 15)) * 16 + pj) << 7), tag);
    }
    TS(15);
    f32x16 acc[4] = {};
    {
      bf16x8 hf[32][2];
      const u32 vA = WS_HN + ((dtA * 128 + w * 32) << 10) + lane * 16, vB = WS_HN + ((dtB * 128 + w * 32) << 10) + lane * 16;
#pragma unroll
      for (int s = 0; s < HND; ++s) { hf[s][0] = gld_sc1(rws, vA, s * 1024); hf[s][1] = gld_sc1(rws, vB, s * 1024); }
      SBAR();
#pragma unroll
      for (int s = 0; s < 32; ++s) {
        if (s + HND < 32) { hf[s + HND][0] = gld_sc1(rws, vA, (s + HND) * 1024); hf[s + HND][1] = gld_sc1(rws, vB, (s + HND) * 1024); }
        if (s >= 2 && s < 16) w2read(s - 2);
        if (s >= 32 - HND && s < 32 - HND + 7) {
          if (s == 32 - HND) asm volatile("s_waitcnt lgkmcnt(0)" ::: "memory");
#pragma unroll
          for (int i = 0; i < 4; ++i) { const int p = (s - (32 - HND)) * 4 + i; dma16(rw2, vw2, (28 + p) * 8 * (SEQ * 2), L0 + L_R + w * L_SLOT + p * 1024); }
        }
        acc[0] = MFMA(w1f[s][0], hf[s][0], acc[0]); acc[1] = MFMA(w1f[s][1], hf[s][0], acc[1]);
        acc[2] = MFMA(w1f[s][0], hf[s][1], acc[2]); acc[3] = MFMA(w1f[s][1], hf[s][1], acc[3]);
        SBAR();
      }
    }
    TS(16);
    BARRIER();
    {
      f32x16 uv = reduce4(acc, L0 + L_Z, w, lane);
#pragma unroll
      for (int i = 0; i < 16; ++i) {
        const u32 bw = bb[i >> 2][(i >> 1) & 1];
        const float z = uv[i] + ((i & 1) ? bfhi(bw) : bflo(bw));
        const float e = __builtin_amdgcn_exp2f(z * (-2.30220819f - 0.10294324f * z * z));
        uv[i] = z * __builtin_amdgcn_rcpf(1.f + e);
      }
      if (gi < 7) {
        const u32 uo = WS_UF + ((dtA * 112 + fb * 4 + (w & 1) * 2) << 10) + lane * 16;
        __builtin_amdgcn_raw_buffer_store_b128(pack8(uv, 0), rws, uo, 0, SC1); __builtin_amdgcn_raw_buffer_store_b128(pack8(uv, 1), rws, uo + 1024, 0, SC1);
      }
      WAIT_VM(0);
      if (gi < 7) set_flag(rws, WS_F4 + (((db * 28 + fb) * 4 + w) << 7), tag, lane);
    }
    TS(17);
    u32x4 hr[2];
    {
      const rsrc_t rb2 = mkrsrc(b2, SEQ * 2);
#pragma unroll
      for (int g = 0; g < 4; ++g) bb[g] = __builtin_amdgcn_raw_buffer_load_b64(rb2, (sb * 64 + (w & 1) * 32 + 4 * hh) * 2, g * 16, 0);
#pragma unroll
      for (int s = 14; s < 28; ++s) w2read(s);
      BARRIER();
      const u32 ho = WS_HR + ((dtA * 128 + sb * 4 + (w & 1) * 2) << 10) + lane * 16;
      hr[0] = __builtin_amdgcn_raw_buffer_load_b128(rws, ho, 0, SC1); hr[1] = __builtin_amdgcn_raw_buffer_load_b128(rws, ho, 1024, SC1);
      const u32 l28 = lane < 28 ? lane : 0, ftg = w * 14 + (l28 >> 1);
      wait_flags(rws, WS_F4 + (((db * 28 + (ftg >> 1)) * 4 + (ftg & 1) + 2 * (l28 & 1)) << 7), tag);
    }
    TS(18);
    if (id == 0 && threadIdx.x == 0) __builtin_amdgcn_raw_buffer_store_b32(tag, rws, 0, 0, SC1);
#pragma unroll
    for (int a = 0; a < 4; ++a) acc[a] = f32x16{};
    {
      bf16x8 uf[28][2];
      const u32 vA = WS_UF + ((dtA * 112 + w * 28) << 10) + lane * 16, vB = WS_UF + ((dtB * 112 + w * 28) << 10) + lane * 16;
#pragma unroll
      for (int s = 0; s < HND; ++s) { uf[s][0] = gld_sc1(rws, vA, s * 1024); uf[s][1] = gld_sc1(rws, vB, s * 1024); }
      SBAR();
#pragma unroll
      for (int s = 0; s < 28; ++s) {
        if (s + HND < 28) { uf[s + HND][0] = gld_sc1(rws, vA, (s + HND) * 1024); uf[s + HND][1] = gld_sc1(rws, vB, (s + HND) * 1024); }
        acc[0] = MFMA(w2f[s][0], uf[s][0], acc[0]); acc[1] = MFMA(w2f[s][1], uf[s][0], acc[1]);
        acc[2] = MFMA(w2f[s][0], uf[s][1], acc[2]); acc[3] = MFMA(w2f[s][1], uf[s][1], acc[3]);
        SBAR();
      }
    }
    const f32x16 yv = reduce4(acc, L0 + L_Z, w, lane);
    const rsrc_t ro = mkrsrc(out, SEQ * DM * 2);
    const u32 vo = (sb * 64 + (w & 1) * 32 + 4 * hh) * 896 + (dtA * 32 + r32) * 2;
#pragma unroll
    for (int i = 0; i < 16; ++i) {
      const u32 bw = bb[i >> 2][(i >> 1) & 1], hw = hr[i >> 3][(i >> 1) & 3];
      const float z = yv[i] + ((i & 1) ? bfhi(bw) : bflo(bw)) + ((i & 1) ? bfhi(hw) : bflo(hw));
      __builtin_amdgcn_raw_buffer_store_b16((unsigned short)cvtpk(z, 0.f), ro, vo, ((i & 3) + 8 * (i >> 2)) * 896, 0);
    }
  }
}

extern "C" void kernel_launch(void* const* d_in, const int* in_sizes, int n_in, void* d_out, int out_size, void* d_ws, size_t ws_size, hipStream_t stream) {
  (void)hipFuncSetAttribute((const void*)fused_block, hipFuncAttributeMaxDynamicSharedMemorySize, LDS_BYTES);
  fused_block<<<256, 256, LDS_BYTES, stream>>>((const u32*)d_in[0], (const u32*)d_in[1], (const u32*)d_in[2], (const u32*)d_in[4], (const u32*)d_in[5],
                                               (const u32*)d_in[6], (const u32*)d_in[7], (unsigned short*)d_out, (char*)d_ws);
}
```

```cpp
#define ENC_I 0
#define ENC_J 0
#define ENC_CYC 0
#include <hip/hip_runtime.h>
#include <hip/hip_bf16.h>
#include <utility>

typedef unsigned u32;
typedef unsigned long long u64;
typedef float f32x16 __attribute__((ext_vector_type(16)));
typedef float f32x4 __attribute__((ext_vector_type(4)));
typedef float f32x2 __attribute__((ext_vector_type(2)));
typedef __bf16 bf16x8 __attribute__((ext_vector_type(8)));
typedef __bf16 bf16x2 __attribute__((ext_vector_type(2)));
typedef short s16x4 __attribute__((ext_vector_type(4)));
typedef u32 u32x4 __attribute__((ext_vector_type(4)));
typedef u32 u32x2 __attribute__((ext_vector_type(2)));
typedef __amdgpu_buffer_rsrc_t rsrc_t;
#define LAS __attribute__((address_space(3)))
#define DI __device__ __forceinline__
#define SBAR() __builtin_amdgcn_sched_barrier(0)
#define MFMA(a, b, c) __builtin_amdgcn_mfma_f32_32x32x16_bf16((a), (b), (c), 0, 0, 0)

constexpr int SEQ = 2048, DM = 448, FF = 1792;
constexpr u32 WS_F1 = 4096;
constexpr u32 WS_F2 = WS_F1 + 131072;
constexpr u32 WS_F3 = WS_F2 + 131072;
constexpr u32 WS_F4 = WS_F3 + 131072;
constexpr u32 WS_ST = WS_F4 + 131072;
constexpr u32 WS_KV = 1u << 20;
constexpr u32 WS_OF = 5u << 20;
constexpr u32 WS_HN = 7u << 20;
constexpr u32 WS_HR = 9u << 20;
constexpr u32 WS_UF = 11u << 20;
constexpr u32 WS_SIZE = 13u << 20;
constexpr u32 TAGX = 0x9E3779B9u;
constexpr int SC1 = 16;
constexpr u32 LDS_BYTES = 163840;
constexpr u32 L_SLOT = 28672;
constexpr u32 L_CS = 143360;
constexpr u32 L_RS = L_CS + 512;
constexpr u32 L_R = 0;
constexpr u32 L_Z = 114688;
constexpr u32 SPIN_CAP = 1u << 20;

#define TS(i) do { if ((i) == 0 || (i) == ENC_I || (i) == ENC_J) { SBAR(); u64 t_, c_; asm volatile("s_memrealtime %0\n\ts_memtime %1\n\ts_waitcnt lgkmcnt(0)" : "=s"(t_), "=s"(c_) :: "memory"); \
  if ((i) == 0) enc0 = t_; if ((i) == ENC_I) enci = ENC_CYC ? c_ : t_; if ((i) == ENC_J) encj = ENC_CYC ? c_ : t_; SBAR(); } } while (0)

template <int... I, class F> DI void static_for_impl(std::integer_sequence<int, I...>, F&& f) { (f(std::integral_constant<int, I>{}), ...); }
template <int N, class F> DI void static_for(F&& f) { static_for_impl(std::make_integer_sequence<int, N>{}, f); }
#define INL __attribute__((always_inline))
DI u32 rfl(u32 x) { return __builtin_amdgcn_readfirstlane(x); }
DI u32 cvtpk(float lo, float hi) { f32x2 v = {lo, hi}; return __builtin_bit_cast(u32, __builtin_convertvector(v, bf16x2)); }
DI float dot2(u32 a, u32 b, float c) { return __builtin_amdgcn_fdot2_f32_bf16(__builtin_bit_cast(bf16x2, a), __builtin_bit_cast(bf16x2, b), c, false); }
DI bf16x8 asbf(u32x4 v) { return __builtin_bit_cast(bf16x8, v); }
DI u32x4 asu(bf16x8 v) { return __builtin_bit_cast(u32x4, v); }
DI float halves_sum(float v) { auto r = __builtin_amdgcn_permlane32_swap(__float_as_uint(v), __float_as_uint(v), false, false); return __uint_as_float(r[0]) + __uint_as_float(r[1]); }
DI float halves_max(float v) { auto r = __builtin_amdgcn_permlane32_swap(__float_as_uint(v), __float_as_uint(v), false, false); return fmaxf(__uint_as_float(r[0]), __uint_as_float(r[1])); }
DI rsrc_t mkrsrc(const void* p, u32 bytes) { return __builtin_amdgcn_make_buffer_rsrc((void*)p, 0, bytes, 0x00020000); }
DI bf16x8 lds_frag(u32 a) { return *(const LAS bf16x8*)a; }
DI f32x4 lds_f4(u32 a) { return *(const LAS f32x4*)a; }
DI bf16x8 lds_tr2(u32 a0, u32 a1) {
  s16x4 lo = __builtin_amdgcn_ds_read_tr16_b64_v4i16((LAS s16x4*)a0), hi = __builtin_amdgcn_ds_read_tr16_b64_v4i16((LAS s16x4*)a1);
  return __builtin_bit_cast(bf16x8, __builtin_shufflevector(lo, hi, 0, 1, 2, 3, 4, 5, 6, 7));
}
DI void mfma_v0(f32x16& d, bf16x8 a, bf16x8 b) { asm volatile("v_mfma_f32_32x32x16_bf16 %0, %1, %2, 0" : "=&v"(d) : "v"(a), "v"(b)); }
DI void mfma_v1(f32x16& d, bf16x8 a, bf16x8 b) { asm volatile("v_mfma_f32_32x32x16_bf16 %0, %1, %2, %0" : "+v"(d) : "v"(a), "v"(b)); }
DI void dma16(rsrc_t r, u32 voff, u32 soff, u32 m0v) {
  asm volatile("s_mov_b32 m0, %3\n\ts_nop 0\n\tbuffer_load_dwordx4 %0, %1, %2 offen lds" :: "v"(voff), "s"(r), "s"(soff), "s"(m0v) : "memory");
}
DI void dma16x4_sc1(rsrc_t r, u32 voff, u32 soff, u32 m0v) {
  asm volatile("s_mov_b32 m0, %3\n\ts_nop 0\n\tbuffer_load_dwordx4 %0, %1, %2 offen sc1 lds\n\tbuffer_load_dwordx4 %0, %1, %2 offen offset:1024 sc1 lds\n\t"
               "buffer_load_dwordx4 %0, %1, %2 offen offset:2048 sc1 lds\n\tbuffer_load_dwordx4 %0, %1, %2 offen offset:3072 sc1 lds" :: "v"(voff), "s"(r), "s"(soff), "s"(m0v) : "memory");
}
#define WAIT_VM(n) asm volatile("s_waitcnt vmcnt(%0)" :: "i"(n) : "memory")
#define BARRIER() asm volatile("s_waitcnt lgkmcnt(0)\n\ts_barrier" ::: "memory")
#define WAIT_VM_BAR(n) asm volatile("s_waitcnt vmcnt(%0) lgkmcnt(0)\n\ts_barrier" :: "i"(n) : "memory")
DI void set_flag(rsrc_t ws, u32 off, u32 tag, u32 lane) {
  u32x2 f = {tag, tag ^ TAGX};
  if (lane == 0) __builtin_amdgcn_raw_buffer_store_b64(f, ws, off, 0, SC1);
}
DI void wait_flags(rsrc_t ws, u32 voff, u32 tag) {
  for (u32 spin = 0; spin < SPIN_CAP; ++spin) {
    u32x2 f = __builtin_amdgcn_raw_buffer_load_b64(ws, voff, 0, SC1);
    if (__all(f.x == tag && f.y == (tag ^ TAGX))) break;
    __builtin_amdgcn_s_sleep(1);
    asm volatile("" ::: "memory");
  }
  asm volatile("" ::: "memory");
}
DI bf16x8 gld_sc1(rsrc_t ws, u32 voff, u32 soff) { return asbf(__builtin_amdgcn_raw_buffer_load_b128(ws, voff, soff, SC1)); }
DI u32x4 pack8(const f32x16& v, int s) { return u32x4{cvtpk(v[8 * s], v[8 * s + 1]), cvtpk(v[8 * s + 2], v[8 * s + 3]), cvtpk(v[8 * s + 4], v[8 * s + 5]), cvtpk(v[8 * s + 6], v[8 * s + 7])}; }
DI float bflo(u32 v) { return __uint_as_float(v << 16); }
DI float bfhi(u32 v) { return __uint_as_float(v & 0xffff0000u); }
DI f32x16 reduce4(const f32x16 (&acc)[4], u32 lz, u32 w, u32 lane) {
#pragma unroll
  for (int a = 1; a < 4; ++a) {
    const u32 base = lz + (((w ^ a) * 3 + (a - 1)) << 12) + lane * 16;
#pragma unroll
    for (int q = 0; q < 4; ++q) *(LAS f32x4*)(base + q * 1024) = f32x4{acc[a][4 * q], acc[a][4 * q + 1], acc[a][4 * q + 2], acc[a][4 * q + 3]};
  }
  BARRIER();
  f32x16 r = acc[0];
  const u32 rb = lz + w * 12288 + lane * 16;
#pragma unroll
  for (int a = 0; a < 3; ++a)
#pragma unroll
    for (int q = 0; q < 4; ++q) { const f32x4 p = lds_f4(rb + a * 4096 + q * 1024); r[4 * q] += p[0]; r[4 * q + 1] += p[1]; r[4 * q + 2] += p[2]; r[4 * q + 3] += p[3]; }
  return r;
}

#define KVD 3
#define HND 10

__global__ void __launch_bounds__(256, 1)
fused_block(const u32* __restrict__ x, const u32* __restrict__ wqkv, const u32* __restrict__ wo, const u32* __restrict__ w1, const u32* __restrict__ b1,
            const u32* __restrict__ w2, const u32* __restrict__ b2, unsigned short* __restrict__ out, char* __restrict__ wsp) {
  extern __shared__ __attribute__((aligned(16))) char smem[];
  const u32 L0 = (u32)(size_t)(LAS char*)smem;
  const u32 lane = threadIdx.x & 63, w = rfl(threadIdx.x >> 6);
  const u32 r32 = lane & 31, hh = lane >> 5;
  const u32 id = blockIdx.x, xg = id & 7, slot = id >> 3;
  u64 enc0 = 0, enci = 0, encj = 0;
  TS(0);
  const rsrc_t rws = mkrsrc(wsp, WS_SIZE), rx = mkrsrc(x, SEQ * DM * 2), rwq = mkrsrc(wqkv, DM * 3 * DM * 2);
  const u32 epoch = __builtin_amdgcn_raw_buffer_load_b32(rws, 0, 0, SC1);
  const u32 tq = (lane & 15) >> 2, tcol = ((lane >> 4) & 1) * 32 + (lane & 3) * 8;

  const u32 h = 4 * (xg >> 1) + (slot & 3), rb = (xg & 1) * 8 + (slot >> 2);
  u32x4 kf0[2], vf0[2], qf0[2];
  {
    const u32 xrow = lane >> 3;
    const u32 vx0 = (rb * 128 + w * 32 + xrow) * 896 + (((lane & 7) ^ (xrow >> 1)) << 4);
    const u32 vx1 = (rb * 128 + w * 32 + 8 + xrow) * 896 + (((lane & 7) ^ (4 + (xrow >> 1))) << 4);
    const u32 vw = (lane >> 2) * 2688 + (lane & 3) * 16 + h * 56;
    u32 sw[3], mw[3];
#pragma unroll
    for (int i = 0; i < 3; ++i) { const u32 pc = 3 * w + i; sw[i] = (pc & 3) * 16 * 2688 + (pc >> 2) * 896; mw[i] = L0 + 16384 + pc * 1024; }
    const u32 mx = L0 + w * 4096;
    auto issue = [&](int c) INL {
      const u32 so = (c % 5) * L_SLOT;
      dma16(rx, vx0, c * 128, mx + so); dma16(rx, vx1, c * 128, mx + so + 1024);
      dma16(rx, vx0, c * 128 + 16 * 896, mx + so + 2048); dma16(rx, vx1, c * 128 + 16 * 896, mx + so + 3072);
#pragma unroll
      for (int i = 0; i < 3; ++i) dma16(rwq, vw, sw[i] + c * 64 * 2688, mw[i] + so);
    };
#pragma unroll
    for (int c = 0; c < 5; ++c) issue(c);
    TS(1);
    u32 xa[4];
#pragma unroll
    for (int i = 0; i < 4; ++i) xa[i] = L0 + w * 4096 + r32 * 128 + (((2 * i + hh) ^ ((r32 >> 1) & 7)) << 4);
    const u32 wa = L0 + 16384 + (8 * hh + tq) * 64 + tcol, wc = wa + (w == 3 ? 0 : w) * 4096;
    f32x16 aq = {}, ak = {}, av = {};
    float sum = 0.f, sq = 0.f, cs = 0.f;
#pragma unroll
    for (int c = 0; c < 7; ++c) {
      if (c == 0) WAIT_VM_BAR(28); else if (c <= 3) WAIT_VM_BAR(21); else if (c == 4) WAIT_VM_BAR(14); else if (c == 5) WAIT_VM_BAR(7); else WAIT_VM_BAR(0);
      if (c == 0) TS(2);
      if (c == 1 || c == 2) issue(c + 4);
      const u32 so = (c % 5) * L_SLOT;
#pragma unroll
      for (int i = 0; i < 4; ++i) {
        const bf16x8 xf = lds_frag(xa[i] + so);
        const u32 wo_ = so + i * 1024;
        const bf16x8 fk = lds_tr2(wa + wo_ + 4096, wa + wo_ + 4096 + 256), fv = lds_tr2(wa + wo_ + 8192, wa + wo_ + 8192 + 256), fq = lds_tr2(wa + wo_, wa + wo_ + 256);
        const bf16x8 fc = lds_tr2(wc + wo_, wc + wo_ + 256);
        ak = MFMA(fk, xf, ak); av = MFMA(xf, fv, av); aq = MFMA(fq, xf, aq);
        const u32x4 xu = asu(xf), cu = asu(fc);
#pragma unroll
        for (int e = 0; e < 4; ++e) { sum = dot2(xu[e], 0x3F803F80u, sum); sq = dot2(xu[e], xu[e], sq); cs = dot2(cu[e], 0x3F803F80u, cs); }
      }
    }
    TS(3);
    sum = halves_sum(sum); sq = halves_sum(sq); cs = halves_sum(cs);
    const float mean = sum * (1.f / DM), var = fmaxf(sq * (1.f / DM) - mean * mean, 0.f), ra = __builtin_amdgcn_rsqf(var + 1e-5f), rbm = ra * mean;
    if (w < 3 && hh == 0) *(LAS float*)(L0 + L_CS + w * 128 + r32 * 4) = cs;
    *(LAS float*)(L0 + L_RS + w * 256 + hh * 128 + r32 * 4) = hh ? rbm : ra;
    BARRIER();
    f32x16 t;
#pragma unroll
    for (int g = 0; g < 4; ++g) { const f32x4 c4 = lds_f4(L0 + L_CS + 128 + g * 32 + hh * 16);
#pragma unroll
      for (int e = 0; e < 4; ++e) t[4 * g + e] = ra * ak[4 * g + e] - rbm * c4[e]; }
    kf0[0] = pack8(t, 0); kf0[1] = pack8(t, 1);
    if (hh) { kf0[1][2] = 0x3F803F80u; kf0[1][3] = 0x00003F80u; }
    const u32 kvo = WS_KV + ((h * 64 + rb * 4 + w) << 12) + lane * 16;
    __builtin_amdgcn_raw_buffer_store_b128(kf0[0], rws, kvo, 0, SC1); __builtin_amdgcn_raw_buffer_store_b128(kf0[1], rws, kvo + 1024, 0, SC1);
    const float cv = *(const LAS float*)(L0 + L_CS + 256 + r32 * 4);
#pragma unroll
    for (int g = 0; g < 4; ++g) { const f32x4 a4 = lds_f4(L0 + L_RS + w * 256 + g * 32 + hh * 16), b4 = lds_f4(L0 + L_RS + w * 256 + 128 + g * 32 + hh * 16);
#pragma unroll
      for (int e = 0; e < 4; ++e) t[4 * g + e] = a4[e] * av[4 * g + e] - b4[e] * cv; }
    vf0[0] = pack8(t, 0); vf0[1] = pack8(t, 1);
    if (r32 >= 28) { const u32 pv = r32 == 28 ? 0x3F803F80u : 0u; vf0[0] = u32x4{pv, pv, pv, pv}; vf0[1] = vf0[0]; }
    __builtin_amdgcn_raw_buffer_store_b128(vf0[0], rws, kvo + 2048, 0, SC1); __builtin_amdgcn_raw_buffer_store_b128(vf0[1], rws, kvo + 3072, 0, SC1);
    const float qs = 1.4426950408889634f / 5.291502622129181f, qa = ra * qs, qb = rbm * qs;
#pragma unroll
    for (int g = 0; g < 4; ++g) { const f32x4 c4 = lds_f4(L0 + L_CS + g * 32 + hh * 16);
#pragma unroll
      for (int e = 0; e < 4; ++e) t[4 * g + e] = qa * aq[4 * g + e] - qb * c4[e]; }
    qf0[0] = pack8(t, 0); qf0[1] = pack8(t, 1);
    if (hh) { qf0[1][2] = 0u; qf0[1][3] = 0u; }
    *(LAS u32x4*)(L0 + L_Z + w * 2048 + lane * 16) = qf0[0]; *(LAS u32x4*)(L0 + L_Z + w * 2048 + 1024 + lane * 16) = qf0[1];
  }
  const u32 tag = rfl(epoch) + 1;
  TS(4);
  WAIT_VM(0);
  set_flag(rws, WS_F1 + (((h * 16 + rb) * 4 + w) << 7), tag, lane);
  BARRIER();
  TS(5);

  const u32 rt = xg * 8 + (slot >> 2), cj = slot & 3, dt3 = ((7 * cj) >> 1) + w;
  const u32 nd = 4 - (xg & 1), gi = (xg & 1) ? (slot * 43) >> 7 : slot >> 2, db = (xg & 1) * 4 + slot - gi * nd;
  const u32 fb = (xg >> 1) * 7 + (gi < 6 ? gi : 6), sb = (xg >> 1) * 8 + (gi < 7 ? gi : 7);
  const rsrc_t rw1 = mkrsrc(w1, SEQ * FF * 2), rw2 = mkrsrc(w2, FF * SEQ * 2), rwo = mkrsrc(wo, DM * DM * 2);
  const u32 wrow = lane >> 3, wpc = ((lane & 7) ^ ((wrow & 2) << 1)) << 4;
  const u32 vw1 = (w * 512 + wrow) * (FF * 2) + fb * 128 + wpc;
  const u32 vw2 = (w * 448 + wrow) * (SEQ * 2) + sb * 128 + wpc;
  const u32 ta0 = (4 * hh + tq) * 128 + (((w & 1) ^ (tq >> 1)) << 6) + tcol, ta1 = ta0 ^ 64;

  bf16x8 w1f[32][2];
  f32x16 ofin;
  {
    bf16x8 qf[4][2], kf[16][2], vf[16][2];
    qf[0][0] = asbf(qf0[0]); qf[0][1] = asbf(qf0[1]);
#pragma unroll
    for (int a = 1; a < 4; ++a) { qf[a][0] = lds_frag(L0 + L_Z + (w ^ a) * 2048 + lane * 16); qf[a][1] = lds_frag(L0 + L_Z + (w ^ a) * 2048 + 1024 + lane * 16); }
    kf[0][0] = asbf(kf0[0]); kf[0][1] = asbf(kf0[1]); vf[0][0] = asbf(vf0[0]); vf[0][1] = asbf(vf0[1]);
    BARRIER();
    const u32 kvb = WS_KV + ((h * 64 + w) << 12) + lane * 16;
    auto kvload = [&](int t) INL {
      const u32 so = ((rb + t) & 15) << 14;
      kf[t][0] = gld_sc1(rws, kvb, so); kf[t][1] = gld_sc1(rws, kvb + 1024, so); vf[t][0] = gld_sc1(rws, kvb + 2048, so); vf[t][1] = gld_sc1(rws, kvb + 3072, so);
    };
    const u32 mst = L0 + L_Z + w * 12288, mwo = L0 + L_R + w * L_SLOT;
    const u32 vwo = (lane >> 2) * 896 + dt3 * 64 + (lane & 3) * 16;
    auto w1dma = [&](int st) INL {
#pragma unroll
      for (int i = 0; i < 4; ++i) dma16(rw1, vw1, (st * 32 + i * 8) * (FF * 2), mst + (st % 3) * 4096 + i * 1024);
    };
    auto w1read = [&](int st) INL {
#pragma unroll
      for (int s = 0; s < 2; ++s) { const u32 o = mst + (st % 3) * 4096 + s * 2048;
        w1f[2 * st + s][0] = lds_tr2(ta0 + o, ta0 + o + 1024); w1f[2 * st + s][1] = lds_tr2(ta1 + o, ta1 + o + 1024); }
    };
    f32x16 o[4] = {}, sc[2];
    u32x4 plo, phi;
    const f32x16 zero = {};
    asm volatile("s_nop 1"); mfma_v0(sc[0], kf[0][0], qf[0][0]); mfma_v1(sc[0], kf[0][1], qf[0][1]); asm volatile("s_nop 15" : "+v"(sc[0]));
    wait_flags(rws, WS_F1 + (((h * 16 + (lane & 15)) * 4 + w) << 7), tag);
    TS(6);
#pragma unroll
    for (int t = 1; t <= KVD; ++t) kvload(t);
    SBAR();
    static_for<64>([&](auto U) INL {
      constexpr int u = U, t = u >> 2, a = u & 3, un = u + 1, tn = (un >> 2) & 15, an = un & 3, up = u > 0 ? u - 1 : 0, tp = up >> 2, ap = up & 3;
      f32x16& s = sc[u & 1]; f32x16& sn = sc[un & 1];
      if (a == 0 && t >= 1 && t + KVD < 16) kvload(t + KVD);
      if (a == 1) { asm volatile("s_waitcnt lgkmcnt(0)" ::: "memory"); w1dma(t);
        if (t >= 2) { dma16(rwo, vwo, (2 * t - 4) * 16 * 896, mwo + (2 * t - 4) * 1024); dma16(rwo, vwo, (2 * t - 3) * 16 * 896, mwo + (2 * t - 3) * 1024); } }
      if constexpr (a == 2 && t >= 2) {
        constexpr int younger = (t >= 4 ? 2 : 0) + ((t - 1 + KVD < 16) ? 4 : 0) + 4 + (t >= 3 ? 2 : 0) + ((t + KVD < 16) ? 4 : 0) + 6;
        WAIT_VM(younger);
        w1read(t - 2);
      }
      SBAR();
      if (u > 0) phi[3] = cvtpk(sn[14], sn[15]);
      SBAR();
      if (un < 64) mfma_v0(sn, kf[tn][0], qf[an][0]);
      SBAR();
      s[0] = __builtin_amdgcn_exp2f(s[0]); s[1] = __builtin_amdgcn_exp2f(s[1]); SBAR();
      s[2] = __builtin_amdgcn_exp2f(s[2]); s[3] = __builtin_amdgcn_exp2f(s[3]); plo[0] = cvtpk(s[0], s[1]); SBAR();
      if (u > 0) o[ap] = MFMA(vf[tp][1], asbf(phi), o[ap]);
      SBAR();
      s[4] = __builtin_amdgcn_exp2f(s[4]); s[5] = __builtin_amdgcn_exp2f(s[5]); plo[1] = cvtpk(s[2], s[3]); SBAR();
      s[6] = __builtin_amdgcn_exp2f(s[6]); s[7] = __builtin_amdgcn_exp2f(s[7]); plo[2] = cvtpk(s[4], s[5]); SBAR();
      if (un < 64) mfma_v1(sn, kf[tn][1], qf[an][1]);
      SBAR();
      s[8] = __builtin_amdgcn_exp2f(s[8]); s[9] = __builtin_amdgcn_exp2f(s[9]); plo[3] = cvtpk(s[6], s[7]); SBAR();
      s[10] = __builtin_amdgcn_exp2f(s[10]); s[11] = __builtin_amdgcn_exp2f(s[11]); phi[0] = cvtpk(s[8], s[9]); SBAR();
      o[a] = MFMA(vf[t][0], asbf(plo), o[a]);
      SBAR();
      s[12] = __builtin_amdgcn_exp2f(s[12]); s[13] = __builtin_amdgcn_exp2f(s[13]); phi[1] = cvtpk(s[10], s[11]); SBAR();
      s[14] = __builtin_amdgcn_exp2f(s[14]); s[15] = __builtin_amdgcn_exp2f(s[15]); phi[2] = cvtpk(s[12], s[13]); SBAR();
    });
    phi[3] = cvtpk(sc[1][14], sc[1][15]);
    o[3] = MFMA(vf[15][1], asbf(phi), o[3]);
    WAIT_VM(0);
    w1read(14); w1read(15);
    TS(7);
    BARRIER();
    ofin = reduce4(o, L0 + L_Z, w, lane);
    float l = halves_sum(hh ? ofin[12] : 0.f), mo = 0.f;
#pragma unroll
    for (int i = 0; i < 16; ++i) mo = fmaxf(mo, fabsf(ofin[i]));
    if (__builtin_expect(!__all(l > 0x1p-64f && l < 0x1p64f && mo < 0x1p127f), 0)) {
      const u32 kva = WS_KV + (h << 18) + lane * 16;
      float m = -3.0e38f;
#pragma unroll 1
      for (u32 t = 0; t < 64; ++t) {
        f32x16 s = MFMA(gld_sc1(rws, kva, t << 12), qf[0][0], zero); s = MFMA(gld_sc1(rws, kva + 1024, t << 12), qf[0][1], s);
#pragma unroll
        for (int i = 0; i < 16; ++i) m = fmaxf(m, s[i]);
      }
      m = -halves_max(m);
      const u32 m0 = cvtpk(m, 0.f) & 0xffffu; const float r0 = m - bflo(m0);
      const u32 m1 = cvtpk(r0, 0.f) & 0xffffu; const u32 m2 = cvtpk(r0 - bflo(m1), 0.f) & 0xffffu;
      u32x4 q1 = asu(qf[0][1]);
      if (hh) { q1[2] = m0 | (m1 << 16); q1[3] = m2; }
      ofin = zero;
#pragma unroll 1
      for (u32 t = 0; t < 64; ++t) {
        f32x16 s = MFMA(gld_sc1(rws, kva, t << 12), qf[0][0], zero); s = MFMA(gld_sc1(rws, kva + 1024, t << 12), asbf(q1), s);
#pragma unroll
        for (int i = 0; i < 16; ++i) s[i] = __builtin_amdgcn_exp2f(s[i]);
        ofin = MFMA(gld_sc1(rws, kva + 2048, t << 12), asbf(pack8(s, 0)), ofin); ofin = MFMA(gld_sc1(rws, kva + 3072, t << 12), asbf(pack8(s, 1)), ofin);
      }
      l = halves_sum(hh ? ofin[12] : 0.f);
    }
    const float rl = __builtin_amdgcn_rcpf(l);
#pragma unroll
    for (int i = 0; i < 16; ++i) ofin[i] *= rl;
    u32x4 o0 = pack8(ofin, 0), o1 = pack8(ofin, 1);
    if (hh) { o1[2] = 0u; o1[3] = 0u; }
    const u32 oo = WS_OF + (((rb * 4 + w) * 16 + h) << 11) + lane * 16;
    __builtin_amdgcn_raw_buffer_store_b128(o0, rws, oo, 0, SC1); __builtin_amdgcn_raw_buffer_store_b128(o1, rws, oo + 1024, 0, SC1);
  }
  TS(8);
  {
    bf16x8 wof[32];
    const u32 wob = L0 + L_R + w * L_SLOT + (4 * hh + tq) * 64 + tcol, wob2 = L0 + L_R + w * L_SLOT + tq * 64 + tcol;
#pragma unroll
    for (int k = 0; k < 32; ++k) { const u32 ro = ((k >> 1) * 28 + (k & 1) * 16) * 64; wof[k] = lds_tr2(wob + ro, ((k & 1) ? wob2 : wob) + ro + 512); }
    WAIT_VM(0);
    set_flag(rws, WS_F2 + (((rb * 4 + w) * 16 + h) << 7), tag, lane);
    float xr[16];
    const u32 vxr = (rt * 32 + 4 * hh) * 896 + (dt3 * 32 + r32) * 2;
#pragma unroll
    for (int i = 0; i < 16; ++i) xr[i] = bflo(__builtin_amdgcn_raw_buffer_load_b16(rx, vxr, ((i & 3) + 8 * (i >> 2)) * 896, 0));
    BARRIER();
    TS(9);
    wait_flags(rws, WS_F2 + ((rt * 16 + w * 4 + (lane & 3)) << 7), tag);
    TS(10);
    dma16x4_sc1(rws, lane * 16, WS_OF + (rt << 15) + (w << 13), L0 + L_Z + (w << 13));
    dma16x4_sc1(rws, lane * 16, WS_OF + (rt << 15) + (w << 13) + 4096, L0 + L_Z + (w << 13) + 4096);
#pragma unroll
    for (int i = 0; i < 28; ++i) dma16(rw2, vw2, i * 8 * (SEQ * 2), L0 + L_R + w * L_SLOT + i * 1024);
    WAIT_VM_BAR(28);
    TS(11);
    f32x16 hv = {};
#pragma unroll
    for (int k = 0; k < 32; ++k) hv = MFMA(lds_frag(L0 + L_Z + k * 1024 + lane * 16), wof[k], hv);
    const u32 col = dt3 * 32 + r32;
    const bool valid = col - cj * 112 < 112u;
    float a[32];
#pragma unroll
    for (int i = 0; i < 16; ++i) { hv[i] += xr[i]; const float z = valid ? hv[i] : 0.f; a[2 * i] = z; a[2 * i + 1] = z * z; }
#pragma unroll
    for (int i = 0; i < 16; ++i) { auto r = __builtin_amdgcn_permlane16_swap(__float_as_uint(a[i]), __float_as_uint(a[i + 16]), false, false); a[i] = __uint_as_float(r[0]) + __uint_as_float(r[1]); }
#define DPPADD(x, ctrl) ((x) + __uint_as_float(__builtin_amdgcn_update_dpp(0, __float_as_uint(x), ctrl, 0xf, 0xf, true)))
#pragma unroll
    for (int i = 0; i < 8; ++i) { const float p = DPPADD(a[i], 0x140), q = DPPADD(a[i + 8], 0x140); a[i] = (lane & 8) ? q : p; }
#pragma unroll
    for (int i = 0; i < 4; ++i) { const float p = DPPADD(a[i], 0x141), q = DPPADD(a[i + 4], 0x141); a[i] = (lane & 4) ? q : p; }
#pragma unroll
    for (int i = 0; i < 2; ++i) { const float p = DPPADD(a[i], 0x4e), q = DPPADD(a[i + 2], 0x4e); a[i] = (lane & 2) ? q : p; }
    { const float p = DPPADD(a[0], 0xb1), q = DPPADD(a[1], 0xb1); a[0] = (lane & 1) ? q : p; }
    const u32 lst = L0 + L_Z + 32768;
    *(LAS float*)(lst + lane * 16 + w * 4) = a[0];
    BARRIER();
    const f32x4 p4 = lds_f4(lst + lane * 16);
    const float part = (p4[0] + p4[1]) + (p4[2] + p4[3]);
    if (w == 0) __builtin_amdgcn_raw_buffer_store_b64(u32x2{__float_as_uint(part), tag}, rws, WS_ST + ((rt * 4 + cj) << 9) + lane * 8, 0, SC1);
    TS(12);
    float tot = 0.f;
    for (u32 spin = 0; spin < SPIN_CAP; ++spin) {
      u32x2 g[4];
#pragma unroll
      for (int j = 0; j < 4; ++j) g[j] = __builtin_amdgcn_raw_buffer_load_b64(rws, WS_ST + (rt << 11) + lane * 8, j * 512, SC1);
      tot = (__uint_as_float(g[0].x) + __uint_as_float(g[1].x)) + (__uint_as_float(g[2].x) + __uint_as_float(g[3].x));
      if (__all(g[0].y == tag && g[1].y == tag && g[2].y == tag && g[3].y == tag)) break;
      __builtin_amdgcn_s_sleep(1);
      asm volatile("" ::: "memory");
    }
    TS(13);
    const float oth = __uint_as_float(__builtin_amdgcn_update_dpp(0, __float_as_uint(tot), 0xb1, 0xf, 0xf, true));
    const float mean = ((lane & 1) ? oth : tot) * (1.f / DM), var = fmaxf(((lane & 1) ? tot : oth) * (1.f / DM) - mean * mean, 0.f);
    const float r1 = __builtin_amdgcn_rsqf(var + 1e-5f), rr = r1 * __builtin_amdgcn_rsqf(var * r1 * r1 + 1e-5f);
    const u32 lpv = lst + 1024 + w * 256 + hh * 128;
    *(LAS float*)(lpv + (lane & 1) * 64 + (r32 >> 1) * 4) = (lane & 1) ? mean * rr : rr;
    f32x16 hn;
#pragma unroll
    for (int g = 0; g < 4; ++g) { const f32x4 a4 = lds_f4(lpv + g * 16), b4 = lds_f4(lpv + 64 + g * 16);
#pragma unroll
      for (int e = 0; e < 4; ++e) hn[4 * g + e] = hv[4 * g + e] * a4[e] - b4[e]; }
    const u32 ho = valid ? ((dt3 * 128 + rt * 2) << 10) + lane * 16 : 0x80000000u;
    __builtin_amdgcn_raw_buffer_store_b128(pack8(hn, 0), rws, ho, WS_HN, SC1); __builtin_amdgcn_raw_buffer_store_b128(pack8(hn, 1), rws, ho, WS_HN + 1024, SC1);
    __builtin_amdgcn_raw_buffer_store_b128(pack8(hv, 0), rws, ho, WS_HR, SC1); __builtin_amdgcn_raw_buffer_store_b128(pack8(hv, 1), rws, ho, WS_HR + 1024, SC1);
    WAIT_VM(0);
    set_flag(rws, WS_F3 + ((rt * 16 + cj * 4 + w) << 7), tag, lane);
  }
  TS(14);
  if (gi < 8) {
    const u32 dtA = db * 2 + (w >> 1), dtB = db * 2 + 1 - (w >> 1);
    bf16x8 w2f[28][2];
    auto w2read = [&](int s) INL { const u32 o = L0 + L_R + w * L_SLOT + (s % 14) * 2048; w2f[s][0] = lds_tr2(ta0 + o, ta0 + o + 1024); w2f[s][1] = lds_tr2(ta1 + o, ta1 + o + 1024); };
    u32x2 bb[4];
    {
      const rsrc_t rb1 = mkrsrc(b1, FF * 2);
#pragma unroll
      for (int g = 0; g < 4; ++g) bb[g] = __builtin_amdgcn_raw_buffer_load_b64(rb1, (fb * 64 + (w & 1) * 32 + 4 * hh) * 2, g * 16, 0);
      const u32 g4 = lane >> 4, dtq = db * 2 + (g4 & 1);
      u32 pj = dtq + (dtq > 3) + (dtq > 10);
      if (g4 >= 2 && (dtq == 3 || dtq == 10)) pj += 1;
      wait_flags(rws, WS_F3 + (((w * 16 + (lane & 15)) * 16 + pj) << 7), tag);
    }
    TS(15);
    f32x16 acc[4] = {};
    {
      bf16x8 hf[32][2];
      const u32 vA = WS_HN + ((dtA * 128 + w * 32) << 10) + lane * 16, vB = WS_HN + ((dtB * 128 + w * 32) << 10) + lane * 16;
#pragma unroll
      for (int s = 0; s < HND; ++s) { hf[s][0] = gld_sc1(rws, vA, s * 1024); hf[s][1] = gld_sc1(rws, vB, s * 1024); }
      SBAR();
#pragma unroll
      for (int s = 0; s < 32; ++s) {
        if (s + HND < 32) { hf[s + HND][0] = gld_sc1(rws, vA, (s + HND) * 1024); hf[s + HND][1] = gld_sc1(rws, vB, (s + HND) * 1024); }
        if (s >= 2 && s < 16) w2read(s - 2);
        if (s >= 32 - HND && s < 32 - HND + 7) {
          if (s == 32 - HND) asm volatile("s_waitcnt lgkmcnt(0)" ::: "memory");
#pragma unroll
          for (int i = 0; i < 4; ++i) { const int p = (s - (32 - HND)) * 4 + i; dma16(rw2, vw2, (28 + p) * 8 * (SEQ * 2), L0 + L_R + w * L_SLOT + p * 1024); }
        }
        acc[0] = MFMA(w1f[s][0], hf[s][0], acc[0]); acc[1] = MFMA(w1f[s][1], hf[s][0], acc[1]);
        acc[2] = MFMA(w1f[s][0], hf[s][1], acc[2]); acc[3] = MFMA(w1f[s][1], hf[s][1], acc[3]);
        SBAR();
      }
    }
    TS(16);
    BARRIER();
    {
      f32x16 uv = reduce4(acc, L0 + L_Z, w, lane);
#pragma unroll
      for (int i = 0; i < 16; ++i) {
        const u32 bw = bb[i >> 2][(i >> 1) & 1];
        const float z = uv[i] + ((i & 1) ? bfhi(bw) : bflo(bw));
        const float e = __builtin_amdgcn_exp2f(z * (-2.30220819f - 0.10294324f * z * z));
        uv[i] = z * __builtin_amdgcn_rcpf(1.f + e);
      }
      if (gi < 7) {
        const u32 uo = WS_UF + ((dtA * 112 + fb * 4 + (w & 1) * 2) << 10) + lane * 16;
        __builtin_amdgcn_raw_buffer_store_b128(pack8(uv, 0), rws, uo, 0, SC1); __builtin_amdgcn_raw_buffer_store_b128(pack8(uv, 1), rws, uo + 1024, 0, SC1);
      }
      WAIT_VM(0);
      if (gi < 7) set_flag(rws, WS_F4 + (((db * 28 + fb) * 4 + w) << 7), tag, lane);
    }
    TS(17);
    u32x4 hr[2];
    {
      const rsrc_t rb2 = mkrsrc(b2, SEQ * 2);
#pragma unroll
      for (int g = 0; g < 4; ++g) bb[g] = __builtin_amdgcn_raw_buffer_load_b64(rb2, (sb * 64 + (w & 1) * 32 + 4 * hh) * 2, g * 16, 0);
#pragma unroll
      for (int s = 14; s < 28; ++s) w2read(s);
      BARRIER();
      const u32 ho = WS_HR + ((dtA * 128 + sb * 4 + (w & 1) * 2) << 10) + lane * 16;
      hr[0] = __builtin_amdgcn_raw_buffer_load_b128(rws, ho, 0, SC1); hr[1] = __builtin_amdgcn_raw_buffer_load_b128(rws, ho, 1024, SC1);
      const u32 l28 = lane < 28 ? lane : 0, ftg = w * 14 + (l28 >> 1);
      wait_flags(rws, WS_F4 + (((db * 28 + (ftg >> 1)) * 4 + (ftg & 1) + 2 * (l28 & 1)) << 7), tag);
    }
    TS(18);
    if (id == 0 && threadIdx.x == 0) __builtin_amdgcn_raw_buffer_store_b32(tag, rws, 0, 0, SC1);
#pragma unroll
    for (int a = 0; a < 4; ++a) acc[a] = f32x16{};
    {
      bf16x8 uf[28][2];
      const u32 vA = WS_UF + ((dtA * 112 + w * 28) << 10) + lane * 16, vB = WS_UF + ((dtB * 112 + w * 28) << 10) + lane * 16;
#pragma unroll
      for (int s = 0; s < HND; ++s) { uf[s][0] = gld_sc1(rws, vA, s * 1024); uf[s][1] = gld_sc1(rws, vB, s * 1024); }
      SBAR();
#pragma unroll
      for (int s = 0; s < 28; ++s) {
        if (s + HND < 28) { uf[s + HND][0] = gld_sc1(rws, vA, (s + HND) * 1024); uf[s + HND][1] = gld_sc1(rws, vB, (s + HND) * 1024); }
        acc[0] = MFMA(w2f[s][0], uf[s][0], acc[0]); acc[1] = MFMA(w2f[s][1], uf[s][0], acc[1]);
        acc[2] = MFMA(w2f[s][0], uf[s][1], acc[2]); acc[3] = MFMA(w2f[s][1], uf[s][1], acc[3]);
        SBAR();
      }
    }
    const f32x16 yv = reduce4(acc, L0 + L_Z, w, lane);
    const rsrc_t ro = mkrsrc(out, SEQ * DM * 2);
    const u32 vo = (sb * 64 + (w & 1) * 32 + 4 * hh) * 896 + (dtA * 32 + r32) * 2;
#pragma unroll
    for (int i = 0; i < 16; ++i) {
      const u32 bw = bb[i >> 2][(i >> 1) & 1], hw = hr[i >> 3][(i >> 1) & 3];
      const float z = yv[i] + ((i & 1) ? bfhi(bw) : bflo(bw)) + ((i & 1) ? bfhi(hw) : bflo(hw));
      __builtin_amdgcn_raw_buffer_store_b16((unsigned short)cvtpk(z, 0.f), ro, vo, ((i & 3) + 8 * (i >> 2)) * 896, SC1);
    }
  }
  WAIT_VM(0);
  TS(19);
  if (id == 0 && w == 0) { const u64 until = enc0 + 10000 + (ENC_CYC ? (enci - encj) / 10 : (enci - encj)); while (__builtin_amdgcn_s_memrealtime() < until) __builtin_amdgcn_s_sleep(1); }
}

extern "C" void kernel_launch(void* const* d_in, const int* in_sizes, int n_in, void* d_out, int out_size, void* d_ws, size_t ws_size, hipStream_t stream) {
  (void)hipFuncSetAttribute((const void*)fused_block, hipFuncAttributeMaxDynamicSharedMemorySize, LDS_BYTES);
  fused_block<<<256, 256, LDS_BYTES, stream>>>((const u32*)d_in[0], (const u32*)d_in[1], (const u32*)d_in[2], (const u32*)d_in[4], (const u32*)d_in[5],
                                               (const u32*)d_in[6], (const u32*)d_in[7], (unsigned short*)d_out, (char*)d_ws);
}
```

```cpp
#include <hip/hip_runtime.h>
#include <hip/hip_bf16.h>

constexpr unsigned WS_KV = 1048576u, WS_OF = 5242880u, WS_HN = 7340032u, WS_HR = 9437184u, WS_UF = 11534336u;
constexpr unsigned LDS_BYTES = 163840;

__global__ void __launch_bounds__(256, 1)
fused_block(const void* x, const void* wqkv, void* ws, const void* w1, const void* wo, const void* w2, const void* b1, const void* b2, void* out) {
  unsigned tid = threadIdx.x;
  asm volatile(
    "mk_begin:\n"
    "s_load_dwordx2 s[4:5], %1, 0\n"
    "s_load_dwordx2 s[8:9], %1, 8\n"
    "s_load_dwordx2 s[12:13], %1, 16\n"
    "s_load_dwordx2 s[16:17], %1, 24\n"
    "s_load_dwordx2 s[20:21], %1, 32\n"
    "s_load_dwordx2 s[24:25], %1, 40\n"
    "s_load_dwordx2 s[28:29], %1, 48\n"
    "s_load_dwordx2 s[32:33], %1, 56\n"
    "s_load_dwordx2 s[36:37], %1, 64\n"
    "s_mov_b32 s45, %2\n"
    "v_readfirstlane_b32 s44, v0\n"
    "s_and_b32 s46, s45, 7\n"
    "s_lshr_b32 s47, s45, 3\n"
    "s_lshr_b32 s60, s46, 1\n"
    "s_and_b32 s51, s47, 3\n"
    "s_lshl2_add_u32 s48, s60, s51\n"
    "s_and_b32 s61, s46, 1\n"
    "s_lshr_b32 s62, s47, 2\n"
    "s_lshl3_add_u32 s49, s61, s62\n"
    "s_lshr_b32 s44, s44, 6\n"
    "v_and_b32 v0, 63, v0\n"
    "v_lshlrev_b32 v1, 4, v0\n"
    "s_getpc_b64 s[40:41]\n"
    "mk_pc:\n"
    "s_sub_u32 s40, s40, mk_pc-mk_begin\n"
    "s_subb_u32 s41, s41, 0\n"
    "s_mov_b32 s42, mk_end-mk_begin\n"
    "s_mov_b32 s43, 0x20000\n"
    "s_lshl2_add_u32 s60, s47, s44\n"
    "s_lshl_b32 s60, s60, 9\n"
    "v_lshlrev_b32 v117, 7, v0\n"
    "s_mov_b64 exec, 15\n"
    "buffer_load_dword a255, v117, s[40:43], s60 offen\n"
    "s_mov_b64 exec, -1\n"
    "s_xor_b32 s63, s51, s62\n"
    "s_and_b32 s63, s63, 1\n"
    "s_mul_i32 s63, s63, 3\n"
    "v_lshrrev_b32 v124, 3, v0\n"
    "v_and_b32 v113, 7, v0\n"
    "v_lshrrev_b32 v114, 4, v0\n"
    "v_xor_b32 v114, v114, v113\n"
    "s_lshl_b32 s64, s49, 2\n"
    "s_add_u32 s64, s64, s44\n"
    "s_mul_i32 s64, s64, 0x7000\n"
    "v_mul_u32_u24 v115, 0x380, v124\n"
    "v_add_u32 v115, s64, v115\n"
    "v_lshl_add_u32 v6, v114, 4, v115\n"
    "v_xor_b32 v114, 4, v114\n"
    "v_lshl_add_u32 v7, v114, 4, v115\n"
    "v_add_u32 v7, 0x1800, v7\n"
    "v_add_u32 v8, 0x3000, v6\n"
    "v_add_u32 v9, 0x3000, v7\n"
    "v_and_b32 v114, 2, v124\n"
    "v_lshlrev_b32 v114, 1, v114\n"
    "v_xor_b32 v114, v114, v113\n"
    "v_lshlrev_b32 v125, 4, v114\n"
    "s_and_b32 s3, s48, 1\n"
    "s_lshr_b32 s66, s48, 1\n"
    "s_mul_i32 s66, s66, 0x70\n"
    "s_mul_i32 s67, s3, 0x380\n"
    "s_add_u32 s66, s66, s67\n"
    "s_mul_i32 s67, s44, 0xa800\n"
    "s_add_u32 s66, s66, s67\n"
    "s_add_u32 s66, s66, 0x380\n"
    "v_mul_u32_u24 v115, 0xa80, v124\n"
    "v_add3_u32 v10, v115, v125, s66\n"
    "v_add_u32 v11, 0x5000, v10\n"
    "s_lshl_b32 s70, s63, 7\n"
    "s_mul_i32 s86, s63, 0x2a000\n"
    "s_mul_i32 s68, s44, 0x7000\n"
    "s_lshl_b32 s69, s44, 11\n"
    "s_add_u32 s69, s69, 0x1c000\n"
    "s_mov_b32 s6, 0x1c0000\n"
    "s_mov_b32 s7, 0x20000\n"
    "s_mov_b32 s10, 0x126000\n"
    "s_mov_b32 s11, 0x20000\n"
    "s_mov_b32 s14, 0xd00000\n"
    "s_mov_b32 s15, 0x20000\n"
    "s_mov_b32 s58, 0xf7f77f7f\n"
    "s_mov_b32 s59, 0xf7f77f7f\n"
    "s_mov_b32 m0, s68\n"
    "s_waitcnt lgkmcnt(0)\n"
    "buffer_load_dwordx4 v6, s[4:7], s70 offen lds\n"
    "buffer_load_dwordx4 v7, s[4:7], s70 offen offset:1024 lds\n"
    "buffer_load_dwordx4 v8, s[4:7], s70 offen offset:2048 lds\n"
    "buffer_load_dwordx4 v9, s[4:7], s70 offen offset:3072 lds\n"
    "s_add_u32 m0, s69, 0\n"
    "s_mov_b64 exec, s[58:59]\n"
    "buffer_load_dwordx4 v10, s[8:11], s86 offen lds\n"
    "buffer_load_dwordx4 v11, s[8:11], s86 offen offset:1024 lds\n"
    "s_mov_b64 exec, -1\n"
    "s_mov_b64 s[0:1], s[58:59]\n"
    "s_add_u32 s71, s70, 0x80\n"
    "s_cmp_eq_u32 s71, 0x380\n"
    "s_cselect_b32 s71, 0, s71\n"
    "s_mul_i32 s87, s71, 0x540\n"
    "s_add_u32 s72, s71, 0x80\n"
    "s_cmp_eq_u32 s72, 0x380\n"
    "s_cselect_b32 s72, 0, s72\n"
    "s_mul_i32 s88, s72, 0x540\n"
    "v_bfe_u32 v120, v0, 2, 2\n"
    "v_bfe_u32 v113, v0, 4, 1\n"
    "v_and_b32 v114, 3, v0\n"
    "v_lshlrev_b32 v114, 3, v114\n"
    "v_lshl_add_u32 v121, v113, 5, v114\n"
    "v_lshrrev_b32 v122, 5, v0\n"
    "v_and_b32 v123, 31, v0\n"
    "v_mov_b32 v20, 0x3f803f80\n"
    "v_mov_b32 v21, 0x3f803f80\n"
    "v_and_b32 v113, 2, v0\n"
    "v_lshlrev_b32 v113, 5, v113\n"
    "v_xor_b32 v113, 0x70, v113\n"
    "s_mul_i32 s60, s44, 0x3000\n"
    "s_add_u32 s60, s60, 0x1c000\n"
    "v_lshl_add_u32 v114, v0, 7, s60\n"
    "v_add_u32 v113, v113, v114\n"
    "ds_write_b64 v113, v[20:21]\n"
    "ds_write_b64 v113, v[20:21] offset:4096\n"
    "v_bfe_u32 v113, v123, 1, 3\n"
    "v_xor_b32 v113, v113, v122\n"
    "v_lshl_add_u32 v114, v123, 7, s68\n"
    "s_add_u32 s64, s44, 0\n"
    "s_and_b32 s64, s64, 3\n"
    "s_add_u32 s65, s44, 1\n"
    "s_and_b32 s65, s65, 3\n"
    "s_add_u32 s66, s44, 2\n"
    "s_and_b32 s66, s66, 3\n"
    "s_add_u32 s67, s44, 3\n"
    "s_and_b32 s67, s67, 3\n"
    "s_lshl_b32 s60, s64, 1\n"
    "v_xor_b32 v115, s60, v113\n"
    "v_lshl_add_u32 v13, v115, 4, v114\n"
    "s_lshl_b32 s60, s65, 1\n"
    "v_xor_b32 v115, s60, v113\n"
    "v_lshl_add_u32 v14, v115, 4, v114\n"
    "s_lshl_b32 s60, s66, 1\n"
    "v_xor_b32 v115, s60, v113\n"
    "v_lshl_add_u32 v15, v115, 4, v114\n"
    "s_lshl_b32 s60, s67, 1\n"
    "v_xor_b32 v115, s60, v113\n"
    "v_lshl_add_u32 v16, v115, 4, v114\n"
    "s_add_u32 m0, s68, 0x1000\n"
    "s_nop 0\n"
    "buffer_load_dwordx4 v6, s[4:7], s71 offen lds\n"
    "buffer_load_dwordx4 v7, s[4:7], s71 offen offset:1024 lds\n"
    "buffer_load_dwordx4 v8, s[4:7], s71 offen offset:2048 lds\n"
    "buffer_load_dwordx4 v9, s[4:7], s71 offen offset:3072 lds\n"
    "s_add_u32 m0, s69, 0x2000\n"
    "s_mov_b64 exec, s[0:1]\n"
    "buffer_load_dwordx4 v10, s[8:11], s87 offen lds\n"
    "buffer_load_dwordx4 v11, s[8:11], s87 offen offset:1024 lds\n"
    "s_mov_b64 exec, -1\n"
    "buffer_load_dword v22, off, s[12:15], 0 sc1\n"
    "v_lshl_add_u32 v113, v122, 3, v120\n"
    "v_bfe_u32 v114, v120, 1, 1\n"
    "v_lshlrev_b32 v114, 6, v114\n"
    "v_lshlrev_b32 v115, 7, v113\n"
    "v_xor_b32 v116, v121, v114\n"
    "v_add_u32 v116, v115, v116\n"
    "v_add_u32 v117, 56, v121\n"
    "v_xor_b32 v117, v117, v114\n"
    "v_add_u32 v117, v115, v117\n"
    "s_lshl_b32 s60, s64, 11\n"
    "s_add_u32 s60, s60, 0x1c000\n"
    "v_add_u32 v60, s60, v116\n"
    "v_add_u32 v17, s60, v117\n"
    "s_lshl_b32 s60, s65, 11\n"
    "s_add_u32 s60, s60, 0x1c000\n"
    "v_add_u32 v61, s60, v116\n"
    "v_add_u32 v18, s60, v117\n"
    "s_lshl_b32 s60, s66, 11\n"
    "s_add_u32 s60, s60, 0x1c000\n"
    "v_add_u32 v62, s60, v116\n"
    "v_add_u32 v19, s60, v117\n"
    "s_lshl_b32 s60, s67, 11\n"
    "s_add_u32 s60, s60, 0x1c000\n"
    "v_add_u32 v63, s60, v116\n"
    "v_add_u32 v118, s60, v117\n"
    "s_lshl_b32 s61, s3, 3\n"
    "v_lshl_add_u32 v115, v113, 6, s61\n"
    "v_add_u32 v117, v115, v121\n"
    "v_lshl_add_u32 v113, v122, 2, v120\n"
    "s_and_b32 s60, s44, 1\n"
    "s_lshl_b32 s60, s60, 6\n"
    "v_xor_b32 v114, s60, v114\n"
    "v_lshl_add_u32 v115, v113, 7, v114\n"
    "v_add_u32 v4, v115, v121\n"
    "v_xor_b32 v5, 64, v4\n"
    "s_add_u32 s60, s68, 0x2000\n"
    "v_mul_u32_u24 v113, 0x48, v113\n"
    "v_add3_u32 v120, v113, v121, s60\n"
    "v_mul_u32_u24 v113, 0x48, v123\n"
    "v_lshlrev_b32 v114, 3, v122\n"
    "v_add3_u32 v121, v113, v114, s60\n"
    "s_add_u32 m0, s68, 0x2000\n"
    "s_nop 0\n"
    "buffer_load_dwordx4 v6, s[4:7], s72 offen lds\n"
    "buffer_load_dwordx4 v7, s[4:7], s72 offen offset:1024 lds\n"
    "buffer_load_dwordx4 v8, s[4:7], s72 offen offset:2048 lds\n"
    "buffer_load_dwordx4 v9, s[4:7], s72 offen offset:3072 lds\n"
    "s_add_u32 m0, s69, 0x4000\n"
    "s_mov_b64 exec, s[0:1]\n"
    "buffer_load_dwordx4 v10, s[8:11], s88 offen lds\n"
    "buffer_load_dwordx4 v11, s[8:11], s88 offen offset:1024 lds\n"
    "s_mov_b64 exec, -1\n"
    "s_lshl4_add_u32 s60, s48, s49\n"
    "s_lshl2_add_u32 s60, s60, s44\n"
    "s_lshl_b32 s60, s60, 7\n"
    "s_add_u32 s60, s60, 0x1000\n"
    "v_lshlrev_b32 v113, 3, v0\n"
    "s_mov_b64 exec, 0xffff\n"
    "buffer_store_dwordx2 v[20:21], v113, s[12:15], s60 offen sc1\n"
    "s_mov_b64 exec, -1\n"
    "s_add_u32 s73, s72, 0x80\n"
    "s_cmp_eq_u32 s73, 0x380\n"
    "s_cselect_b32 s73, 0, s73\n"
    "s_mul_i32 s89, s73, 0x540\n"
    "s_add_u32 s74, s73, 0x80\n"
    "s_cmp_eq_u32 s74, 0x380\n"
    "s_cselect_b32 s74, 0, s74\n"
    "s_mul_i32 s90, s74, 0x540\n"
    "s_add_u32 s75, s74, 0x80\n"
    "s_cmp_eq_u32 s75, 0x380\n"
    "s_cselect_b32 s75, 0, s75\n"
    "s_mul_i32 s91, s75, 0x540\n"
    "s_add_u32 s76, s75, 0x80\n"
    "s_cmp_eq_u32 s76, 0x380\n"
    "s_cselect_b32 s76, 0, s76\n"
    "s_mul_i32 s92, s76, 0x540\n"
    "s_waitcnt vmcnt(14) lgkmcnt(0)\n"
    "s_barrier\n"
    "ds_read_b128 v[128:131], v13\n"
    "ds_read_b64_tr_b16 v[24:25], v60\n"
    "ds_read_b64_tr_b16 v[26:27], v60 offset:512\n"
    "ds_read_b64_tr_b16 v[28:29], v17\n"
    "ds_read_b64_tr_b16 v[30:31], v17 offset:512\n"
    "ds_read_b128 v[132:135], v14\n"
    "ds_read_b64_tr_b16 v[32:33], v61\n"
    "ds_read_b64_tr_b16 v[34:35], v61 offset:512\n"
    "ds_read_b64_tr_b16 v[36:37], v18\n"
    "ds_read_b64_tr_b16 v[38:39], v18 offset:512\n"
    "ds_read_b128 v[136:139], v15\n"
    "ds_read_b128 v[140:143], v16\n"
    "s_waitcnt lgkmcnt(7)\n"
    "v_mfma_f32_32x32x16_bf16 v[64:79], v[24:27], v[128:131], 0\n"
    "ds_read_b64_tr_b16 v[40:41], v62\n"
    "ds_read_b64_tr_b16 v[42:43], v62 offset:512\n"
    "v_mfma_f32_32x32x16_bf16 v[80:95], v[28:31], v[128:131], 0\n"
    "ds_read_b64_tr_b16 v[44:45], v19\n"
    "ds_read_b64_tr_b16 v[46:47], v19 offset:512\n"
    "v_mfma_f32_4x4x4_16b_bf16 v[52:55], v[20:21], v[24:25], 0\n"
    "v_mfma_f32_4x4x4_16b_bf16 v[48:51], v[128:129], v[128:129], 0\n"
    "s_add_u32 m0, s69, 0x6000\n"
    "s_mov_b64 exec, s[0:1]\n"
    "buffer_load_dwordx4 v10, s[8:11], s89 offen lds\n"
    "buffer_load_dwordx4 v11, s[8:11], s89 offen offset:1024 lds\n"
    "s_mov_b64 exec, -1\n"
    "v_mfma_f32_4x4x4_16b_bf16 v[56:59], v[20:21], v[28:29], 0\n"
    "v_mfma_f32_4x4x4_16b_bf16 v[240:243], v[130:131], v[130:131], 0\n"
    "s_mul_i32 s61, s48, 56\n"
    "v_mfma_f32_4x4x4_16b_bf16 v[52:55], v[20:21], v[26:27], v[52:55]\n"
    "s_lshl_b32 s62, s3, 3\n"
    "s_waitcnt lgkmcnt(10)\n"
    "v_mfma_f32_4x4x4_16b_bf16 v[48:51], v[132:133], v[132:133], v[48:51]\n"
    "s_sub_u32 s61, s61, s62\n"
    "v_mfma_f32_4x4x4_16b_bf16 v[56:59], v[20:21], v[30:31], v[56:59]\n"
    "s_mul_i32 s62, s44, 0xa800\n"
    "v_mfma_f32_4x4x4_16b_bf16 v[240:243], v[134:135], v[134:135], v[240:243]\n"
    "s_add_u32 s61, s61, s62\n"
    "s_waitcnt lgkmcnt(5)\n"
    "v_mfma_f32_4x4x4_16b_bf16 v[48:51], v[136:137], v[136:137], v[48:51]\n"
    "v_lshrrev_b32 v113, 2, v0\n"
    "v_mfma_f32_4x4x4_16b_bf16 v[240:243], v[138:139], v[138:139], v[240:243]\n"
    "v_mul_u32_u24 v113, 0xa80, v113\n"
    "s_waitcnt lgkmcnt(4)\n"
    "v_mfma_f32_4x4x4_16b_bf16 v[48:51], v[140:141], v[140:141], v[48:51]\n"
    "v_and_b32 v114, 3, v0\n"
    "v_mfma_f32_4x4x4_16b_bf16 v[240:243], v[142:143], v[142:143], v[240:243]\n"
    "v_lshlrev_b32 v114, 4, v114\n"
    "v_mfma_f32_32x32x16_bf16 v[64:79], v[32:35], v[132:135], v[64:79]\n"
    "ds_read_b64_tr_b16 v[24:25], v63\n"
    "ds_read_b64_tr_b16 v[26:27], v63 offset:512\n"
    "s_add_u32 m0, s68, 0x3000\n"
    "s_nop 0\n"
    "buffer_load_dwordx4 v6, s[4:7], s73 offen lds\n"
    "v_mfma_f32_32x32x16_bf16 v[80:95], v[36:39], v[132:135], v[80:95]\n"
    "ds_read_b64_tr_b16 v[28:29], v118\n"
    "ds_read_b64_tr_b16 v[30:31], v118 offset:512\n"
    "buffer_load_dwordx4 v7, s[4:7], s73 offen offset:1024 lds\n"
    "v_add3_u32 v12, v113, v114, s61\n"
    "s_lshr_b32 s60, s47, 2\n"
    "s_waitcnt lgkmcnt(4)\n"
    "v_mfma_f32_32x32x16_bf16 v[64:79], v[40:43], v[136:139], v[64:79]\n"
    "s_waitcnt vmcnt(12)\n"
    "s_barrier\n"
    "ds_read_b128 v[144:147], v13 offset:4096\n"
    "ds_read_b64_tr_b16 v[32:33], v60 offset:8192\n"
    "ds_read_b64_tr_b16 v[34:35], v60 offset:8704\n"
    "v_mfma_f32_32x32x16_bf16 v[80:95], v[44:47], v[136:139], v[80:95]\n"
    "ds_read_b128 v[148:151], v14 offset:4096\n"
    "ds_read_b64_tr_b16 v[36:37], v17 offset:8192\n"
    "ds_read_b64_tr_b16 v[38:39], v17 offset:8704\n"
    "s_waitcnt lgkmcnt(6)\n"
    "v_mfma_f32_32x32x16_bf16 v[64:79], v[24:27], v[140:143], v[64:79]\n"
    "ds_read_b128 v[152:155], v15 offset:4096\n"
    "ds_read_b64_tr_b16 v[40:41], v61 offset:8192\n"
    "ds_read_b64_tr_b16 v[42:43], v61 offset:8704\n"
    "buffer_load_dwordx4 v8, s[4:7], s73 offen offset:2048 lds\n"
    "v_mfma_f32_32x32x16_bf16 v[80:95], v[28:31], v[140:143], v[80:95]\n"
    "ds_read_b128 v[156:159], v16 offset:4096\n"
    "ds_read_b64_tr_b16 v[44:45], v18 offset:8192\n"
    "ds_read_b64_tr_b16 v[46:47], v18 offset:8704\n"
    "buffer_load_dwordx4 v9, s[4:7], s73 offen offset:3072 lds\n"
    "s_waitcnt lgkmcnt(6)\n"
    "v_mfma_f32_32x32x16_bf16 v[64:79], v[32:35], v[144:147], v[64:79]\n"
    "ds_read_b64_tr_b16 v[24:25], v62 offset:8192\n"
    "ds_read_b64_tr_b16 v[26:27], v62 offset:8704\n"
    "v_mfma_f32_32x32x16_bf16 v[80:95], v[36:39], v[144:147], v[80:95]\n"
    "ds_read_b64_tr_b16 v[28:29], v19 offset:8192\n"
    "ds_read_b64_tr_b16 v[30:31], v19 offset:8704\n"
    "v_mfma_f32_4x4x4_16b_bf16 v[52:55], v[20:21], v[32:33], v[52:55]\n"
    "v_mfma_f32_4x4x4_16b_bf16 v[48:51], v[144:145], v[144:145], v[48:51]\n"
    "s_add_u32 m0, s69, 0x8000\n"
    "s_mov_b64 exec, s[0:1]\n"
    "buffer_load_dwordx4 v10, s[8:11], s90 offen lds\n"
    "buffer_load_dwordx4 v11, s[8:11], s90 offen offset:1024 lds\n"
    "s_mov_b64 exec, -1\n"
    "v_mfma_f32_4x4x4_16b_bf16 v[56:59], v[20:21], v[36:37], v[56:59]\n"
    "v_mfma_f32_4x4x4_16b_bf16 v[240:243], v[146:147], v[146:147], v[240:243]\n"
    "s_lshl3_add_u32 s50, s46, s60\n"
    "v_mfma_f32_4x4x4_16b_bf16 v[52:55], v[20:21], v[34:35], v[52:55]\n"
    "s_lshl2_add_u32 s52, s51, s44\n"
    "v_mfma_f32_4x4x4_16b_bf16 v[48:51], v[148:149], v[148:149], v[48:51]\n"
    "s_mul_i32 s61, s47, 43\n"
    "v_mfma_f32_4x4x4_16b_bf16 v[56:59], v[20:21], v[38:39], v[56:59]\n"
    "s_lshr_b32 s61, s61, 7\n"
    "v_mfma_f32_4x4x4_16b_bf16 v[240:243], v[150:151], v[150:151], v[240:243]\n"
    "s_and_b32 s62, s46, 1\n"
    "s_waitcnt lgkmcnt(9)\n"
    "v_mfma_f32_4x4x4_16b_bf16 v[48:51], v[152:153], v[152:153], v[48:51]\n"
    "s_cmp_eq_u32 s62, 1\n"
    "s_cselect_b32 s56, s61, s60\n"
    "s_cselect_b32 s63, 3, 4\n"
    "v_mfma_f32_4x4x4_16b_bf16 v[240:243], v[154:155], v[154:155], v[240:243]\n"
    "s_mul_i32 s63, s63, s56\n"
    "s_waitcnt lgkmcnt(6)\n"
    "v_mfma_f32_4x4x4_16b_bf16 v[48:51], v[156:157], v[156:157], v[48:51]\n"
    "s_sub_u32 s63, s47, s63\n"
    "v_mfma_f32_4x4x4_16b_bf16 v[240:243], v[158:159], v[158:159], v[240:243]\n"
    "s_lshl2_add_u32 s53, s62, s63\n"
    "s_waitcnt lgkmcnt(4)\n"
    "v_mfma_f32_32x32x16_bf16 v[64:79], v[40:43], v[148:151], v[64:79]\n"
    "ds_read_b64_tr_b16 v[32:33], v63 offset:8192\n"
    "ds_read_b64_tr_b16 v[34:35], v63 offset:8704\n"
    "s_add_u32 m0, s68, 0x4000\n"
    "s_nop 0\n"
    "buffer_load_dwordx4 v6, s[4:7], s74 offen lds\n"
    "v_mfma_f32_32x32x16_bf16 v[80:95], v[44:47], v[148:151], v[80:95]\n"
    "ds_read_b64_tr_b16 v[36:37], v118 offset:8192\n"
    "ds_read_b64_tr_b16 v[38:39], v118 offset:8704\n"
    "buffer_load_dwordx4 v7, s[4:7], s74 offen offset:1024 lds\n"
    "s_and_b32 s61, s56, 0\n"
    "s_mov_b32 s57, 0\n"
    "s_waitcnt lgkmcnt(4)\n"
    "v_mfma_f32_32x32x16_bf16 v[64:79], v[24:27], v[152:155], v[64:79]\n"
    "s_waitcnt vmcnt(11)\n"
    "s_barrier\n"
    "ds_read_b128 v[160:163], v13 offset:8192\n"
    "ds_read_b64_tr_b16 v[40:41], v60 offset:16384\n"
    "ds_read_b64_tr_b16 v[42:43], v60 offset:16896\n"
    "v_mfma_f32_32x32x16_bf16 v[80:95], v[28:31], v[152:155], v[80:95]\n"
    "ds_read_b128 v[164:167], v14 offset:8192\n"
    "ds_read_b64_tr_b16 v[44:45], v17 offset:16384\n"
    "ds_read_b64_tr_b16 v[46:47], v17 offset:16896\n"
    "s_waitcnt lgkmcnt(6)\n"
    "v_mfma_f32_32x32x16_bf16 v[64:79], v[32:35], v[156:159], v[64:79]\n"
    "ds_read_b128 v[168:171], v15 offset:8192\n"
    "ds_read_b64_tr_b16 v[24:25], v61 offset:16384\n"
    "ds_read_b64_tr_b16 v[26:27], v61 offset:16896\n"
    "buffer_load_dwordx4 v8, s[4:7], s74 offen offset:2048 lds\n"
    "v_mfma_f32_32x32x16_bf16 v[80:95], v[36:39], v[156:159], v[80:95]\n"
    "ds_read_b128 v[172:175], v16 offset:8192\n"
    "ds_read_b64_tr_b16 v[28:29], v18 offset:16384\n"
    "ds_read_b64_tr_b16 v[30:31], v18 offset:16896\n"
    "buffer_load_dwordx4 v9, s[4:7], s74 offen offset:3072 lds\n"
    "s_waitcnt lgkmcnt(6)\n"
    "v_mfma_f32_32x32x16_bf16 v[64:79], v[40:43], v[160:163], v[64:79]\n"
    "ds_read_b64_tr_b16 v[32:33], v62 offset:16384\n"
    "ds_read_b64_tr_b16 v[34:35], v62 offset:16896\n"
    "v_mfma_f32_32x32x16_bf16 v[80:95], v[44:47], v[160:163], v[80:95]\n"
    "ds_read_b64_tr_b16 v[36:37], v19 offset:16384\n"
    "ds_read_b64_tr_b16 v[38:39], v19 offset:16896\n"
    "v_mfma_f32_4x4x4_16b_bf16 v[52:55], v[20:21], v[40:41], v[52:55]\n"
    "v_mfma_f32_4x4x4_16b_bf16 v[48:51], v[160:161], v[160:161], v[48:51]\n"
    "s_add_u32 m0, s69, 0xa000\n"
    "s_mov_b64 exec, s[0:1]\n"
    "buffer_load_dwordx4 v10, s[8:11], s91 offen lds\n"
    "buffer_load_dwordx4 v11, s[8:11], s91 offen offset:1024 lds\n"
    "s_mov_b64 exec, -1\n"
    "v_mfma_f32_4x4x4_16b_bf16 v[56:59], v[20:21], v[44:45], v[56:59]\n"
    "v_mfma_f32_4x4x4_16b_bf16 v[240:243], v[162:163], v[162:163], v[240:243]\n"
    "s_lshl_b32 s2, s56, 1\n"
    "v_mfma_f32_4x4x4_16b_bf16 v[52:55], v[20:21], v[42:43], v[52:55]\n"
    "s_and_b32 s2, s2, 0\n"
    "v_mfma_f32_4x4x4_16b_bf16 v[48:51], v[164:165], v[164:165], v[48:51]\n"
    "s_xor_b32 s2, s2, s63\n"
    "v_mfma_f32_4x4x4_16b_bf16 v[56:59], v[20:21], v[46:47], v[56:59]\n"
    "s_lshr_b32 s60, s46, 1\n"
    "v_mfma_f32_4x4x4_16b_bf16 v[240:243], v[166:167], v[166:167], v[240:243]\n"
    "s_min_u32 s61, s56, 6\n"
    "s_waitcnt lgkmcnt(9)\n"
    "v_mfma_f32_4x4x4_16b_bf16 v[48:51], v[168:169], v[168:169], v[48:51]\n"
    "s_mul_i32 s54, s60, 7\n"
    "v_mfma_f32_4x4x4_16b_bf16 v[240:243], v[170:171], v[170:171], v[240:243]\n"
    "s_add_u32 s54, s54, s61\n"
    "s_waitcnt lgkmcnt(6)\n"
    "v_mfma_f32_4x4x4_16b_bf16 v[48:51], v[172:173], v[172:173], v[48:51]\n"
    "s_min_u32 s61, s56, 7\n"
    "v_mfma_f32_4x4x4_16b_bf16 v[240:243], v[174:175], v[174:175], v[240:243]\n"
    "s_lshl3_add_u32 s55, s60, s61\n"
    "s_waitcnt lgkmcnt(4)\n"
    "v_mfma_f32_32x32x16_bf16 v[64:79], v[24:27], v[164:167], v[64:79]\n"
    "ds_read_b64_tr_b16 v[40:41], v63 offset:16384\n"
    "ds_read_b64_tr_b16 v[42:43], v63 offset:16896\n"
    "s_add_u32 m0, s68, 0x5000\n"
    "s_nop 0\n"
    "buffer_load_dwordx4 v6, s[4:7], s75 offen lds\n"
    "v_mfma_f32_32x32x16_bf16 v[80:95], v[28:31], v[164:167], v[80:95]\n"
    "ds_read_b64_tr_b16 v[44:45], v118 offset:16384\n"
    "ds_read_b64_tr_b16 v[46:47], v118 offset:16896\n"
    "buffer_load_dwordx4 v7, s[4:7], s75 offen offset:1024 lds\n"
    "s_mov_b32 s18, 0x700000\n"
    "s_mov_b32 s19, 0x20000\n"
    "s_mov_b32 s22, 0x62000\n"
    "s_mov_b32 s23, 0x20000\n"
    "s_waitcnt lgkmcnt(4)\n"
    "v_mfma_f32_32x32x16_bf16 v[64:79], v[32:35], v[168:171], v[64:79]\n"
    "s_waitcnt vmcnt(10)\n"
    "s_barrier\n"
    "ds_read_b128 v[176:179], v13 offset:12288\n"
    "ds_read_b64_tr_b16 v[24:25], v60 offset:24576\n"
    "ds_read_b64_tr_b16 v[26:27], v60 offset:25088\n"
    "v_mfma_f32_32x32x16_bf16 v[80:95], v[36:39], v[168:171], v[80:95]\n"
    "ds_read_b128 v[180:183], v14 offset:12288\n"
    "ds_read_b64_tr_b16 v[28:29], v17 offset:24576\n"
    "ds_read_b64_tr_b16 v[30:31], v17 offset:25088\n"
    "s_waitcnt lgkmcnt(6)\n"
    "v_mfma_f32_32x32x16_bf16 v[64:79], v[40:43], v[172:175], v[64:79]\n"
    "ds_read_b128 v[184:187], v15 offset:12288\n"
    "ds_read_b64_tr_b16 v[32:33], v61 offset:24576\n"
    "ds_read_b64_tr_b16 v[34:35], v61 offset:25088\n"
    "buffer_load_dwordx4 v8, s[4:7], s75 offen offset:2048 lds\n"
    "v_mfma_f32_32x32x16_bf16 v[80:95], v[44:47], v[172:175], v[80:95]\n"
    "ds_read_b128 v[188:191], v16 offset:12288\n"
    "ds_read_b64_tr_b16 v[36:37], v18 offset:24576\n"
    "ds_read_b64_tr_b16 v[38:39], v18 offset:25088\n"
    "buffer_load_dwordx4 v9, s[4:7], s75 offen offset:3072 lds\n"
    "s_waitcnt lgkmcnt(6)\n"
    "v_mfma_f32_32x32x16_bf16 v[64:79], v[24:27], v[176:179], v[64:79]\n"
    "ds_read_b64_tr_b16 v[40:41], v62 offset:24576\n"
    "ds_read_b64_tr_b16 v[42:43], v62 offset:25088\n"
    "v_mfma_f32_32x32x16_bf16 v[80:95], v[28:31], v[176:179], v[80:95]\n"
    "ds_read_b64_tr_b16 v[44:45], v19 offset:24576\n"
    "ds_read_b64_tr_b16 v[46:47], v19 offset:25088\n"
    "v_mfma_f32_4x4x4_16b_bf16 v[52:55], v[20:21], v[24:25], v[52:55]\n"
    "v_mfma_f32_4x4x4_16b_bf16 v[48:51], v[176:177], v[176:177], v[48:51]\n"
    "s_add_u32 m0, s69, 0\n"
    "s_mov_b64 exec, s[0:1]\n"
    "buffer_load_dwordx4 v10, s[8:11], s92 offen lds\n"
    "buffer_load_dwordx4 v11, s[8:11], s92 offen offset:1024 lds\n"
    "s_mov_b64 exec, -1\n"
    "v_mfma_f32_4x4x4_16b_bf16 v[56:59], v[20:21], v[28:29], v[56:59]\n"
    "v_mfma_f32_4x4x4_16b_bf16 v[240:243], v[178:179], v[178:179], v[240:243]\n"
    "s_mov_b32 s26, 0x700000\n"
    "s_mov_b32 s27, 0x20000\n"
    "v_mfma_f32_4x4x4_16b_bf16 v[52:55], v[20:21], v[26:27], v[52:55]\n"
    "s_mov_b32 s30, 0xe00\n"
    "s_mov_b32 s31, 0x20000\n"
    "v_mfma_f32_4x4x4_16b_bf16 v[48:51], v[180:181], v[180:181], v[48:51]\n"
    "s_mov_b32 s34, 0x1000\n"
    "s_mov_b32 s35, 0x20000\n"
    "v_mfma_f32_4x4x4_16b_bf16 v[56:59], v[20:21], v[30:31], v[56:59]\n"
    "s_mov_b32 s38, 0x1c0000\n"
    "s_mov_b32 s39, 0x20000\n"
    "v_mfma_f32_4x4x4_16b_bf16 v[240:243], v[182:183], v[182:183], v[240:243]\n"
    "s_waitcnt lgkmcnt(9)\n"
    "v_mfma_f32_4x4x4_16b_bf16 v[48:51], v[184:185], v[184:185], v[48:51]\n"
    "v_mfma_f32_4x4x4_16b_bf16 v[240:243], v[186:187], v[186:187], v[240:243]\n"
    "s_waitcnt lgkmcnt(6)\n"
    "v_mfma_f32_4x4x4_16b_bf16 v[48:51], v[188:189], v[188:189], v[48:51]\n"
    "v_mfma_f32_4x4x4_16b_bf16 v[240:243], v[190:191], v[190:191], v[240:243]\n"
    "s_waitcnt lgkmcnt(4)\n"
    "v_mfma_f32_32x32x16_bf16 v[64:79], v[32:35], v[180:183], v[64:79]\n"
    "ds_read_b64_tr_b16 v[24:25], v63 offset:24576\n"
    "ds_read_b64_tr_b16 v[26:27], v63 offset:25088\n"
    "s_add_u32 m0, s68, 0x6000\n"
    "s_nop 0\n"
    "buffer_load_dwordx4 v6, s[4:7], s76 offen lds\n"
    "v_mfma_f32_32x32x16_bf16 v[80:95], v[36:39], v[180:183], v[80:95]\n"
    "ds_read_b64_tr_b16 v[28:29], v118 offset:24576\n"
    "ds_read_b64_tr_b16 v[30:31], v118 offset:25088\n"
    "buffer_load_dwordx4 v7, s[4:7], s76 offen offset:1024 lds\n"
    "s_waitcnt lgkmcnt(4)\n"
    "v_mfma_f32_32x32x16_bf16 v[64:79], v[40:43], v[184:187], v[64:79]\n"
    "s_waitcnt vmcnt(10)\n"
    "s_barrier\n"
    "ds_read_b128 v[192:195], v13 offset:16384\n"
    "ds_read_b64_tr_b16 v[32:33], v60 offset:32768\n"
    "ds_read_b64_tr_b16 v[34:35], v60 offset:33280\n"
    "v_mfma_f32_32x32x16_bf16 v[80:95], v[44:47], v[184:187], v[80:95]\n"
    "ds_read_b128 v[196:199], v14 offset:16384\n"
    "ds_read_b64_tr_b16 v[36:37], v17 offset:32768\n"
    "ds_read_b64_tr_b16 v[38:39], v17 offset:33280\n"
    "s_waitcnt lgkmcnt(6)\n"
    "v_mfma_f32_32x32x16_bf16 v[64:79], v[24:27], v[188:191], v[64:79]\n"
    "ds_read_b128 v[200:203], v15 offset:16384\n"
    "ds_read_b64_tr_b16 v[40:41], v61 offset:32768\n"
    "ds_read_b64_tr_b16 v[42:43], v61 offset:33280\n"
    "buffer_load_dwordx4 v8, s[4:7], s76 offen offset:2048 lds\n"
    "s_lshl_b32 s0, s44, 10\n"
    "s_add_u32 s0, s0, 0x1e000\n"
    "v_mfma_f32_32x32x16_bf16 v[80:95], v[28:31], v[188:191], v[80:95]\n"
    "ds_read_b128 v[204:207], v16 offset:16384\n"
    "ds_read_b64_tr_b16 v[44:45], v18 offset:32768\n"
    "ds_read_b64_tr_b16 v[46:47], v18 offset:33280\n"
    "buffer_load_dwordx4 v9, s[4:7], s76 offen offset:3072 lds\n"
    "s_waitcnt lgkmcnt(6)\n"
    "v_mfma_f32_32x32x16_bf16 v[64:79], v[32:35], v[192:195], v[64:79]\n"
    "ds_read_b64_tr_b16 v[24:25], v62 offset:32768\n"
    "ds_read_b64_tr_b16 v[26:27], v62 offset:33280\n"
    "s_add_u32 m0, s0, 0\n"
    "s_nop 0\n"
    "buffer_load_dwordx4 v12, s[8:11], s86 offen lds\n"
    "v_mfma_f32_32x32x16_bf16 v[80:95], v[36:39], v[192:195], v[80:95]\n"
    "ds_read_b64_tr_b16 v[28:29], v19 offset:32768\n"
    "ds_read_b64_tr_b16 v[30:31], v19 offset:33280\n"
    "v_mfma_f32_4x4x4_16b_bf16 v[52:55], v[20:21], v[32:33], v[52:55]\n"
    "v_mfma_f32_4x4x4_16b_bf16 v[48:51], v[192:193], v[192:193], v[48:51]\n"
    "v_mfma_f32_4x4x4_16b_bf16 v[56:59], v[20:21], v[36:37], v[56:59]\n"
    "v_mfma_f32_4x4x4_16b_bf16 v[240:243], v[194:195], v[194:195], v[240:243]\n"
    "v_mfma_f32_4x4x4_16b_bf16 v[52:55], v[20:21], v[34:35], v[52:55]\n"
    "v_mfma_f32_4x4x4_16b_bf16 v[48:51], v[196:197], v[196:197], v[48:51]\n"
    "v_mfma_f32_4x4x4_16b_bf16 v[56:59], v[20:21], v[38:39], v[56:59]\n"
    "v_mfma_f32_4x4x4_16b_bf16 v[240:243], v[198:199], v[198:199], v[240:243]\n"
    "s_waitcnt lgkmcnt(9)\n"
    "v_mfma_f32_4x4x4_16b_bf16 v[48:51], v[200:201], v[200:201], v[48:51]\n"
    "v_mfma_f32_4x4x4_16b_bf16 v[240:243], v[202:203], v[202:203], v[240:243]\n"
    "s_waitcnt lgkmcnt(6)\n"
    "v_mfma_f32_4x4x4_16b_bf16 v[48:51], v[204:205], v[204:205], v[48:51]\n"
    "v_mfma_f32_4x4x4_16b_bf16 v[240:243], v[206:207], v[206:207], v[240:243]\n"
    "s_waitcnt lgkmcnt(4)\n"
    "v_mfma_f32_32x32x16_bf16 v[64:79], v[40:43], v[196:199], v[64:79]\n"
    "ds_read_b64_tr_b16 v[32:33], v63 offset:32768\n"
    "ds_read_b64_tr_b16 v[34:35], v63 offset:33280\n"
    "s_add_u32 m0, s0, 0x1000\n"
    "s_nop 0\n"
    "buffer_load_dwordx4 v12, s[8:11], s87 offen lds\n"
    "v_mfma_f32_32x32x16_bf16 v[80:95], v[44:47], v[196:199], v[80:95]\n"
    "ds_read_b64_tr_b16 v[36:37], v118 offset:32768\n"
    "ds_read_b64_tr_b16 v[38:39], v118 offset:33280\n"
    "s_waitcnt lgkmcnt(4)\n"
    "v_mfma_f32_32x32x16_bf16 v[64:79], v[24:27], v[200:203], v[64:79]\n"
    "s_waitcnt vmcnt(8)\n"
    "s_barrier\n"
    "ds_read_b128 v[208:211], v13 offset:20480\n"
    "ds_read_b64_tr_b16 v[40:41], v60 offset:40960\n"
    "ds_read_b64_tr_b16 v[42:43], v60 offset:41472\n"
    "v_mfma_f32_32x32x16_bf16 v[80:95], v[28:31], v[200:203], v[80:95]\n"
    "ds_read_b128 v[212:215], v14 offset:20480\n"
    "ds_read_b64_tr_b16 v[44:45], v17 offset:40960\n"
    "ds_read_b64_tr_b16 v[46:47], v17 offset:41472\n"
    "s_waitcnt lgkmcnt(6)\n"
    "v_mfma_f32_32x32x16_bf16 v[64:79], v[32:35], v[204:207], v[64:79]\n"
    "ds_read_b128 v[216:219], v15 offset:20480\n"
    "ds_read_b64_tr_b16 v[24:25], v61 offset:40960\n"
    "ds_read_b64_tr_b16 v[26:27], v61 offset:41472\n"
    "s_add_u32 m0, s0, 0x2000\n"
    "s_nop 0\n"
    "buffer_load_dwordx4 v12, s[8:11], s88 offen lds\n"
    "v_mfma_f32_32x32x16_bf16 v[80:95], v[36:39], v[204:207], v[80:95]\n"
    "ds_read_b128 v[220:223], v16 offset:20480\n"
    "ds_read_b64_tr_b16 v[28:29], v18 offset:40960\n"
    "ds_read_b64_tr_b16 v[30:31], v18 offset:41472\n"
    "s_waitcnt lgkmcnt(6)\n"
    "v_mfma_f32_32x32x16_bf16 v[64:79], v[40:43], v[208:211], v[64:79]\n"
    "ds_read_b64_tr_b16 v[32:33], v62 offset:40960\n"
    "ds_read_b64_tr_b16 v[34:35], v62 offset:41472\n"
    "s_add_u32 m0, s0, 0x3000\n"
    "s_nop 0\n"
    "buffer_load_dwordx4 v12, s[8:11], s89 offen lds\n"
    "v_mfma_f32_32x32x16_bf16 v[80:95], v[44:47], v[208:211], v[80:95]\n"
    "ds_read_b64_tr_b16 v[36:37], v19 offset:40960\n"
    "ds_read_b64_tr_b16 v[38:39], v19 offset:41472\n"
    "v_mfma_f32_4x4x4_16b_bf16 v[52:55], v[20:21], v[40:41], v[52:55]\n"
    "v_mfma_f32_4x4x4_16b_bf16 v[48:51], v[208:209], v[208:209], v[48:51]\n"
    "v_mfma_f32_4x4x4_16b_bf16 v[56:59], v[20:21], v[44:45], v[56:59]\n"
    "v_mfma_f32_4x4x4_16b_bf16 v[240:243], v[210:211], v[210:211], v[240:243]\n"
    "v_mfma_f32_4x4x4_16b_bf16 v[52:55], v[20:21], v[42:43], v[52:55]\n"
    "v_mfma_f32_4x4x4_16b_bf16 v[48:51], v[212:213], v[212:213], v[48:51]\n"
    "v_mfma_f32_4x4x4_16b_bf16 v[56:59], v[20:21], v[46:47], v[56:59]\n"
    "v_mfma_f32_4x4x4_16b_bf16 v[240:243], v[214:215], v[214:215], v[240:243]\n"
    "s_waitcnt lgkmcnt(9)\n"
    "v_mfma_f32_4x4x4_16b_bf16 v[48:51], v[216:217], v[216:217], v[48:51]\n"
    "v_mfma_f32_4x4x4_16b_bf16 v[240:243], v[218:219], v[218:219], v[240:243]\n"
    "s_waitcnt lgkmcnt(6)\n"
    "v_mfma_f32_4x4x4_16b_bf16 v[48:51], v[220:221], v[220:221], v[48:51]\n"
    "v_mfma_f32_4x4x4_16b_bf16 v[240:243], v[222:223], v[222:223], v[240:243]\n"
    "s_waitcnt lgkmcnt(4)\n"
    "v_mfma_f32_32x32x16_bf16 v[64:79], v[24:27], v[212:215], v[64:79]\n"
    "ds_read_b64_tr_b16 v[40:41], v63 offset:40960\n"
    "ds_read_b64_tr_b16 v[42:43], v63 offset:41472\n"
    "s_add_u32 m0, s0, 0x4000\n"
    "s_nop 0\n"
    "buffer_load_dwordx4 v12, s[8:11], s90 offen lds\n"
    "v_mfma_f32_32x32x16_bf16 v[80:95], v[28:31], v[212:215], v[80:95]\n"
    "ds_read_b64_tr_b16 v[44:45], v118 offset:40960\n"
    "ds_read_b64_tr_b16 v[46:47], v118 offset:41472\n"
    "s_and_b32 s60, s46, 1\n"
    "s_lshl_b32 s60, s60, 5\n"
    "s_add_u32 s60, s60, s47\n"
    "s_lshl2_add_u32 s85, s60, s44\n"
    "s_mul_i32 s69, s85, 0x3800\n"
    "s_waitcnt lgkmcnt(4)\n"
    "v_mfma_f32_32x32x16_bf16 v[64:79], v[32:35], v[216:219], v[64:79]\n"
    "s_waitcnt vmcnt(5)\n"
    "s_barrier\n"
    "ds_read_b128 v[224:227], v13 offset:24576\n"
    "ds_read_b64_tr_b16 v[24:25], v60\n"
    "ds_read_b64_tr_b16 v[26:27], v60 offset:512\n"
    "v_mfma_f32_32x32x16_bf16 v[80:95], v[36:39], v[216:219], v[80:95]\n"
    "ds_read_b128 v[228:231], v14 offset:24576\n"
    "ds_read_b64_tr_b16 v[28:29], v17\n"
    "ds_read_b64_tr_b16 v[30:31], v17 offset:512\n"
    "s_lshl_b32 s60, s44, 2\n"
    "s_add_u32 s60, s60, 0x25000\n"
    "v_add_u32 v255, s60, v1\n"
    "v_add_u32 v116, 0x25000, v1\n"
    "s_lshr_b32 s60, s46, 1\n"
    "s_mul_i32 s60, s60, 0x380\n"
    "s_add_u32 s69, s69, s60\n"
    "s_and_b32 s63, s85, 0x7f\n"
    "s_mul_i32 s63, s63, 0xc80\n"
    "s_waitcnt lgkmcnt(6)\n"
    "v_mfma_f32_32x32x16_bf16 v[64:79], v[40:43], v[220:223], v[64:79]\n"
    "ds_read_b128 v[232:235], v15 offset:24576\n"
    "ds_read_b64_tr_b16 v[32:33], v61\n"
    "ds_read_b64_tr_b16 v[34:35], v61 offset:512\n"
    "s_add_u32 m0, s0, 0x5000\n"
    "s_nop 0\n"
    "buffer_load_dwordx4 v12, s[8:11], s91 offen lds\n"
    "v_mfma_f32_32x32x16_bf16 v[80:95], v[44:47], v[220:223], v[80:95]\n"
    "ds_read_b128 v[236:239], v16 offset:24576\n"
    "ds_read_b64_tr_b16 v[36:37], v18\n"
    "ds_read_b64_tr_b16 v[38:39], v18 offset:512\n"
    "s_add_u32 m0, s0, 0x6000\n"
    "s_nop 0\n"
    "buffer_load_dwordx4 v12, s[8:11], s92 offen lds\n"
    "s_lshl_b32 s60, s44, 8\n"
    "s_add_u32 s60, s60, 0x25400\n"
    "v_lshl_add_u32 v12, v0, 2, s60\n"
    "v_lshl_add_u32 v119, v122, 4, s60\n"
    "s_mov_b32 vcc_lo, 0xaaaaaaaa\n"
    "s_mov_b32 vcc_hi, 0xaaaaaaaa\n"
    "s_mov_b32 s58, 0xcccccccc\n"
    "s_mov_b32 s59, 0xcccccccc\n"
    "s_waitcnt lgkmcnt(6)\n"
    "v_mfma_f32_32x32x16_bf16 v[64:79], v[24:27], v[224:227], v[64:79]\n"
    "ds_read_b64_tr_b16 v[40:41], v62\n"
    "ds_read_b64_tr_b16 v[42:43], v62 offset:512\n"
    "v_mfma_f32_32x32x16_bf16 v[80:95], v[28:31], v[224:227], v[80:95]\n"
    "ds_read_b64_tr_b16 v[44:45], v19\n"
    "ds_read_b64_tr_b16 v[46:47], v19 offset:512\n"
    "v_mfma_f32_4x4x4_16b_bf16 v[52:55], v[20:21], v[24:25], v[52:55]\n"
    "v_mfma_f32_4x4x4_16b_bf16 v[48:51], v[224:225], v[224:225], v[48:51]\n"
    "v_mfma_f32_4x4x4_16b_bf16 v[56:59], v[20:21], v[28:29], v[56:59]\n"
    "v_mfma_f32_4x4x4_16b_bf16 v[240:243], v[226:227], v[226:227], v[240:243]\n"
    "s_lshl_b32 s60, s64, 10\n"
    "s_add_u32 s60, s60, 0x1e000\n"
    "v_mfma_f32_4x4x4_16b_bf16 v[52:55], v[20:21], v[26:27], v[52:55]\n"
    "v_add_u32 v6, s60, v117\n"
    "v_mfma_f32_4x4x4_16b_bf16 v[48:51], v[228:229], v[228:229], v[48:51]\n"
    "s_lshl_b32 s60, s65, 10\n"
    "s_add_u32 s60, s60, 0x1e000\n"
    "v_mfma_f32_4x4x4_16b_bf16 v[56:59], v[20:21], v[30:31], v[56:59]\n"
    "v_add_u32 v7, s60, v117\n"
    "v_mfma_f32_4x4x4_16b_bf16 v[240:243], v[230:231], v[230:231], v[240:243]\n"
    "s_lshl_b32 s60, s66, 10\n"
    "s_add_u32 s60, s60, 0x1e000\n"
    "s_waitcnt lgkmcnt(9)\n"
    "v_mfma_f32_4x4x4_16b_bf16 v[48:51], v[232:233], v[232:233], v[48:51]\n"
    "v_add_u32 v8, s60, v117\n"
    "v_mfma_f32_4x4x4_16b_bf16 v[240:243], v[234:235], v[234:235], v[240:243]\n"
    "s_lshl_b32 s60, s67, 10\n"
    "s_add_u32 s60, s60, 0x1e000\n"
    "s_waitcnt lgkmcnt(6)\n"
    "v_mfma_f32_4x4x4_16b_bf16 v[48:51], v[236:237], v[236:237], v[48:51]\n"
    "v_add_u32 v9, s60, v117\n"
    "v_mfma_f32_4x4x4_16b_bf16 v[240:243], v[238:239], v[238:239], v[240:243]\n"
    "v_min_u32 v14, 24, v0\n"
    "v_lshlrev_b32 v14, 7, v14\n"
    "buffer_load_dword a254, v14, s[20:23], s63 offen\n"
    "v_min_u32 v13, 13, v0\n"
    "v_mul_u32_u24 v14, 37, v13\n"
    "v_lshrrev_b32 v14, 8, v14\n"
    "v_mul_u32_u24 v14, 0xa80, v14\n"
    "v_lshl_add_u32 v13, v13, 7, v14\n"
    "s_waitcnt lgkmcnt(4)\n"
    "v_mfma_f32_32x32x16_bf16 v[64:79], v[32:35], v[228:231], v[64:79]\n"
    "ds_read_b64_tr_b16 v[24:25], v63\n"
    "ds_read_b64_tr_b16 v[26:27], v63 offset:512\n"
    "v_permlane32_swap_b32 v52, v56\n"
    "v_cndmask_b32 v112, v48, v49, vcc\n"
    "v_add_f32 v52, v52, v56\n"
    "ds_write_b32 v255, v52\n"
    "v_mfma_f32_32x32x16_bf16 v[80:95], v[36:39], v[228:231], v[80:95]\n"
    "ds_read_b64_tr_b16 v[28:29], v118\n"
    "ds_read_b64_tr_b16 v[30:31], v118 offset:512\n"
    "v_cndmask_b32 v113, v50, v51, vcc\n"
    "v_cndmask_b32 v114, v240, v241, vcc\n"
    "v_cndmask_b32 v115, v242, v243, vcc\n"
    "v_add_f32 v112, v112, v114\n"
    "v_add_f32 v113, v113, v115\n"
    "v_cndmask_b32 v112, v112, v113, s[58:59]\n"
    "v_mov_b32 v113, v112\n"
    "s_waitcnt lgkmcnt(5)\n"
    "v_mfma_f32_32x32x16_bf16 v[64:79], v[40:43], v[232:235], v[64:79]\n"
    "s_waitcnt lgkmcnt(0)\n"
    "s_barrier\n"
    "ds_read_b128 v[52:55], v116\n"
    "v_mfma_f32_32x32x16_bf16 v[80:95], v[44:47], v[232:235], v[80:95]\n"
    "v_permlane32_swap_b32 v112, v113\n"
    "s_and_b32 s61, s48, 14\n"
    "s_lshl_b32 s61, s61, 6\n"
    "s_lshl2_add_u32 s62, s49, s44\n"
    "s_add_u32 s61, s61, s62\n"
    "v_add_f32 v112, v112, v113\n"
    "v_mul_f32 v112, 0x3b124925, v112\n"
    "s_lshl1_add_u32 s61, s61, s3\n"
    "s_lshl_b32 s61, s61, 11\n"
    "s_add_u32 s61, s61, 0x100000\n"
    "s_add_u32 s62, s61, 0x40000\n"
    "v_mfma_f32_32x32x16_bf16 v[80:95], v[28:31], v[236:239], v[80:95]\n"
    "s_waitcnt lgkmcnt(0)\n"
    "v_pk_add_f32 v[52:53], v[52:53], v[54:55]\n"
    "v_add_f32 v52, v52, v53\n"
    "ds_write_b32 v12, v52\n"
    "v_mfma_f32_32x32x16_bf16 v[64:79], v[24:27], v[236:239], v[64:79]\n"
    "ds_read_b128 v[240:243], v119 offset:128\n"
    "ds_read_b128 v[244:247], v119 offset:160\n"
    "ds_read_b128 v[248:251], v119 offset:192\n"
    "ds_read_b128 v[252:255], v119 offset:224\n"
    "ds_read_b128 v[96:99], v119\n"
    "ds_read_b128 v[100:103], v119 offset:32\n"
    "ds_read_b128 v[104:107], v119 offset:64\n"
    "ds_read_b128 v[108:111], v119 offset:96\n"
    "s_cmp_eq_u32 s3, 0\n"
    "s_cselect_b32 s64, 0x3f80, 0\n"
    "s_lshl_b32 s63, s64, 16\n"
    "s_or_b32 s63, s63, 0x3f80\n"
    "v_mov_b32 v113, v92\n"
    "v_mov_b32 v114, v92\n"
    "s_nop 1\n"
    "v_permlane32_swap_b32 v113, v114\n"
    "v_mul_f32 v126, 0x3b124925, v114\n"
    "v_fma_f32 v112, -v126, v126, v112\n"
    "s_waitcnt lgkmcnt(4)\n"
    "v_pk_fma_f32 v[80:81], v[240:241], v[126:127], v[80:81] op_sel_hi:[1,0,1] neg_lo:[0,1,0] neg_hi:[0,1,0]\n"
    "v_max_f32 v112, 0, v112\n"
    "v_pk_fma_f32 v[82:83], v[242:243], v[126:127], v[82:83] op_sel_hi:[1,0,1] neg_lo:[0,1,0] neg_hi:[0,1,0]\n"
    "v_add_f32 v112, 0x3727c5ac, v112\n"
    "v_pk_fma_f32 v[84:85], v[244:245], v[126:127], v[84:85] op_sel_hi:[1,0,1] neg_lo:[0,1,0] neg_hi:[0,1,0]\n"
    "v_rsq_f32 v127, v112\n"
    "s_nop 0\n"
    "v_pk_fma_f32 v[86:87], v[246:247], v[126:127], v[86:87] op_sel_hi:[1,0,1] neg_lo:[0,1,0] neg_hi:[0,1,0]\n"
    "v_pk_fma_f32 v[88:89], v[248:249], v[126:127], v[88:89] op_sel_hi:[1,0,1] neg_lo:[0,1,0] neg_hi:[0,1,0]\n"
    "v_pk_fma_f32 v[90:91], v[250:251], v[126:127], v[90:91] op_sel_hi:[1,0,1] neg_lo:[0,1,0] neg_hi:[0,1,0]\n"
    "v_pk_fma_f32 v[92:93], v[252:253], v[126:127], v[92:93] op_sel_hi:[1,0,1] neg_lo:[0,1,0] neg_hi:[0,1,0]\n"
    "v_pk_fma_f32 v[94:95], v[254:255], v[126:127], v[94:95] op_sel_hi:[1,0,1] neg_lo:[0,1,0] neg_hi:[0,1,0]\n"
    "v_pk_mul_f32 v[80:81], v[80:81], v[126:127] op_sel:[0,1] op_sel_hi:[1,1]\n"
    "v_pk_mul_f32 v[82:83], v[82:83], v[126:127] op_sel:[0,1] op_sel_hi:[1,1]\n"
    "v_pk_mul_f32 v[84:85], v[84:85], v[126:127] op_sel:[0,1] op_sel_hi:[1,1]\n"
    "v_pk_mul_f32 v[86:87], v[86:87], v[126:127] op_sel:[0,1] op_sel_hi:[1,1]\n"
    "v_pk_mul_f32 v[88:89], v[88:89], v[126:127] op_sel:[0,1] op_sel_hi:[1,1]\n"
    "v_pk_mul_f32 v[90:91], v[90:91], v[126:127] op_sel:[0,1] op_sel_hi:[1,1]\n"
    "v_pk_mul_f32 v[92:93], v[92:93], v[126:127] op_sel:[0,1] op_sel_hi:[1,1]\n"
    "v_pk_mul_f32 v[94:95], v[94:95], v[126:127] op_sel:[0,1] op_sel_hi:[1,1]\n"
    "v_cvt_pk_bf16_f32 v32, v80, v81\n"
    "v_cvt_pk_bf16_f32 v33, v82, v83\n"
    "v_cvt_pk_bf16_f32 v34, v84, v85\n"
    "v_cvt_pk_bf16_f32 v35, v86, v87\n"
    "v_cvt_pk_bf16_f32 v36, v88, v89\n"
    "v_cvt_pk_bf16_f32 v37, v90, v91\n"
    "v_cvt_pk_bf16_f32 v38, v92, v93\n"
    "v_cvt_pk_bf16_f32 v39, v94, v95\n"
    "s_mov_b32 exec_lo, 0\n"
    "v_mov_b32 v38, s63\n"
    "v_mov_b32 v39, s64\n"
    "s_mov_b32 exec_lo, -1\n"
    "s_cmp_eq_u32 s3, 0\n"
    "s_cbranch_scc0 L_roleV\n"
    "buffer_store_dwordx4 v[32:35], v1, s[12:15], s62 offen sc1\n"
    "buffer_store_dwordx4 v[36:39], v1, s[12:15], s62 offen offset:1024 sc1\n"
    "s_waitcnt lgkmcnt(0)\n"
    "v_pk_fma_f32 v[64:65], v[96:97], v[126:127], v[64:65] op_sel_hi:[1,0,1] neg_lo:[0,1,0] neg_hi:[0,1,0]\n"
    "v_pk_fma_f32 v[66:67], v[98:99], v[126:127], v[66:67] op_sel_hi:[1,0,1] neg_lo:[0,1,0] neg_hi:[0,1,0]\n"
    "v_pk_fma_f32 v[68:69], v[100:101], v[126:127], v[68:69] op_sel_hi:[1,0,1] neg_lo:[0,1,0] neg_hi:[0,1,0]\n"
    "v_pk_fma_f32 v[70:71], v[102:103], v[126:127], v[70:71] op_sel_hi:[1,0,1] neg_lo:[0,1,0] neg_hi:[0,1,0]\n"
    "v_pk_fma_f32 v[72:73], v[104:105], v[126:127], v[72:73] op_sel_hi:[1,0,1] neg_lo:[0,1,0] neg_hi:[0,1,0]\n"
    "v_pk_fma_f32 v[74:75], v[106:107], v[126:127], v[74:75] op_sel_hi:[1,0,1] neg_lo:[0,1,0] neg_hi:[0,1,0]\n"
    "v_pk_fma_f32 v[76:77], v[108:109], v[126:127], v[76:77] op_sel_hi:[1,0,1] neg_lo:[0,1,0] neg_hi:[0,1,0]\n"
    "v_pk_fma_f32 v[78:79], v[110:111], v[126:127], v[78:79] op_sel_hi:[1,0,1] neg_lo:[0,1,0] neg_hi:[0,1,0]\n"
    "v_pk_mul_f32 v[64:65], v[64:65], v[126:127] op_sel:[0,1] op_sel_hi:[1,1]\n"
    "v_pk_mul_f32 v[66:67], v[66:67], v[126:127] op_sel:[0,1] op_sel_hi:[1,1]\n"
    "v_pk_mul_f32 v[68:69], v[68:69], v[126:127] op_sel:[0,1] op_sel_hi:[1,1]\n"
    "v_pk_mul_f32 v[70:71], v[70:71], v[126:127] op_sel:[0,1] op_sel_hi:[1,1]\n"
    "v_pk_mul_f32 v[72:73], v[72:73], v[126:127] op_sel:[0,1] op_sel_hi:[1,1]\n"
    "v_pk_mul_f32 v[74:75], v[74:75], v[126:127] op_sel:[0,1] op_sel_hi:[1,1]\n"
    "v_pk_mul_f32 v[76:77], v[76:77], v[126:127] op_sel:[0,1] op_sel_hi:[1,1]\n"
    "v_pk_mul_f32 v[78:79], v[78:79], v[126:127] op_sel:[0,1] op_sel_hi:[1,1]\n"
    "v_cvt_pk_bf16_f32 v40, v64, v65\n"
    "v_cvt_pk_bf16_f32 v41, v66, v67\n"
    "v_cvt_pk_bf16_f32 v42, v68, v69\n"
    "v_cvt_pk_bf16_f32 v43, v70, v71\n"
    "v_cvt_pk_bf16_f32 v44, v72, v73\n"
    "v_cvt_pk_bf16_f32 v45, v74, v75\n"
    "v_cvt_pk_bf16_f32 v46, v76, v77\n"
    "v_cvt_pk_bf16_f32 v47, v78, v79\n"
    "s_mov_b32 exec_lo, 0\n"
    "v_mov_b32 v46, s63\n"
    "v_mov_b32 v47, s64\n"
    "s_mov_b32 exec_lo, -1\n"
    "buffer_store_dwordx4 v[40:43], v1, s[12:15], s61 offen sc1\n"
    "buffer_store_dwordx4 v[44:47], v1, s[12:15], s61 offen offset:1024 sc1\n"
    "s_branch L_joinKV\n"
    "L_roleV:\n"
    "ds_write_b64 v121, v[32:33] offset:2304\n"
    "ds_write_b64 v121, v[34:35] offset:2320\n"
    "ds_write_b64 v121, v[36:37] offset:2336\n"
    "ds_write_b64 v121, v[38:39] offset:2352\n"
    "ds_read_b64_tr_b16 v[80:81], v120 offset:2304\n"
    "ds_read_b64_tr_b16 v[82:83], v120 offset:2880\n"
    "ds_read_b64_tr_b16 v[84:85], v120 offset:3456\n"
    "ds_read_b64_tr_b16 v[86:87], v120 offset:4032\n"
    "s_waitcnt lgkmcnt(8)\n"
    "v_pk_fma_f32 v[64:65], v[96:97], v[126:127], v[64:65] op_sel_hi:[1,0,1] neg_lo:[0,1,0] neg_hi:[0,1,0]\n"
    "v_pk_fma_f32 v[66:67], v[98:99], v[126:127], v[66:67] op_sel_hi:[1,0,1] neg_lo:[0,1,0] neg_hi:[0,1,0]\n"
    "v_pk_fma_f32 v[68:69], v[100:101], v[126:127], v[68:69] op_sel_hi:[1,0,1] neg_lo:[0,1,0] neg_hi:[0,1,0]\n"
    "v_pk_fma_f32 v[70:71], v[102:103], v[126:127], v[70:71] op_sel_hi:[1,0,1] neg_lo:[0,1,0] neg_hi:[0,1,0]\n"
    "v_pk_fma_f32 v[72:73], v[104:105], v[126:127], v[72:73] op_sel_hi:[1,0,1] neg_lo:[0,1,0] neg_hi:[0,1,0]\n"
    "v_pk_fma_f32 v[74:75], v[106:107], v[126:127], v[74:75] op_sel_hi:[1,0,1] neg_lo:[0,1,0] neg_hi:[0,1,0]\n"
    "v_pk_fma_f32 v[76:77], v[108:109], v[126:127], v[76:77] op_sel_hi:[1,0,1] neg_lo:[0,1,0] neg_hi:[0,1,0]\n"
    "v_pk_fma_f32 v[78:79], v[110:111], v[126:127], v[78:79] op_sel_hi:[1,0,1] neg_lo:[0,1,0] neg_hi:[0,1,0]\n"
    "s_waitcnt lgkmcnt(0)\n"
    "buffer_store_dwordx4 v[80:83], v1, s[12:15], s62 offen sc1\n"
    "buffer_store_dwordx4 v[84:87], v1, s[12:15], s62 offen offset:1024 sc1\n"
    "v_pk_mul_f32 v[64:65], v[64:65], v[126:127] op_sel:[0,1] op_sel_hi:[1,1]\n"
    "v_pk_mul_f32 v[66:67], v[66:67], v[126:127] op_sel:[0,1] op_sel_hi:[1,1]\n"
    "v_pk_mul_f32 v[68:69], v[68:69], v[126:127] op_sel:[0,1] op_sel_hi:[1,1]\n"
    "v_pk_mul_f32 v[70:71], v[70:71], v[126:127] op_sel:[0,1] op_sel_hi:[1,1]\n"
    "v_pk_mul_f32 v[72:73], v[72:73], v[126:127] op_sel:[0,1] op_sel_hi:[1,1]\n"
    "v_pk_mul_f32 v[74:75], v[74:75], v[126:127] op_sel:[0,1] op_sel_hi:[1,1]\n"
    "v_pk_mul_f32 v[76:77], v[76:77], v[126:127] op_sel:[0,1] op_sel_hi:[1,1]\n"
    "v_pk_mul_f32 v[78:79], v[78:79], v[126:127] op_sel:[0,1] op_sel_hi:[1,1]\n"
    "v_cvt_pk_bf16_f32 v40, v64, v65\n"
    "v_cvt_pk_bf16_f32 v41, v66, v67\n"
    "v_cvt_pk_bf16_f32 v42, v68, v69\n"
    "v_cvt_pk_bf16_f32 v43, v70, v71\n"
    "v_cvt_pk_bf16_f32 v44, v72, v73\n"
    "v_cvt_pk_bf16_f32 v45, v74, v75\n"
    "v_cvt_pk_bf16_f32 v46, v76, v77\n"
    "v_cvt_pk_bf16_f32 v47, v78, v79\n"
    "s_mov_b32 exec_lo, 0\n"
    "v_mov_b32 v46, s63\n"
    "v_mov_b32 v47, s64\n"
    "s_mov_b32 exec_lo, -1\n"
    "ds_write_b64 v121, v[40:41]\n"
    "ds_write_b64 v121, v[42:43] offset:16\n"
    "ds_write_b64 v121, v[44:45] offset:32\n"
    "ds_write_b64 v121, v[46:47] offset:48\n"
    "ds_read_b64_tr_b16 v[64:65], v120\n"
    "ds_read_b64_tr_b16 v[66:67], v120 offset:576\n"
    "ds_read_b64_tr_b16 v[68:69], v120 offset:1152\n"
    "ds_read_b64_tr_b16 v[70:71], v120 offset:1728\n"
    "s_waitcnt lgkmcnt(0)\n"
    "buffer_store_dwordx4 v[64:67], v1, s[12:15], s61 offen sc1\n"
    "buffer_store_dwordx4 v[68:71], v1, s[12:15], s61 offen offset:1024 sc1\n"
    "L_joinKV:\n"
    "s_waitcnt vmcnt(5)\n"
    "s_barrier\n"
    "ds_read_b64_tr_b16 v[64:65], v6\n"
    "ds_read_b64_tr_b16 v[66:67], v6 offset:256\n"
    "ds_read_b64_tr_b16 v[68:69], v6 offset:4096\n"
    "ds_read_b64_tr_b16 v[70:71], v6 offset:4352\n"
    "ds_read_b64_tr_b16 v[72:73], v6 offset:8192\n"
    "ds_read_b64_tr_b16 v[74:75], v6 offset:8448\n"
    "ds_read_b64_tr_b16 v[76:77], v6 offset:12288\n"
    "ds_read_b64_tr_b16 v[78:79], v6 offset:12544\n"
    "ds_read_b64_tr_b16 v[80:81], v6 offset:16384\n"
    "ds_read_b64_tr_b16 v[82:83], v6 offset:16640\n"
    "ds_read_b64_tr_b16 v[84:85], v6 offset:20480\n"
    "ds_read_b64_tr_b16 v[86:87], v6 offset:20736\n"
    "ds_read_b64_tr_b16 v[88:89], v6 offset:24576\n"
    "ds_read_b64_tr_b16 v[90:91], v6 offset:24832\n"
    "v_mov_b64 v[44:45], -1\n"
    "v_mov_b64 v[46:47], -1\n"
    "s_add_u32 s69, s69, 0x1c00\n"
    "v_mov_b32 v38, v13\n"
    "s_sub_u32 s69, s69, 0x1c00\n"
    "s_mul_i32 s60, s85, 0x493\n"
    "s_lshr_b32 s60, s60, 16\n"
    "s_mul_i32 s60, s60, 56\n"
    "s_add_u32 s60, s60, s85\n"
    "s_lshl_b32 s64, s60, 14\n"
    "s_lshr_b32 s60, s46, 1\n"
    "s_lshl_b32 s60, s60, 10\n"
    "s_add_u32 s64, s64, s60\n"
    "v_add_u32 v22, 1, v22\n"
    "v_cmp_eq_u32 vcc, -1, v22\n"
    "s_nop 1\n"
    "v_cndmask_b32 v22, v22, 0, vcc\n"
    "v_xor_b32 v23, 0x9e3779b9, v22\n"
    "v_mul_f32 v127, 0x3e8b97f6, v127\n"
    "s_waitcnt lgkmcnt(12)\n"
    "v_mfma_f32_32x32x16_bf16 v[96:111], v[64:67], v[128:131], 0\n"
    "s_lshl_b32 s60, s44, 2\n"
    "s_add_u32 s60, s60, 0x25000\n"
    "v_add_u32 v117, s60, v1\n"
    "s_waitcnt lgkmcnt(10)\n"
    "v_mfma_f32_32x32x16_bf16 v[96:111], v[68:71], v[144:147], v[96:111]\n"
    "s_lshr_b32 s60, s44, 1\n"
    "s_lshl1_add_u32 s61, s51, s60\n"
    "s_cmp_lt_u32 s61, 7\n"
    "s_cselect_b32 s22, s22, 0\n"
    "s_waitcnt lgkmcnt(8)\n"
    "v_mfma_f32_32x32x16_bf16 v[96:111], v[72:75], v[160:163], v[96:111]\n"
    "s_lshl_b32 s61, s61, 7\n"
    "s_and_b32 s62, s44, 1\n"
    "s_mul_i32 s62, s62, 0x31000\n"
    "s_waitcnt lgkmcnt(6)\n"
    "v_mfma_f32_32x32x16_bf16 v[96:111], v[76:79], v[176:179], v[96:111]\n"
    "s_add_u32 s61, s61, s62\n"
    "v_mul_u32_u24 v112, 0x380, v124\n"
    "v_add3_u32 v17, v112, v125, s61\n"
    "s_waitcnt lgkmcnt(4)\n"
    "v_mfma_f32_32x32x16_bf16 v[96:111], v[80:83], v[192:195], v[96:111]\n"
    "ds_read_b64_tr_b16 v[24:25], v7\n"
    "ds_read_b64_tr_b16 v[26:27], v7 offset:256\n"
    "v_add_u32 v18, 0x1800, v17\n"
    "v_add_u32 v19, 0x1800, v18\n"
    "v_add_u32 v10, 0x1800, v19\n"
    "s_waitcnt lgkmcnt(4)\n"
    "v_mfma_f32_32x32x16_bf16 v[96:111], v[84:87], v[208:211], v[96:111]\n"
    "ds_read_b64_tr_b16 v[28:29], v8\n"
    "ds_read_b64_tr_b16 v[30:31], v8 offset:256\n"
    "s_lshr_b32 s1, s47, 2\n"
    "s_cmp_eq_u32 s1, 7\n"
    "s_cselect_b32 s1, 0, s1\n"
    "s_mul_i32 s66, s1, 0x7000\n"
    "s_waitcnt lgkmcnt(4)\n"
    "v_mfma_f32_32x32x16_bf16 v[96:111], v[88:91], v[224:227], v[96:111]\n"
    "ds_read_b64_tr_b16 v[48:49], v9\n"
    "ds_read_b64_tr_b16 v[50:51], v9 offset:256\n"
    "v_mfma_f32_4x4x4_16b_bf16 v[56:59], v[20:21], v[64:65], 0\n"
    "s_lshl_b32 s1, s1, 12\n"
    "v_mfma_f32_4x4x4_16b_bf16 v[92:95], v[20:21], v[66:67], 0\n"
    "s_lshl4_add_u32 s65, s48, s49\n"
    "v_mfma_f32_4x4x4_16b_bf16 v[56:59], v[20:21], v[68:69], v[56:59]\n"
    "s_lshl2_add_u32 s65, s65, s44\n"
    "v_mfma_f32_4x4x4_16b_bf16 v[92:95], v[20:21], v[70:71], v[92:95]\n"
    "s_lshl_b32 s65, s65, 7\n"
    "v_mfma_f32_4x4x4_16b_bf16 v[56:59], v[20:21], v[72:73], v[56:59]\n"
    "s_add_u32 s65, s65, 0x1000\n"
    "v_mfma_f32_4x4x4_16b_bf16 v[92:95], v[20:21], v[74:75], v[92:95]\n"
    "v_and_b32 v11, 31, v0\n"
    "v_mfma_f32_4x4x4_16b_bf16 v[56:59], v[20:21], v[76:77], v[56:59]\n"
    "s_and_b32 s61, s49, 9\n"
    "v_mfma_f32_4x4x4_16b_bf16 v[92:95], v[20:21], v[78:79], v[92:95]\n"
    "v_xor_b32 v11, s61, v11\n"
    "v_mfma_f32_4x4x4_16b_bf16 v[56:59], v[20:21], v[80:81], v[56:59]\n"
    "s_and_b32 s60, s48, 14\n"
    "v_mfma_f32_4x4x4_16b_bf16 v[92:95], v[20:21], v[82:83], v[92:95]\n"
    "s_lshl_b32 s60, s60, 6\n"
    "v_mfma_f32_4x4x4_16b_bf16 v[56:59], v[20:21], v[84:85], v[56:59]\n"
    "s_add_u32 s60, s60, s44\n"
    "v_mfma_f32_4x4x4_16b_bf16 v[92:95], v[20:21], v[86:87], v[92:95]\n"
    "s_lshl_b32 s60, s60, 7\n"
    "v_mfma_f32_4x4x4_16b_bf16 v[56:59], v[20:21], v[88:89], v[56:59]\n"
    "s_add_u32 s60, s60, 0x1000\n"
    "v_mfma_f32_4x4x4_16b_bf16 v[92:95], v[20:21], v[90:91], v[92:95]\n"
    "v_lshl_add_u32 v11, v11, 9, s60\n"
    "s_waitcnt lgkmcnt(4)\n"
    "v_mfma_f32_32x32x16_bf16 v[96:111], v[24:27], v[132:135], v[96:111]\n"
    "ds_read_b64_tr_b16 v[52:53], v7 offset:4096\n"
    "ds_read_b64_tr_b16 v[54:55], v7 offset:4352\n"
    "s_lshl4_add_u32 s60, s48, s61\n"
    "s_lshl2_add_u32 s60, s60, s44\n"
    "s_lshl_b32 s60, s60, 12\n"
    "s_add_u32 m0, s68, s1\n"
    "s_nop 0\n"
    "buffer_load_dwordx4 v17, s[20:23], s66 offen lds\n"
    "s_waitcnt lgkmcnt(4)\n"
    "v_mfma_f32_32x32x16_bf16 v[96:111], v[28:31], v[136:139], v[96:111]\n"
    "ds_read_b64_tr_b16 v[24:25], v8 offset:4096\n"
    "ds_read_b64_tr_b16 v[26:27], v8 offset:4352\n"
    "v_add_f32 v56, v56, v92\n"
    "v_mov_b32 v57, v56\n"
    "s_waitcnt lgkmcnt(4)\n"
    "v_mfma_f32_32x32x16_bf16 v[96:111], v[48:51], v[140:143], v[96:111]\n"
    "ds_read_b64_tr_b16 v[28:29], v9 offset:4096\n"
    "ds_read_b64_tr_b16 v[30:31], v9 offset:4352\n"
    "v_permlane32_swap_b32 v56, v57\n"
    "v_add_f32 v56, v56, v57\n"
    "ds_write_b32 v117, v56\n"
    "buffer_load_dwordx4 v18, s[20:23], s66 offen offset:1024 lds\n"
    "s_waitcnt lgkmcnt(5)\n"
    "v_mfma_f32_32x32x16_bf16 v[96:111], v[52:55], v[148:151], v[96:111]\n"
    "ds_read_b64_tr_b16 v[48:49], v7 offset:8192\n"
    "ds_read_b64_tr_b16 v[50:51], v7 offset:8448\n"
    "s_add_u32 s70, s60, 0x100000\n"
    "s_xor_b32 s71, s70, 0x4000\n"
    "s_mul_i32 s60, s44, 0xe0000\n"
    "s_waitcnt lgkmcnt(5)\n"
    "v_mfma_f32_32x32x16_bf16 v[96:111], v[24:27], v[152:155], v[96:111]\n"
    "ds_read_b64_tr_b16 v[52:53], v8 offset:8192\n"
    "ds_read_b64_tr_b16 v[54:55], v8 offset:8448\n"
    "s_waitcnt lgkmcnt(4)\n"
    "s_barrier\n"
    "ds_read_b128 v[56:59], v116\n"
    "buffer_load_dwordx4 v19, s[20:23], s66 offen offset:2048 lds\n"
    "v_mfma_f32_32x32x16_bf16 v[96:111], v[28:31], v[156:159], v[96:111]\n"
    "ds_read_b64_tr_b16 v[24:25], v9 offset:8192\n"
    "ds_read_b64_tr_b16 v[26:27], v9 offset:8448\n"
    "s_lshl_b32 s61, s54, 7\n"
    "s_add_u32 s60, s60, s61\n"
    "s_cmp_lt_u32 s56, 7\n"
    "s_cselect_b32 s60, s60, 0x70000000\n"
    "s_waitcnt lgkmcnt(5)\n"
    "v_mfma_f32_32x32x16_bf16 v[96:111], v[48:51], v[164:167], v[96:111]\n"
    "ds_read_b64_tr_b16 v[28:29], v7 offset:12288\n"
    "ds_read_b64_tr_b16 v[30:31], v7 offset:12544\n"
    "v_mul_u32_u24 v112, 0xe00, v124\n"
    "v_add3_u32 v13, v112, v125, s60\n"
    "v_add_u32 v14, 0x6c00, v13\n"
    "s_waitcnt vmcnt(3)\n"
    "s_mov_b64 exec, 1\n"
    "buffer_store_dwordx2 v[22:23], off, s[12:15], s65 sc1\n"
    "s_mov_b64 exec, -1\n"
    "s_lshl2_add_u32 s0, s49, s44\n"
    "s_lshl4_add_u32 s0, s0, s48\n"
    "s_lshl_b32 s0, s0, 11\n"
    "s_add_u32 s0, s0, 0x500000\n"
    "s_lshl_b32 s34, s52, 6\n"
    "s_add_u32 s34, s34, s50\n"
    "s_lshl_b32 s34, s34, 11\n"
    "s_add_u32 s34, s34, 0x700000\n"
    "s_cmp_lt_u32 s52, 14\n"
    "s_cselect_b32 s34, s34, 0x70000000\n"
    "buffer_store_dwordx4 v[44:47], v1, s[12:15], s0 offen sc1\n"
    "buffer_store_dwordx4 v[44:47], v1, s[12:15], s0 offen offset:1024 sc1\n"
    "buffer_store_dwordx4 v[44:47], v1, s[12:15], s34 offen sc1\n"
    "buffer_store_dwordx4 v[44:47], v1, s[12:15], s34 offen offset:1024 sc1\n"
    "s_mov_b32 s34, 0x1000\n"
    "s_waitcnt lgkmcnt(5)\n"
    "v_mfma_f32_32x32x16_bf16 v[96:111], v[52:55], v[168:171], v[96:111]\n"
    "ds_read_b64_tr_b16 v[48:49], v8 offset:12288\n"
    "ds_read_b64_tr_b16 v[50:51], v8 offset:12544\n"
    "s_waitcnt lgkmcnt(6)\n"
    "v_pk_add_f32 v[56:57], v[56:57], v[58:59]\n"
    "v_add_f32 v56, v56, v57\n"
    "ds_write_b32 v12, v56\n"
    "s_waitcnt lgkmcnt(5)\n"
    "v_mfma_f32_32x32x16_bf16 v[96:111], v[24:27], v[172:175], v[96:111]\n"
    "ds_read_b64_tr_b16 v[52:53], v9 offset:12288\n"
    "ds_read_b64_tr_b16 v[54:55], v9 offset:12544\n"
    "ds_read_b128 v[240:243], v119\n"
    "ds_read_b128 v[244:247], v119 offset:32\n"
    "ds_read_b128 v[248:251], v119 offset:64\n"
    "ds_read_b128 v[252:255], v119 offset:96\n"
    "buffer_load_dwordx4 v10, s[20:23], s66 offen offset:3072 lds\n"
    "s_add_u32 s1, s1, 0x1000\n"
    "s_add_u32 s66, s66, 0x7000\n"
    "s_cmp_eq_u32 s1, 0x7000\n"
    "s_cselect_b32 s1, 0, s1\n"
    "s_cselect_b32 s66, 0, s66\n"
    "s_waitcnt lgkmcnt(9)\n"
    "v_mfma_f32_32x32x16_bf16 v[96:111], v[28:31], v[180:183], v[96:111]\n"
    "ds_read_b64_tr_b16 v[24:25], v7 offset:16384\n"
    "ds_read_b64_tr_b16 v[26:27], v7 offset:16640\n"
    "v_add_u32 v15, 0x6c00, v14\n"
    "v_add_u32 v16, 0x6c00, v15\n"
    "v_mov_b32 v60, v122\n"
    "s_waitcnt lgkmcnt(9)\n"
    "v_mfma_f32_32x32x16_bf16 v[96:111], v[48:51], v[184:187], v[96:111]\n"
    "ds_read_b64_tr_b16 v[28:29], v8 offset:16384\n"
    "ds_read_b64_tr_b16 v[30:31], v8 offset:16640\n"
    "v_mov_b32 v61, v123\n"
    "v_mov_b32 v62, v124\n"
    "v_mov_b32 v63, v125\n"
    "s_add_u32 m0, s68, s1\n"
    "s_nop 0\n"
    "buffer_load_dwordx4 v17, s[20:23], s66 offen lds\n"
    "s_waitcnt lgkmcnt(8)\n"
    "v_mfma_f32_32x32x16_bf16 v[96:111], v[52:55], v[188:191], v[96:111]\n"
    "ds_read_b64_tr_b16 v[48:49], v9 offset:16384\n"
    "ds_read_b64_tr_b16 v[50:51], v9 offset:16640\n"
    "s_mul_i32 s67, s44, 0x3000\n"
    "s_add_u32 s67, s67, 0x1c000\n"
    "s_xor_b32 s60, s44, 0\n"
    "s_waitcnt lgkmcnt(4)\n"
    "v_mfma_f32_32x32x16_bf16 v[96:111], v[24:27], v[196:199], v[96:111]\n"
    "ds_read_b64_tr_b16 v[52:53], v7 offset:20480\n"
    "ds_read_b64_tr_b16 v[54:55], v7 offset:20736\n"
    "s_lshl_b32 s60, s60, 11\n"
    "s_add_u32 s60, s60, 0x1c000\n"
    "v_add_u32 v113, s60, v1\n"
    "buffer_load_dwordx4 v18, s[20:23], s66 offen offset:1024 lds\n"
    "s_waitcnt lgkmcnt(4)\n"
    "v_mfma_f32_32x32x16_bf16 v[96:111], v[28:31], v[200:203], v[96:111]\n"
    "ds_read_b64_tr_b16 v[24:25], v8 offset:20480\n"
    "ds_read_b64_tr_b16 v[26:27], v8 offset:20736\n"
    "s_xor_b32 s60, s44, 1\n"
    "s_lshl_b32 s60, s60, 11\n"
    "s_add_u32 s60, s60, 0x1c000\n"
    "s_waitcnt lgkmcnt(4)\n"
    "v_mfma_f32_32x32x16_bf16 v[96:111], v[48:51], v[204:207], v[96:111]\n"
    "ds_read_b64_tr_b16 v[28:29], v9 offset:20480\n"
    "ds_read_b64_tr_b16 v[30:31], v9 offset:20736\n"
    "v_add_u32 v114, s60, v1\n"
    "s_xor_b32 s60, s44, 2\n"
    "s_lshl_b32 s60, s60, 11\n"
    "buffer_load_dwordx4 v19, s[20:23], s66 offen offset:2048 lds\n"
    "s_waitcnt lgkmcnt(4)\n"
    "v_mfma_f32_32x32x16_bf16 v[96:111], v[52:55], v[212:215], v[96:111]\n"
    "ds_read_b64_tr_b16 v[48:49], v7 offset:24576\n"
    "ds_read_b64_tr_b16 v[50:51], v7 offset:24832\n"
    "s_add_u32 s60, s60, 0x1c000\n"
    "v_add_u32 v115, s60, v1\n"
    "s_xor_b32 s60, s44, 3\n"
    "s_waitcnt lgkmcnt(4)\n"
    "v_mfma_f32_32x32x16_bf16 v[96:111], v[24:27], v[216:219], v[96:111]\n"
    "ds_read_b64_tr_b16 v[52:53], v8 offset:24576\n"
    "ds_read_b64_tr_b16 v[54:55], v8 offset:24832\n"
    "s_lshl_b32 s60, s60, 11\n"
    "s_add_u32 s60, s60, 0x1c000\n"
    "v_add_u32 v118, s60, v1\n"
    "buffer_load_dwordx4 v10, s[20:23], s66 offen offset:3072 lds\n"
    "s_add_u32 s1, s1, 0x1000\n"
    "s_add_u32 s66, s66, 0x7000\n"
    "s_cmp_eq_u32 s1, 0x7000\n"
    "s_cselect_b32 s1, 0, s1\n"
    "s_cselect_b32 s66, 0, s66\n"
    "s_waitcnt lgkmcnt(4)\n"
    "v_mfma_f32_32x32x16_bf16 v[96:111], v[28:31], v[220:223], v[96:111]\n"
    "ds_read_b64_tr_b16 v[24:25], v9 offset:24576\n"
    "ds_read_b64_tr_b16 v[26:27], v9 offset:24832\n"
    "v_add_u32 v20, s67, v4\n"
    "v_add_u32 v21, s67, v5\n"
    "s_waitcnt lgkmcnt(4)\n"
    "v_mfma_f32_32x32x16_bf16 v[96:111], v[48:51], v[228:231], v[96:111]\n"
    "s_add_u32 m0, s68, s1\n"
    "s_nop 0\n"
    "buffer_load_dwordx4 v17, s[20:23], s66 offen lds\n"
    "s_waitcnt lgkmcnt(2)\n"
    "v_mfma_f32_32x32x16_bf16 v[96:111], v[52:55], v[232:235], v[96:111]\n"
    "s_waitcnt lgkmcnt(0)\n"
    "v_mfma_f32_32x32x16_bf16 v[96:111], v[24:27], v[236:239], v[96:111]\n"
    "buffer_load_dwordx4 v18, s[20:23], s66 offen offset:1024 lds\n"
    "s_mov_b32 exec_lo, 0x70007\n"
    "s_mov_b32 exec_hi, 0\n"
    "buffer_load_dwordx2 v[32:33], v11, s[12:15], 0 offen sc1\n"
    "s_mov_b64 exec, -1\n"
    "buffer_load_dwordx4 v19, s[20:23], s66 offen offset:2048 lds\n"
    "buffer_load_dwordx4 v10, s[20:23], s66 offen offset:3072 lds\n"
    "s_add_u32 s1, s1, 0x1000\n"
    "s_add_u32 s66, s66, 0x7000\n"
    "s_cmp_eq_u32 s1, 0x7000\n"
    "s_cselect_b32 s1, 0, s1\n"
    "s_cselect_b32 s66, 0, s66\n"
    "v_pk_fma_f32 v[96:97], v[240:241], v[126:127], v[96:97] op_sel_hi:[1,0,1] neg_lo:[0,1,0] neg_hi:[0,1,0]\n"
    "v_pk_fma_f32 v[98:99], v[242:243], v[126:127], v[98:99] op_sel_hi:[1,0,1] neg_lo:[0,1,0] neg_hi:[0,1,0]\n"
    "v_pk_fma_f32 v[100:101], v[244:245], v[126:127], v[100:101] op_sel_hi:[1,0,1] neg_lo:[0,1,0] neg_hi:[0,1,0]\n"
    "v_pk_fma_f32 v[102:103], v[246:247], v[126:127], v[102:103] op_sel_hi:[1,0,1] neg_lo:[0,1,0] neg_hi:[0,1,0]\n"
    "v_pk_fma_f32 v[104:105], v[248:249], v[126:127], v[104:105] op_sel_hi:[1,0,1] neg_lo:[0,1,0] neg_hi:[0,1,0]\n"
    "v_pk_fma_f32 v[106:107], v[250:251], v[126:127], v[106:107] op_sel_hi:[1,0,1] neg_lo:[0,1,0] neg_hi:[0,1,0]\n"
    "v_pk_fma_f32 v[108:109], v[252:253], v[126:127], v[108:109] op_sel_hi:[1,0,1] neg_lo:[0,1,0] neg_hi:[0,1,0]\n"
    "v_pk_fma_f32 v[110:111], v[254:255], v[126:127], v[110:111] op_sel_hi:[1,0,1] neg_lo:[0,1,0] neg_hi:[0,1,0]\n"
    "s_add_u32 m0, s68, s1\n"
    "s_nop 0\n"
    "buffer_load_dwordx4 v17, s[20:23], s66 offen lds\n"
    "buffer_load_dwordx4 v18, s[20:23], s66 offen offset:1024 lds\n"
    "v_pk_mul_f32 v[96:97], v[96:97], v[126:127] op_sel:[0,1] op_sel_hi:[1,1]\n"
    "v_pk_mul_f32 v[98:99], v[98:99], v[126:127] op_sel:[0,1] op_sel_hi:[1,1]\n"
    "v_pk_mul_f32 v[100:101], v[100:101], v[126:127] op_sel:[0,1] op_sel_hi:[1,1]\n"
    "v_pk_mul_f32 v[102:103], v[102:103], v[126:127] op_sel:[0,1] op_sel_hi:[1,1]\n"
    "v_pk_mul_f32 v[104:105], v[104:105], v[126:127] op_sel:[0,1] op_sel_hi:[1,1]\n"
    "v_pk_mul_f32 v[106:107], v[106:107], v[126:127] op_sel:[0,1] op_sel_hi:[1,1]\n"
    "v_pk_mul_f32 v[108:109], v[108:109], v[126:127] op_sel:[0,1] op_sel_hi:[1,1]\n"
    "v_pk_mul_f32 v[110:111], v[110:111], v[126:127] op_sel:[0,1] op_sel_hi:[1,1]\n"
    "v_cvt_pk_bf16_f32 v128, v96, v97\n"
    "v_cvt_pk_bf16_f32 v129, v98, v99\n"
    "v_cvt_pk_bf16_f32 v130, v100, v101\n"
    "v_cvt_pk_bf16_f32 v131, v102, v103\n"
    "v_cvt_pk_bf16_f32 v132, v104, v105\n"
    "v_cvt_pk_bf16_f32 v133, v106, v107\n"
    "v_cvt_pk_bf16_f32 v134, v108, v109\n"
    "v_cvt_pk_bf16_f32 v135, v110, v111\n"
    "s_mov_b32 exec_lo, 0\n"
    "v_mov_b32 v134, 0\n"
    "v_mov_b32 v135, 0\n"
    "s_mov_b32 exec_lo, -1\n"
    "buffer_load_dwordx4 v19, s[20:23], s66 offen offset:2048 lds\n"
    "buffer_load_dwordx4 v10, s[20:23], s66 offen offset:3072 lds\n"
    "s_add_u32 s1, s1, 0x1000\n"
    "s_add_u32 s66, s66, 0x7000\n"
    "s_cmp_eq_u32 s1, 0x7000\n"
    "s_cselect_b32 s1, 0, s1\n"
    "s_cselect_b32 s66, 0, s66\n"
    "ds_write_b128 v113, v[128:131]\n"
    "ds_write_b128 v113, v[132:135] offset:1024\n"
    "s_waitcnt lgkmcnt(0)\n"
    "s_barrier\n"
    "ds_read_b128 v[136:139], v114\n"
    "ds_read_b128 v[140:143], v114 offset:1024\n"
    "ds_read_b128 v[144:147], v115\n"
    "ds_read_b128 v[148:151], v115 offset:1024\n"
    "ds_read_b128 v[152:155], v118\n"
    "ds_read_b128 v[156:159], v118 offset:1024\n"
    "s_mov_b32 exec_lo, 0x70007\n"
    "s_mov_b32 exec_hi, 0\n"
    "s_mov_b32 s60, 0x100000\n"
    "s_waitcnt vmcnt(6)\n"
    "s_branch Lchk_3\n"
    "Lpoll_1:\n"
    "buffer_load_dwordx2 v[32:33], v11, s[12:15], 0 offen sc1\n"
    "s_waitcnt vmcnt(0)\n"
    "Lchk_3:\n"
    "v_cmp_ne_u64 vcc, v[32:33], v[22:23]\n"
    "s_nop 0\n"
    "s_cbranch_vccz Ldone_2\n"
    "s_sub_u32 s60, s60, 1\n"
    "s_sleep 1\n"
    "s_cbranch_scc0 Lpoll_1\n"
    "Ldone_2:\n"
    "s_mov_b64 exec, -1\n"
    "buffer_load_dwordx4 v[192:195], v1, s[12:15], s70 offen sc1\n"
    "buffer_load_dwordx4 v[196:199], v1, s[12:15], s70 offen offset:1024 sc1\n"
    "buffer_load_dwordx4 v[200:203], v1, s[12:15], s70 offen offset:2048 sc1\n"
    "buffer_load_dwordx4 v[204:207], v1, s[12:15], s70 offen offset:3072 sc1\n"
    "s_mov_b32 exec_lo, 0xff00ff\n"
    "s_mov_b32 exec_hi, 0\n"
    "buffer_load_dwordx2 v[32:33], v11, s[12:15], 0 offen sc1\n"
    "s_mov_b64 exec, -1\n"
    "buffer_load_dwordx4 v[208:211], v1, s[12:15], s71 offen sc1\n"
    "buffer_load_dwordx4 v[212:215], v1, s[12:15], s71 offen offset:1024 sc1\n"
    "buffer_load_dwordx4 v[216:219], v1, s[12:15], s71 offen offset:2048 sc1\n"
    "buffer_load_dwordx4 v[220:223], v1, s[12:15], s71 offen offset:3072 sc1\n"
    "s_xor_b32 s72, s70, 0x8000\n"
    "s_xor_b32 s73, s70, 0xc000\n"
    "s_xor_b32 s74, s70, 0x10000\n"
    "s_xor_b32 s75, s70, 0x14000\n"
    "s_xor_b32 s76, s70, 0x18000\n"
    "s_xor_b32 s77, s70, 0x1c000\n"
    "s_xor_b32 s78, s70, 0x20000\n"
    "s_xor_b32 s79, s70, 0x24000\n"
    "s_xor_b32 s80, s70, 0x28000\n"
    "s_xor_b32 s81, s70, 0x2c000\n"
    "s_xor_b32 s82, s70, 0x30000\n"
    "s_xor_b32 s83, s70, 0x34000\n"
    "s_xor_b32 s84, s70, 0x38000\n"
    "s_xor_b32 s85, s70, 0x3c000\n"
    "s_xor_b32 s86, s2, 0\n"
    "s_mul_i32 s86, s86, 0x1c000\n"
    "s_xor_b32 s87, s2, 1\n"
    "s_mul_i32 s87, s87, 0x1c000\n"
    "s_xor_b32 s88, s2, 2\n"
    "s_mul_i32 s88, s88, 0x1c000\n"
    "s_xor_b32 s89, s2, 3\n"
    "s_mul_i32 s89, s89, 0x1c000\n"
    "s_xor_b32 s90, s2, 4\n"
    "s_mul_i32 s90, s90, 0x1c000\n"
    "s_xor_b32 s91, s2, 5\n"
    "s_mul_i32 s91, s91, 0x1c000\n"
    "s_xor_b32 s92, s2, 6\n"
    "s_mul_i32 s92, s92, 0x1c000\n"
    "s_xor_b32 s93, s2, 7\n"
    "s_mul_i32 s93, s93, 0x1c000\n"
    "s_xor_b32 s94, s2, 32\n"
    "s_mul_i32 s94, s94, 0x1c000\n"
    "s_xor_b32 s95, s2, 33\n"
    "s_mul_i32 s95, s95, 0x1c000\n"
    "s_xor_b32 s96, s2, 34\n"
    "s_mul_i32 s96, s96, 0x1c000\n"
    "s_xor_b32 s97, s2, 35\n"
    "s_mul_i32 s97, s97, 0x1c000\n"
    "s_xor_b32 s98, s2, 36\n"
    "s_mul_i32 s98, s98, 0x1c000\n"
    "s_xor_b32 s99, s2, 37\n"
    "s_mul_i32 s99, s99, 0x1c000\n"
    "s_xor_b32 s100, s2, 38\n"
    "s_mul_i32 s100, s100, 0x1c000\n"
    "s_xor_b32 s101, s2, 39\n"
    "s_mul_i32 s101, s101, 0x1c000\n"
    "s_xor_b32 s60, s44, 1\n"
    "s_mul_i32 s60, s60, 0x3000\n"
    "s_add_u32 s60, s60, 0x1c000\n"
    "v_add_u32 v6, s60, v1\n"
    "s_xor_b32 s60, s44, 2\n"
    "s_mul_i32 s60, s60, 0x3000\n"
    "s_add_u32 s60, s60, 0x1d000\n"
    "v_add_u32 v7, s60, v1\n"
    "s_xor_b32 s60, s44, 3\n"
    "s_mul_i32 s60, s60, 0x3000\n"
    "s_add_u32 s60, s60, 0x1e000\n"
    "v_add_u32 v8, s60, v1\n"
    "s_mul_i32 s60, s44, 0x3000\n"
    "s_add_u32 s60, s60, 0x1c000\n"
    "v_add_u32 v9, s60, v1\n"
    "v_min_u32 v12, 31, v0\n"
    "v_and_b32 v112, 7, v12\n"
    "v_lshrrev_b32 v12, 3, v12\n"
    "v_lshlrev_b32 v12, 12, v12\n"
    "v_lshl_add_u32 v12, v112, 7, v12\n"
    "s_add_u32 s65, s65, 0x20000\n"
    "v_and_b32 v41, 15, v0\n"
    "s_lshl_b32 s60, s50, 7\n"
    "s_add_u32 s60, s60, 0x21000\n"
    "v_lshl_add_u32 v112, v41, 13, s60\n"
    "s_lshl_b32 s60, s48, 6\n"
    "s_add_u32 s60, s60, s44\n"
    "s_lshl_b32 s60, s60, 7\n"
    "s_add_u32 s60, s60, 0x21000\n"
    "v_lshl_add_u32 v41, v41, 9, s60\n"
    "s_mov_b32 exec_lo, 0xffff\n"
    "s_mov_b32 exec_hi, 0xffff\n"
    "v_mov_b32 v41, v112\n"
    "s_mov_b64 exec, -1\n"
    "s_waitcnt lgkmcnt(0)\n"
    "s_barrier\n"
    "s_waitcnt vmcnt(5)\n"
    "v_mfma_f32_32x32x16_bf16 v[160:175], v[192:195], v[136:139], 0\n"
    "v_mfma_f32_32x32x16_bf16 v[160:175], v[196:199], v[140:143], v[160:175]\n"
    "s_waitcnt vmcnt(4)\n"
    "v_cmp_ne_u64 vcc, v[32:33], v[22:23]\n"
    "s_nop 0\n"
    "s_and_b32 vcc_lo, vcc_lo, 0xff00ff\n"
    "s_cbranch_scc1 L_f1\n"
    "L_f1_back:\n"
    "v_mfma_f32_32x32x16_bf16 v[176:191], v[192:195], v[144:147], 0\n"
    "buffer_load_dwordx4 v[224:227], v1, s[12:15], s72 offen sc1\n"
    "s_nop 4\n"
    "v_exp_f32 v160, v160\n"
    "v_exp_f32 v161, v161\n"
    "v_exp_f32 v162, v162\n"
    "v_cvt_pk_bf16_f32 v24, v160, v161\n"
    "v_exp_f32 v163, v163\n"
    "buffer_load_dwordx4 v[228:231], v1, s[12:15], s72 offen offset:1024 sc1\n"
    "v_exp_f32 v164, v164\n"
    "v_exp_f32 v165, v165\n"
    "v_cvt_pk_bf16_f32 v25, v162, v163\n"
    "v_exp_f32 v166, v166\n"
    "v_exp_f32 v167, v167\n"
    "v_cvt_pk_bf16_f32 v26, v164, v165\n"
    "v_mfma_f32_32x32x16_bf16 v[176:191], v[196:199], v[148:151], v[176:191]\n"
    "buffer_load_dwordx4 v[232:235], v1, s[12:15], s72 offen offset:2048 sc1\n"
    "v_exp_f32 v168, v168\n"
    "v_exp_f32 v169, v169\n"
    "v_cvt_pk_bf16_f32 v27, v166, v167\n"
    "v_exp_f32 v170, v170\n"
    "v_exp_f32 v171, v171\n"
    "v_cvt_pk_bf16_f32 v28, v168, v169\n"
    "v_mfma_f32_32x32x16_bf16 v[80:95], v[200:203], v[24:27], 0\n"
    "buffer_load_dwordx4 v[236:239], v1, s[12:15], s72 offen offset:3072 sc1\n"
    "buffer_load_dword v39, v38, s[16:19], s69 offen\n"
    "s_add_u32 s69, s69, 0x1c00\n"
    "buffer_load_dword v40, v38, s[16:19], s69 offen\n"
    "s_add_u32 s69, s69, 0x37e400\n"
    "v_exp_f32 v172, v172\n"
    "v_exp_f32 v173, v173\n"
    "v_cvt_pk_bf16_f32 v29, v170, v171\n"
    "v_exp_f32 v174, v174\n"
    "v_exp_f32 v175, v175\n"
    "v_cvt_pk_bf16_f32 v30, v172, v173\n"
    "v_cvt_pk_bf16_f32 v31, v174, v175\n"
    "v_mfma_f32_32x32x16_bf16 v[160:175], v[192:195], v[152:155], 0\n"
    "buffer_load_dwordx4 v[244:247], v1, s[12:15], s73 offen offset:1024 sc1\n"
    "v_exp_f32 v176, v176\n"
    "v_exp_f32 v177, v177\n"
    "v_exp_f32 v178, v178\n"
    "v_cvt_pk_bf16_f32 v24, v176, v177\n"
    "v_exp_f32 v179, v179\n"
    "v_mfma_f32_32x32x16_bf16 v[80:95], v[204:207], v[28:31], v[80:95]\n"
    "v_exp_f32 v180, v180\n"
    "v_exp_f32 v181, v181\n"
    "v_cvt_pk_bf16_f32 v25, v178, v179\n"
    "v_exp_f32 v182, v182\n"
    "v_exp_f32 v183, v183\n"
    "v_cvt_pk_bf16_f32 v26, v180, v181\n"
    "v_mfma_f32_32x32x16_bf16 v[160:175], v[196:199], v[156:159], v[160:175]\n"
    "buffer_load_dwordx4 v[240:243], v1, s[12:15], s73 offen sc1\n"
    "v_exp_f32 v184, v184\n"
    "v_exp_f32 v185, v185\n"
    "v_cvt_pk_bf16_f32 v27, v182, v183\n"
    "v_exp_f32 v186, v186\n"
    "v_exp_f32 v187, v187\n"
    "v_cvt_pk_bf16_f32 v28, v184, v185\n"
    "v_mfma_f32_32x32x16_bf16 v[96:111], v[200:203], v[24:27], 0\n"
    "v_exp_f32 v188, v188\n"
    "v_exp_f32 v189, v189\n"
    "v_cvt_pk_bf16_f32 v29, v186, v187\n"
    "v_exp_f32 v190, v190\n"
    "v_exp_f32 v191, v191\n"
    "v_cvt_pk_bf16_f32 v30, v188, v189\n"
    "v_cvt_pk_bf16_f32 v31, v190, v191\n"
    "v_mfma_f32_32x32x16_bf16 v[176:191], v[192:195], v[128:131], 0\n"
    "buffer_load_dwordx4 v[248:251], v1, s[12:15], s73 offen offset:2048 sc1\n"
    "v_exp_f32 v160, v160\n"
    "v_exp_f32 v161, v161\n"
    "v_exp_f32 v162, v162\n"
    "v_cvt_pk_bf16_f32 v24, v160, v161\n"
    "v_exp_f32 v163, v163\n"
    "v_mfma_f32_32x32x16_bf16 v[96:111], v[204:207], v[28:31], v[96:111]\n"
    "v_exp_f32 v164, v164\n"
    "v_exp_f32 v165, v165\n"
    "v_cvt_pk_bf16_f32 v25, v162, v163\n"
    "v_exp_f32 v166, v166\n"
    "v_exp_f32 v167, v167\n"
    "v_cvt_pk_bf16_f32 v26, v164, v165\n"
    "v_mfma_f32_32x32x16_bf16 v[176:191], v[196:199], v[132:135], v[176:191]\n"
    "v_exp_f32 v168, v168\n"
    "v_exp_f32 v169, v169\n"
    "v_cvt_pk_bf16_f32 v27, v166, v167\n"
    "v_exp_f32 v170, v170\n"
    "v_exp_f32 v171, v171\n"
    "v_cvt_pk_bf16_f32 v28, v168, v169\n"
    "v_mfma_f32_32x32x16_bf16 v[112:127], v[200:203], v[24:27], 0\n"
    "s_waitcnt vmcnt(9)\n"
    "v_exp_f32 v172, v172\n"
    "v_exp_f32 v173, v173\n"
    "v_cvt_pk_bf16_f32 v29, v170, v171\n"
    "v_exp_f32 v174, v174\n"
    "v_exp_f32 v175, v175\n"
    "v_cvt_pk_bf16_f32 v30, v172, v173\n"
    "v_cvt_pk_bf16_f32 v31, v174, v175\n"
    "v_mfma_f32_32x32x16_bf16 v[160:175], v[208:211], v[136:139], 0\n"
    "buffer_load_dwordx4 v[252:255], v1, s[12:15], s73 offen offset:3072 sc1\n"
    "v_exp_f32 v176, v176\n"
    "v_exp_f32 v177, v177\n"
    "v_exp_f32 v178, v178\n"
    "v_cvt_pk_bf16_f32 v24, v176, v177\n"
    "v_exp_f32 v179, v179\n"
    "v_mfma_f32_32x32x16_bf16 v[112:127], v[204:207], v[28:31], v[112:127]\n"
    "v_exp_f32 v180, v180\n"
    "v_exp_f32 v181, v181\n"
    "v_cvt_pk_bf16_f32 v25, v178, v179\n"
    "v_exp_f32 v182, v182\n"
    "v_exp_f32 v183, v183\n"
    "v_cvt_pk_bf16_f32 v26, v180, v181\n"
    "v_mfma_f32_32x32x16_bf16 v[160:175], v[212:215], v[140:143], v[160:175]\n"
    "v_exp_f32 v184, v184\n"
    "v_exp_f32 v185, v185\n"
    "v_cvt_pk_bf16_f32 v27, v182, v183\n"
    "v_exp_f32 v186, v186\n"
    "v_exp_f32 v187, v187\n"
    "v_cvt_pk_bf16_f32 v28, v184, v185\n"
    "v_mfma_f32_32x32x16_bf16 v[64:79], v[200:203], v[24:27], 0\n"
    "v_exp_f32 v188, v188\n"
    "v_exp_f32 v189, v189\n"
    "v_cvt_pk_bf16_f32 v29, v186, v187\n"
    "v_exp_f32 v190, v190\n"
    "v_exp_f32 v191, v191\n"
    "v_cvt_pk_bf16_f32 v30, v188, v189\n"
    "v_cvt_pk_bf16_f32 v31, v190, v191\n"
    "v_mfma_f32_32x32x16_bf16 v[176:191], v[208:211], v[144:147], 0\n"
    "buffer_load_dwordx4 v[192:195], v1, s[12:15], s74 offen sc1\n"
    "v_exp_f32 v160, v160\n"
    "v_exp_f32 v161, v161\n"
    "v_exp_f32 v162, v162\n"
    "v_cvt_pk_bf16_f32 v24, v160, v161\n"
    "v_exp_f32 v163, v163\n"
    "v_mfma_f32_32x32x16_bf16 v[64:79], v[204:207], v[28:31], v[64:79]\n"
    "v_exp_f32 v164, v164\n"
    "v_exp_f32 v165, v165\n"
    "v_cvt_pk_bf16_f32 v25, v162, v163\n"
    "v_exp_f32 v166, v166\n"
    "v_exp_f32 v167, v167\n"
    "v_cvt_pk_bf16_f32 v26, v164, v165\n"
    "v_mfma_f32_32x32x16_bf16 v[176:191], v[212:215], v[148:151], v[176:191]\n"
    "v_exp_f32 v168, v168\n"
    "v_exp_f32 v169, v169\n"
    "v_cvt_pk_bf16_f32 v27, v166, v167\n"
    "v_exp_f32 v170, v170\n"
    "v_exp_f32 v171, v171\n"
    "v_cvt_pk_bf16_f32 v28, v168, v169\n"
    "v_mfma_f32_32x32x16_bf16 v[80:95], v[216:219], v[24:27], v[80:95]\n"
    "v_exp_f32 v172, v172\n"
    "v_exp_f32 v173, v173\n"
    "v_cvt_pk_bf16_f32 v29, v170, v171\n"
    "v_exp_f32 v174, v174\n"
    "v_exp_f32 v175, v175\n"
    "v_cvt_pk_bf16_f32 v30, v172, v173\n"
    "v_cvt_pk_bf16_f32 v31, v174, v175\n"
    "v_mfma_f32_32x32x16_bf16 v[160:175], v[208:211], v[152:155], 0\n"
    "buffer_load_dwordx4 v[196:199], v1, s[12:15], s74 offen offset:1024 sc1\n"
    "v_exp_f32 v176, v176\n"
    "v_exp_f32 v177, v177\n"
    "v_exp_f32 v178, v178\n"
    "v_cvt_pk_bf16_f32 v24, v176, v177\n"
    "v_exp_f32 v179, v179\n"
    "v_mfma_f32_32x32x16_bf16 v[80:95], v[220:223], v[28:31], v[80:95]\n"
    "v_exp_f32 v180, v180\n"
    "v_exp_f32 v181, v181\n"
    "v_cvt_pk_bf16_f32 v25, v178, v179\n"
    "v_exp_f32 v182, v182\n"
    "v_exp_f32 v183, v183\n"
    "v_cvt_pk_bf16_f32 v26, v180, v181\n"
    "v_mfma_f32_32x32x16_bf16 v[160:175], v[212:215], v[156:159], v[160:175]\n"
    "v_exp_f32 v184, v184\n"
    "v_exp_f32 v185, v185\n"
    "v_cvt_pk_bf16_f32 v27, v182, v183\n"
    "v_exp_f32 v186, v186\n"
    "v_exp_f32 v187, v187\n"
    "v_cvt_pk_bf16_f32 v28, v184, v185\n"
    "v_mfma_f32_32x32x16_bf16 v[96:111], v[216:219], v[24:27], v[96:111]\n"
    "v_exp_f32 v188, v188\n"
    "v_exp_f32 v189, v189\n"
    "v_cvt_pk_bf16_f32 v29, v186, v187\n"
    "v_exp_f32 v190, v190\n"
    "v_exp_f32 v191, v191\n"
    "v_cvt_pk_bf16_f32 v30, v188, v189\n"
    "v_cvt_pk_bf16_f32 v31, v190, v191\n"
    "v_mfma_f32_32x32x16_bf16 v[176:191], v[208:211], v[128:131], 0\n"
    "buffer_load_dwordx4 v[200:203], v1, s[12:15], s74 offen offset:2048 sc1\n"
    "v_exp_f32 v160, v160\n"
    "v_exp_f32 v161, v161\n"
    "v_exp_f32 v162, v162\n"
    "v_cvt_pk_bf16_f32 v24, v160, v161\n"
    "v_exp_f32 v163, v163\n"
    "v_mfma_f32_32x32x16_bf16 v[96:111], v[220:223], v[28:31], v[96:111]\n"
    "v_exp_f32 v164, v164\n"
    "v_exp_f32 v165, v165\n"
    "v_cvt_pk_bf16_f32 v25, v162, v163\n"
    "v_exp_f32 v166, v166\n"
    "v_exp_f32 v167, v167\n"
    "v_cvt_pk_bf16_f32 v26, v164, v165\n"
    "v_mfma_f32_32x32x16_bf16 v[176:191], v[212:215], v[132:135], v[176:191]\n"
    "v_exp_f32 v168, v168\n"
    "v_exp_f32 v169, v169\n"
    "v_cvt_pk_bf16_f32 v27, v166, v167\n"
    "v_exp_f32 v170, v170\n"
    "v_exp_f32 v171, v171\n"
    "v_cvt_pk_bf16_f32 v28, v168, v169\n"
    "v_mfma_f32_32x32x16_bf16 v[112:127], v[216:219], v[24:27], v[112:127]\n"
    "s_waitcnt vmcnt(9)\n"
    "v_exp_f32 v172, v172\n"
    "v_exp_f32 v173, v173\n"
    "v_cvt_pk_bf16_f32 v29, v170, v171\n"
    "v_exp_f32 v174, v174\n"
    "v_exp_f32 v175, v175\n"
    "v_cvt_pk_bf16_f32 v30, v172, v173\n"
    "v_cvt_pk_bf16_f32 v31, v174, v175\n"
    "v_mfma_f32_32x32x16_bf16 v[160:175], v[224:227], v[136:139], 0\n"
    "buffer_load_dwordx4 v[204:207], v1, s[12:15], s74 offen offset:3072 sc1\n"
    "v_exp_f32 v176, v176\n"
    "v_exp_f32 v177, v177\n"
    "v_exp_f32 v178, v178\n"
    "v_cvt_pk_bf16_f32 v24, v176, v177\n"
    "v_exp_f32 v179, v179\n"
    "v_mfma_f32_32x32x16_bf16 v[112:127], v[220:223], v[28:31], v[112:127]\n"
    "v_exp_f32 v180, v180\n"
    "v_exp_f32 v181, v181\n"
    "v_cvt_pk_bf16_f32 v25, v178, v179\n"
    "v_exp_f32 v182, v182\n"
    "v_exp_f32 v183, v183\n"
    "v_cvt_pk_bf16_f32 v26, v180, v181\n"
    "v_mfma_f32_32x32x16_bf16 v[160:175], v[228:231], v[140:143], v[160:175]\n"
    "v_exp_f32 v184, v184\n"
    "v_exp_f32 v185, v185\n"
    "v_cvt_pk_bf16_f32 v27, v182, v183\n"
    "v_exp_f32 v186, v186\n"
    "v_exp_f32 v187, v187\n"
    "v_cvt_pk_bf16_f32 v28, v184, v185\n"
    "v_mfma_f32_32x32x16_bf16 v[64:79], v[216:219], v[24:27], v[64:79]\n"
    "v_exp_f32 v188, v188\n"
    "v_exp_f32 v189, v189\n"
    "v_cvt_pk_bf16_f32 v29, v186, v187\n"
    "v_exp_f32 v190, v190\n"
    "v_exp_f32 v191, v191\n"
    "v_cvt_pk_bf16_f32 v30, v188, v189\n"
    "v_cvt_pk_bf16_f32 v31, v190, v191\n"
    "v_mfma_f32_32x32x16_bf16 v[176:191], v[224:227], v[144:147], 0\n"
    "buffer_load_dwordx4 v[208:211], v1, s[12:15], s75 offen sc1\n"
    "v_exp_f32 v160, v160\n"
    "v_exp_f32 v161, v161\n"
    "v_exp_f32 v162, v162\n"
    "v_cvt_pk_bf16_f32 v24, v160, v161\n"
    "v_exp_f32 v163, v163\n"
    "v_mfma_f32_32x32x16_bf16 v[64:79], v[220:223], v[28:31], v[64:79]\n"
    "v_exp_f32 v164, v164\n"
    "v_exp_f32 v165, v165\n"
    "v_cvt_pk_bf16_f32 v25, v162, v163\n"
    "v_exp_f32 v166, v166\n"
    "v_exp_f32 v167, v167\n"
    "v_cvt_pk_bf16_f32 v26, v164, v165\n"
    "v_mfma_f32_32x32x16_bf16 v[176:191], v[228:231], v[148:151], v[176:191]\n"
    "v_exp_f32 v168, v168\n"
    "v_exp_f32 v169, v169\n"
    "v_cvt_pk_bf16_f32 v27, v166, v167\n"
    "v_exp_f32 v170, v170\n"
    "v_exp_f32 v171, v171\n"
    "v_cvt_pk_bf16_f32 v28, v168, v169\n"
    "v_mfma_f32_32x32x16_bf16 v[80:95], v[232:235], v[24:27], v[80:95]\n"
    "v_exp_f32 v172, v172\n"
    "v_exp_f32 v173, v173\n"
    "v_cvt_pk_bf16_f32 v29, v170, v171\n"
    "v_exp_f32 v174, v174\n"
    "v_exp_f32 v175, v175\n"
    "v_cvt_pk_bf16_f32 v30, v172, v173\n"
    "v_cvt_pk_bf16_f32 v31, v174, v175\n"
    "v_mfma_f32_32x32x16_bf16 v[160:175], v[224:227], v[152:155], 0\n"
    "buffer_load_dwordx4 v[212:215], v1, s[12:15], s75 offen offset:1024 sc1\n"
    "v_exp_f32 v176, v176\n"
    "v_exp_f32 v177, v177\n"
    "v_exp_f32 v178, v178\n"
    "v_cvt_pk_bf16_f32 v24, v176, v177\n"
    "v_exp_f32 v179, v179\n"
    "v_mfma_f32_32x32x16_bf16 v[80:95], v[236:239], v[28:31], v[80:95]\n"
    "v_exp_f32 v180, v180\n"
    "v_exp_f32 v181, v181\n"
    "v_cvt_pk_bf16_f32 v25, v178, v179\n"
    "v_exp_f32 v182, v182\n"
    "v_exp_f32 v183, v183\n"
    "v_cvt_pk_bf16_f32 v26, v180, v181\n"
    "v_mfma_f32_32x32x16_bf16 v[160:175], v[228:231], v[156:159], v[160:175]\n"
    "v_exp_f32 v184, v184\n"
    "v_exp_f32 v185, v185\n"
    "v_cvt_pk_bf16_f32 v27, v182, v183\n"
    "v_exp_f32 v186, v186\n"
    "v_exp_f32 v187, v187\n"
    "v_cvt_pk_bf16_f32 v28, v184, v185\n"
    "v_mfma_f32_32x32x16_bf16 v[96:111], v[232:235], v[24:27], v[96:111]\n"
    "v_exp_f32 v188, v188\n"
    "v_exp_f32 v189, v189\n"
    "v_cvt_pk_bf16_f32 v29, v186, v187\n"
    "v_exp_f32 v190, v190\n"
    "v_exp_f32 v191, v191\n"
    "v_cvt_pk_bf16_f32 v30, v188, v189\n"
    "v_cvt_pk_bf16_f32 v31, v190, v191\n"
    "v_mfma_f32_32x32x16_bf16 v[176:191], v[224:227], v[128:131], 0\n"
    "buffer_load_dwordx4 v[216:219], v1, s[12:15], s75 offen offset:2048 sc1\n"
    "s_add_u32 m0, s67, 0\n"
    "v_exp_f32 v160, v160\n"
    "v_exp_f32 v161, v161\n"
    "v_exp_f32 v162, v162\n"
    "v_cvt_pk_bf16_f32 v24, v160, v161\n"
    "v_exp_f32 v163, v163\n"
    "v_mfma_f32_32x32x16_bf16 v[96:111], v[236:239], v[28:31], v[96:111]\n"
    "buffer_load_dwordx4 v13, s[16:19], s86 offen lds\n"
    "v_exp_f32 v164, v164\n"
    "v_exp_f32 v165, v165\n"
    "v_cvt_pk_bf16_f32 v25, v162, v163\n"
    "v_exp_f32 v166, v166\n"
    "v_exp_f32 v167, v167\n"
    "v_cvt_pk_bf16_f32 v26, v164, v165\n"
    "v_mfma_f32_32x32x16_bf16 v[176:191], v[228:231], v[132:135], v[176:191]\n"
    "v_exp_f32 v168, v168\n"
    "v_exp_f32 v169, v169\n"
    "v_cvt_pk_bf16_f32 v27, v166, v167\n"
    "v_exp_f32 v170, v170\n"
    "v_exp_f32 v171, v171\n"
    "v_cvt_pk_bf16_f32 v28, v168, v169\n"
    "v_mfma_f32_32x32x16_bf16 v[112:127], v[232:235], v[24:27], v[112:127]\n"
    "s_waitcnt vmcnt(8)\n"
    "buffer_load_dwordx4 v14, s[16:19], s86 offen offset:1024 lds\n"
    "s_mov_b64 exec, 1\n"
    "buffer_store_dwordx2 v[22:23], off, s[12:15], s65 sc1\n"
    "s_mov_b64 exec, -1\n"
    "v_exp_f32 v172, v172\n"
    "v_exp_f32 v173, v173\n"
    "v_cvt_pk_bf16_f32 v29, v170, v171\n"
    "v_exp_f32 v174, v174\n"
    "v_exp_f32 v175, v175\n"
    "v_cvt_pk_bf16_f32 v30, v172, v173\n"
    "v_cvt_pk_bf16_f32 v31, v174, v175\n"
    "v_mfma_f32_32x32x16_bf16 v[160:175], v[240:243], v[136:139], 0\n"
    "buffer_load_dwordx4 v[220:223], v1, s[12:15], s75 offen offset:3072 sc1\n"
    "v_exp_f32 v176, v176\n"
    "v_exp_f32 v177, v177\n"
    "v_exp_f32 v178, v178\n"
    "v_cvt_pk_bf16_f32 v24, v176, v177\n"
    "v_exp_f32 v179, v179\n"
    "v_mfma_f32_32x32x16_bf16 v[112:127], v[236:239], v[28:31], v[112:127]\n"
    "buffer_load_dwordx4 v15, s[16:19], s86 offen offset:2048 lds\n"
    "v_exp_f32 v180, v180\n"
    "v_exp_f32 v181, v181\n"
    "v_cvt_pk_bf16_f32 v25, v178, v179\n"
    "v_exp_f32 v182, v182\n"
    "v_exp_f32 v183, v183\n"
    "v_cvt_pk_bf16_f32 v26, v180, v181\n"
    "v_mfma_f32_32x32x16_bf16 v[160:175], v[244:247], v[140:143], v[160:175]\n"
    "v_exp_f32 v184, v184\n"
    "v_exp_f32 v185, v185\n"
    "v_cvt_pk_bf16_f32 v27, v182, v183\n"
    "v_exp_f32 v186, v186\n"
    "v_exp_f32 v187, v187\n"
    "v_cvt_pk_bf16_f32 v28, v184, v185\n"
    "v_mfma_f32_32x32x16_bf16 v[64:79], v[232:235], v[24:27], v[64:79]\n"
    "buffer_load_dwordx4 v16, s[16:19], s86 offen offset:3072 lds\n"
    "v_exp_f32 v188, v188\n"
    "v_exp_f32 v189, v189\n"
    "v_cvt_pk_bf16_f32 v29, v186, v187\n"
    "v_exp_f32 v190, v190\n"
    "v_exp_f32 v191, v191\n"
    "v_cvt_pk_bf16_f32 v30, v188, v189\n"
    "v_cvt_pk_bf16_f32 v31, v190, v191\n"
    "v_mfma_f32_32x32x16_bf16 v[176:191], v[240:243], v[144:147], 0\n"
    "buffer_load_dwordx4 v[224:227], v1, s[12:15], s76 offen sc1\n"
    "s_add_u32 m0, s67, 0x1000\n"
    "v_exp_f32 v160, v160\n"
    "v_exp_f32 v161, v161\n"
    "v_exp_f32 v162, v162\n"
    "v_cvt_pk_bf16_f32 v24, v160, v161\n"
    "v_exp_f32 v163, v163\n"
    "v_mfma_f32_32x32x16_bf16 v[64:79], v[236:239], v[28:31], v[64:79]\n"
    "buffer_load_dwordx4 v13, s[16:19], s87 offen lds\n"
    "v_exp_f32 v164, v164\n"
    "v_exp_f32 v165, v165\n"
    "v_cvt_pk_bf16_f32 v25, v162, v163\n"
    "v_exp_f32 v166, v166\n"
    "v_exp_f32 v167, v167\n"
    "v_cvt_pk_bf16_f32 v26, v164, v165\n"
    "v_mfma_f32_32x32x16_bf16 v[176:191], v[244:247], v[148:151], v[176:191]\n"
    "buffer_load_dwordx2 v[32:33], v11, s[12:15], 0 offen sc1\n"
    "v_exp_f32 v168, v168\n"
    "v_exp_f32 v169, v169\n"
    "v_cvt_pk_bf16_f32 v27, v166, v167\n"
    "v_exp_f32 v170, v170\n"
    "v_exp_f32 v171, v171\n"
    "v_cvt_pk_bf16_f32 v28, v168, v169\n"
    "v_mfma_f32_32x32x16_bf16 v[80:95], v[248:251], v[24:27], v[80:95]\n"
    "buffer_load_dwordx4 v14, s[16:19], s87 offen offset:1024 lds\n"
    "v_exp_f32 v172, v172\n"
    "v_exp_f32 v173, v173\n"
    "v_cvt_pk_bf16_f32 v29, v170, v171\n"
    "v_exp_f32 v174, v174\n"
    "v_exp_f32 v175, v175\n"
    "v_cvt_pk_bf16_f32 v30, v172, v173\n"
    "v_cvt_pk_bf16_f32 v31, v174, v175\n"
    "v_mfma_f32_32x32x16_bf16 v[160:175], v[240:243], v[152:155], 0\n"
    "buffer_load_dwordx4 v[228:231], v1, s[12:15], s76 offen offset:1024 sc1\n"
    "v_exp_f32 v176, v176\n"
    "v_exp_f32 v177, v177\n"
    "v_exp_f32 v178, v178\n"
    "v_cvt_pk_bf16_f32 v24, v176, v177\n"
    "v_exp_f32 v179, v179\n"
    "v_mfma_f32_32x32x16_bf16 v[80:95], v[252:255], v[28:31], v[80:95]\n"
    "buffer_load_dwordx4 v15, s[16:19], s87 offen offset:2048 lds\n"
    "v_exp_f32 v180, v180\n"
    "v_exp_f32 v181, v181\n"
    "v_cvt_pk_bf16_f32 v25, v178, v179\n"
    "v_exp_f32 v182, v182\n"
    "v_exp_f32 v183, v183\n"
    "v_cvt_pk_bf16_f32 v26, v180, v181\n"
    "v_mfma_f32_32x32x16_bf16 v[160:175], v[244:247], v[156:159], v[160:175]\n"
    "v_exp_f32 v184, v184\n"
    "v_exp_f32 v185, v185\n"
    "v_cvt_pk_bf16_f32 v27, v182, v183\n"
    "v_exp_f32 v186, v186\n"
    "v_exp_f32 v187, v187\n"
    "v_cvt_pk_bf16_f32 v28, v184, v185\n"
    "v_mfma_f32_32x32x16_bf16 v[96:111], v[248:251], v[24:27], v[96:111]\n"
    "buffer_load_dwordx4 v16, s[16:19], s87 offen offset:3072 lds\n"
    "v_exp_f32 v188, v188\n"
    "v_exp_f32 v189, v189\n"
    "v_cvt_pk_bf16_f32 v29, v186, v187\n"
    "v_exp_f32 v190, v190\n"
    "v_exp_f32 v191, v191\n"
    "v_cvt_pk_bf16_f32 v30, v188, v189\n"
    "v_cvt_pk_bf16_f32 v31, v190, v191\n"
    "v_mfma_f32_32x32x16_bf16 v[176:191], v[240:243], v[128:131], 0\n"
    "buffer_load_dwordx4 v[232:235], v1, s[12:15], s76 offen offset:2048 sc1\n"
    "s_add_u32 m0, s67, 0x2000\n"
    "v_exp_f32 v160, v160\n"
    "v_exp_f32 v161, v161\n"
    "v_exp_f32 v162, v162\n"
    "v_cvt_pk_bf16_f32 v24, v160, v161\n"
    "v_exp_f32 v163, v163\n"
    "v_mfma_f32_32x32x16_bf16 v[96:111], v[252:255], v[28:31], v[96:111]\n"
    "buffer_load_dwordx4 v13, s[16:19], s88 offen lds\n"
    "v_exp_f32 v164, v164\n"
    "v_exp_f32 v165, v165\n"
    "v_cvt_pk_bf16_f32 v25, v162, v163\n"
    "v_exp_f32 v166, v166\n"
    "v_exp_f32 v167, v167\n"
    "v_cvt_pk_bf16_f32 v26, v164, v165\n"
    "v_mfma_f32_32x32x16_bf16 v[176:191], v[244:247], v[132:135], v[176:191]\n"
    "v_exp_f32 v168, v168\n"
    "v_exp_f32 v169, v169\n"
    "v_cvt_pk_bf16_f32 v27, v166, v167\n"
    "v_exp_f32 v170, v170\n"
    "v_exp_f32 v171, v171\n"
    "v_cvt_pk_bf16_f32 v28, v168, v169\n"
    "v_mfma_f32_32x32x16_bf16 v[112:127], v[248:251], v[24:27], v[112:127]\n"
    "s_waitcnt vmcnt(18)\n"
    "buffer_load_dwordx4 v14, s[16:19], s88 offen offset:1024 lds\n"
    "v_exp_f32 v172, v172\n"
    "v_exp_f32 v173, v173\n"
    "v_cvt_pk_bf16_f32 v29, v170, v171\n"
    "v_exp_f32 v174, v174\n"
    "v_exp_f32 v175, v175\n"
    "v_cvt_pk_bf16_f32 v30, v172, v173\n"
    "v_cvt_pk_bf16_f32 v31, v174, v175\n"
    "v_mfma_f32_32x32x16_bf16 v[160:175], v[192:195], v[136:139], 0\n"
    "buffer_load_dwordx4 v[236:239], v1, s[12:15], s76 offen offset:3072 sc1\n"
    "v_exp_f32 v176, v176\n"
    "v_exp_f32 v177, v177\n"
    "v_exp_f32 v178, v178\n"
    "v_cvt_pk_bf16_f32 v24, v176, v177\n"
    "v_exp_f32 v179, v179\n"
    "v_mfma_f32_32x32x16_bf16 v[112:127], v[252:255], v[28:31], v[112:127]\n"
    "buffer_load_dwordx4 v15, s[16:19], s88 offen offset:2048 lds\n"
    "v_exp_f32 v180, v180\n"
    "v_exp_f32 v181, v181\n"
    "v_cvt_pk_bf16_f32 v25, v178, v179\n"
    "v_exp_f32 v182, v182\n"
    "v_exp_f32 v183, v183\n"
    "v_cvt_pk_bf16_f32 v26, v180, v181\n"
    "v_mfma_f32_32x32x16_bf16 v[160:175], v[196:199], v[140:143], v[160:175]\n"
    "v_exp_f32 v184, v184\n"
    "v_exp_f32 v185, v185\n"
    "v_cvt_pk_bf16_f32 v27, v182, v183\n"
    "v_exp_f32 v186, v186\n"
    "v_exp_f32 v187, v187\n"
    "v_cvt_pk_bf16_f32 v28, v184, v185\n"
    "v_mfma_f32_32x32x16_bf16 v[64:79], v[248:251], v[24:27], v[64:79]\n"
    "buffer_load_dwordx4 v16, s[16:19], s88 offen offset:3072 lds\n"
    "v_exp_f32 v188, v188\n"
    "v_exp_f32 v189, v189\n"
    "v_cvt_pk_bf16_f32 v29, v186, v187\n"
    "v_exp_f32 v190, v190\n"
    "v_exp_f32 v191, v191\n"
    "v_cvt_pk_bf16_f32 v30, v188, v189\n"
    "v_cvt_pk_bf16_f32 v31, v190, v191\n"
    "v_mfma_f32_32x32x16_bf16 v[176:191], v[192:195], v[144:147], 0\n"
    "buffer_load_dwordx4 v[240:243], v1, s[12:15], s77 offen sc1\n"
    "s_waitcnt vmcnt(14)\n"
    "ds_read_b64_tr_b16 a[0:1], v20\n"
    "s_add_u32 m0, s68, s1\n"
    "v_exp_f32 v160, v160\n"
    "v_exp_f32 v161, v161\n"
    "v_exp_f32 v162, v162\n"
    "v_cvt_pk_bf16_f32 v24, v160, v161\n"
    "v_exp_f32 v163, v163\n"
    "v_mfma_f32_32x32x16_bf16 v[64:79], v[252:255], v[28:31], v[64:79]\n"
    "ds_read_b64_tr_b16 a[2:3], v20 offset:1024\n"
    "buffer_load_dwordx4 v17, s[20:23], s66 offen lds\n"
    "v_exp_f32 v164, v164\n"
    "v_exp_f32 v165, v165\n"
    "v_cvt_pk_bf16_f32 v25, v162, v163\n"
    "v_exp_f32 v166, v166\n"
    "v_exp_f32 v167, v167\n"
    "v_cvt_pk_bf16_f32 v26, v164, v165\n"
    "v_mfma_f32_32x32x16_bf16 v[176:191], v[196:199], v[148:151], v[176:191]\n"
    "ds_read_b64_tr_b16 a[4:5], v21\n"
    "v_exp_f32 v168, v168\n"
    "v_exp_f32 v169, v169\n"
    "v_cvt_pk_bf16_f32 v27, v166, v167\n"
    "v_exp_f32 v170, v170\n"
    "v_exp_f32 v171, v171\n"
    "v_cvt_pk_bf16_f32 v28, v168, v169\n"
    "v_mfma_f32_32x32x16_bf16 v[80:95], v[200:203], v[24:27], v[80:95]\n"
    "ds_read_b64_tr_b16 a[6:7], v21 offset:1024\n"
    "buffer_load_dwordx4 v18, s[20:23], s66 offen offset:1024 lds\n"
    "v_exp_f32 v172, v172\n"
    "v_exp_f32 v173, v173\n"
    "v_cvt_pk_bf16_f32 v29, v170, v171\n"
    "v_exp_f32 v174, v174\n"
    "v_exp_f32 v175, v175\n"
    "v_cvt_pk_bf16_f32 v30, v172, v173\n"
    "v_cvt_pk_bf16_f32 v31, v174, v175\n"
    "v_mfma_f32_32x32x16_bf16 v[160:175], v[192:195], v[152:155], 0\n"
    "buffer_load_dwordx4 v[244:247], v1, s[12:15], s77 offen offset:1024 sc1\n"
    "ds_read_b64_tr_b16 a[8:9], v20 offset:2048\n"
    "v_exp_f32 v176, v176\n"
    "v_exp_f32 v177, v177\n"
    "v_exp_f32 v178, v178\n"
    "v_cvt_pk_bf16_f32 v24, v176, v177\n"
    "v_exp_f32 v179, v179\n"
    "v_mfma_f32_32x32x16_bf16 v[80:95], v[204:207], v[28:31], v[80:95]\n"
    "ds_read_b64_tr_b16 a[10:11], v20 offset:3072\n"
    "buffer_load_dwordx4 v19, s[20:23], s66 offen offset:2048 lds\n"
    "v_exp_f32 v180, v180\n"
    "v_exp_f32 v181, v181\n"
    "v_cvt_pk_bf16_f32 v25, v178, v179\n"
    "v_exp_f32 v182, v182\n"
    "v_exp_f32 v183, v183\n"
    "v_cvt_pk_bf16_f32 v26, v180, v181\n"
    "v_mfma_f32_32x32x16_bf16 v[160:175], v[196:199], v[156:159], v[160:175]\n"
    "ds_read_b64_tr_b16 a[12:13], v21 offset:2048\n"
    "v_exp_f32 v184, v184\n"
    "v_exp_f32 v185, v185\n"
    "v_cvt_pk_bf16_f32 v27, v182, v183\n"
    "v_exp_f32 v186, v186\n"
    "v_exp_f32 v187, v187\n"
    "v_cvt_pk_bf16_f32 v28, v184, v185\n"
    "v_mfma_f32_32x32x16_bf16 v[96:111], v[200:203], v[24:27], v[96:111]\n"
    "ds_read_b64_tr_b16 a[14:15], v21 offset:3072\n"
    "buffer_load_dwordx4 v10, s[20:23], s66 offen offset:3072 lds\n"
    "s_add_u32 s1, s1, 0x1000\n"
    "s_add_u32 s66, s66, 0x7000\n"
    "s_cmp_eq_u32 s1, 0x7000\n"
    "s_cselect_b32 s1, 0, s1\n"
    "s_cselect_b32 s66, 0, s66\n"
    "v_exp_f32 v188, v188\n"
    "v_exp_f32 v189, v189\n"
    "v_cvt_pk_bf16_f32 v29, v186, v187\n"
    "v_exp_f32 v190, v190\n"
    "v_exp_f32 v191, v191\n"
    "v_cvt_pk_bf16_f32 v30, v188, v189\n"
    "v_cvt_pk_bf16_f32 v31, v190, v191\n"
    "v_mfma_f32_32x32x16_bf16 v[176:191], v[192:195], v[128:131], 0\n"
    "buffer_load_dwordx4 v[248:251], v1, s[12:15], s77 offen offset:2048 sc1\n"
    "s_waitcnt vmcnt(13)\n"
    "ds_read_b64_tr_b16 a[16:17], v20 offset:4096\n"
    "s_waitcnt lgkmcnt(1)\n"
    "s_add_u32 m0, s67, 0\n"
    "v_exp_f32 v160, v160\n"
    "v_exp_f32 v161, v161\n"
    "v_exp_f32 v162, v162\n"
    "v_cvt_pk_bf16_f32 v24, v160, v161\n"
    "v_exp_f32 v163, v163\n"
    "v_mfma_f32_32x32x16_bf16 v[96:111], v[204:207], v[28:31], v[96:111]\n"
    "ds_read_b64_tr_b16 a[18:19], v20 offset:5120\n"
    "buffer_load_dwordx4 v13, s[16:19], s89 offen lds\n"
    "v_exp_f32 v164, v164\n"
    "v_exp_f32 v165, v165\n"
    "v_cvt_pk_bf16_f32 v25, v162, v163\n"
    "v_exp_f32 v166, v166\n"
    "v_exp_f32 v167, v167\n"
    "v_cvt_pk_bf16_f32 v26, v164, v165\n"
    "v_mfma_f32_32x32x16_bf16 v[176:191], v[196:199], v[132:135], v[176:191]\n"
    "ds_read_b64_tr_b16 a[20:21], v21 offset:4096\n"
    "v_exp_f32 v168, v168\n"
    "v_exp_f32 v169, v169\n"
    "v_cvt_pk_bf16_f32 v27, v166, v167\n"
    "v_exp_f32 v170, v170\n"
    "v_exp_f32 v171, v171\n"
    "v_cvt_pk_bf16_f32 v28, v168, v169\n"
    "v_mfma_f32_32x32x16_bf16 v[112:127], v[200:203], v[24:27], v[112:127]\n"
    "ds_read_b64_tr_b16 a[22:23], v21 offset:5120\n"
    "buffer_load_dwordx4 v14, s[16:19], s89 offen offset:1024 lds\n"
    "v_exp_f32 v172, v172\n"
    "v_exp_f32 v173, v173\n"
    "v_cvt_pk_bf16_f32 v29, v170, v171\n"
    "v_exp_f32 v174, v174\n"
    "v_exp_f32 v175, v175\n"
    "v_cvt_pk_bf16_f32 v30, v172, v173\n"
    "v_cvt_pk_bf16_f32 v31, v174, v175\n"
    "v_mfma_f32_32x32x16_bf16 v[160:175], v[208:211], v[136:139], 0\n"
    "buffer_load_dwordx4 v[252:255], v1, s[12:15], s77 offen offset:3072 sc1\n"
    "ds_read_b64_tr_b16 a[24:25], v20 offset:6144\n"
    "v_exp_f32 v176, v176\n"
    "v_exp_f32 v177, v177\n"
    "v_exp_f32 v178, v178\n"
    "v_cvt_pk_bf16_f32 v24, v176, v177\n"
    "v_exp_f32 v179, v179\n"
    "v_mfma_f32_32x32x16_bf16 v[112:127], v[204:207], v[28:31], v[112:127]\n"
    "ds_read_b64_tr_b16 a[26:27], v20 offset:7168\n"
    "buffer_load_dwordx4 v15, s[16:19], s89 offen offset:2048 lds\n"
    "v_exp_f32 v180, v180\n"
    "v_exp_f32 v181, v181\n"
    "v_cvt_pk_bf16_f32 v25, v178, v179\n"
    "v_exp_f32 v182, v182\n"
    "v_exp_f32 v183, v183\n"
    "v_cvt_pk_bf16_f32 v26, v180, v181\n"
    "v_mfma_f32_32x32x16_bf16 v[160:175], v[212:215], v[140:143], v[160:175]\n"
    "ds_read_b64_tr_b16 a[28:29], v21 offset:6144\n"
    "v_cmp_ne_u64 vcc, v[32:33], v[22:23]\n"
    "s_nop 0\n"
    "s_cbranch_vccnz L_f3\n"
    "L_f3_back:\n"
    "v_exp_f32 v184, v184\n"
    "v_exp_f32 v185, v185\n"
    "v_cvt_pk_bf16_f32 v27, v182, v183\n"
    "v_exp_f32 v186, v186\n"
    "v_exp_f32 v187, v187\n"
    "v_cvt_pk_bf16_f32 v28, v184, v185\n"
    "v_mfma_f32_32x32x16_bf16 v[64:79], v[200:203], v[24:27], v[64:79]\n"
    "ds_read_b64_tr_b16 a[30:31], v21 offset:7168\n"
    "buffer_load_dwordx4 v16, s[16:19], s89 offen offset:3072 lds\n"
    "v_exp_f32 v188, v188\n"
    "v_exp_f32 v189, v189\n"
    "v_cvt_pk_bf16_f32 v29, v186, v187\n"
    "v_exp_f32 v190, v190\n"
    "v_exp_f32 v191, v191\n"
    "v_cvt_pk_bf16_f32 v30, v188, v189\n"
    "v_cvt_pk_bf16_f32 v31, v190, v191\n"
    "v_mfma_f32_32x32x16_bf16 v[176:191], v[208:211], v[144:147], 0\n"
    "buffer_load_dwordx4 v[192:195], v1, s[12:15], s78 offen sc1\n"
    "s_waitcnt vmcnt(13)\n"
    "ds_read_b64_tr_b16 a[32:33], v20 offset:8192\n"
    "s_waitcnt lgkmcnt(1)\n"
    "s_add_u32 m0, s67, 0x1000\n"
    "v_exp_f32 v160, v160\n"
    "v_exp_f32 v161, v161\n"
    "v_exp_f32 v162, v162\n"
    "v_cvt_pk_bf16_f32 v24, v160, v161\n"
    "v_exp_f32 v163, v163\n"
    "v_mfma_f32_32x32x16_bf16 v[64:79], v[204:207], v[28:31], v[64:79]\n"
    "ds_read_b64_tr_b16 a[34:35], v20 offset:9216\n"
    "buffer_load_dwordx4 v13, s[16:19], s90 offen lds\n"
    "v_exp_f32 v164, v164\n"
    "v_exp_f32 v165, v165\n"
    "v_cvt_pk_bf16_f32 v25, v162, v163\n"
    "v_exp_f32 v166, v166\n"
    "v_exp_f32 v167, v167\n"
    "v_cvt_pk_bf16_f32 v26, v164, v165\n"
    "v_mfma_f32_32x32x16_bf16 v[176:191], v[212:215], v[148:151], v[176:191]\n"
    "ds_read_b64_tr_b16 a[36:37], v21 offset:8192\n"
    "v_exp_f32 v168, v168\n"
    "v_exp_f32 v169, v169\n"
    "v_cvt_pk_bf16_f32 v27, v166, v167\n"
    "v_exp_f32 v170, v170\n"
    "v_exp_f32 v171, v171\n"
    "v_cvt_pk_bf16_f32 v28, v168, v169\n"
    "v_mfma_f32_32x32x16_bf16 v[80:95], v[216:219], v[24:27], v[80:95]\n"
    "ds_read_b64_tr_b16 a[38:39], v21 offset:9216\n"
    "buffer_load_dwordx4 v14, s[16:19], s90 offen offset:1024 lds\n"
    "v_exp_f32 v172, v172\n"
    "v_exp_f32 v173, v173\n"
    "v_cvt_pk_bf16_f32 v29, v170, v171\n"
    "v_exp_f32 v174, v174\n"
    "v_exp_f32 v175, v175\n"
    "v_cvt_pk_bf16_f32 v30, v172, v173\n"
    "v_cvt_pk_bf16_f32 v31, v174, v175\n"
    "v_mfma_f32_32x32x16_bf16 v[160:175], v[208:211], v[152:155], 0\n"
    "buffer_load_dwordx4 v[196:199], v1, s[12:15], s78 offen offset:1024 sc1\n"
    "ds_read_b64_tr_b16 a[40:41], v20 offset:10240\n"
    "v_exp_f32 v176, v176\n"
    "v_exp_f32 v177, v177\n"
    "v_exp_f32 v178, v178\n"
    "v_cvt_pk_bf16_f32 v24, v176, v177\n"
    "v_exp_f32 v179, v179\n"
    "v_mfma_f32_32x32x16_bf16 v[80:95], v[220:223], v[28:31], v[80:95]\n"
    "ds_read_b64_tr_b16 a[42:43], v20 offset:11264\n"
    "buffer_load_dwordx4 v15, s[16:19], s90 offen offset:2048 lds\n"
    "v_exp_f32 v180, v180\n"
    "v_exp_f32 v181, v181\n"
    "v_cvt_pk_bf16_f32 v25, v178, v179\n"
    "v_exp_f32 v182, v182\n"
    "v_exp_f32 v183, v183\n"
    "v_cvt_pk_bf16_f32 v26, v180, v181\n"
    "v_mfma_f32_32x32x16_bf16 v[160:175], v[212:215], v[156:159], v[160:175]\n"
    "ds_read_b64_tr_b16 a[44:45], v21 offset:10240\n"
    "v_exp_f32 v184, v184\n"
    "v_exp_f32 v185, v185\n"
    "v_cvt_pk_bf16_f32 v27, v182, v183\n"
    "v_exp_f32 v186, v186\n"
    "v_exp_f32 v187, v187\n"
    "v_cvt_pk_bf16_f32 v28, v184, v185\n"
    "v_mfma_f32_32x32x16_bf16 v[96:111], v[216:219], v[24:27], v[96:111]\n"
    "ds_read_b64_tr_b16 a[46:47], v21 offset:11264\n"
    "buffer_load_dwordx4 v16, s[16:19], s90 offen offset:3072 lds\n"
    "buffer_load_dword v34, v38, s[16:19], s69 offen\n"
    "s_add_u32 s69, s69, 0x1c00\n"
    "buffer_load_dword v35, v38, s[16:19], s69 offen\n"
    "v_exp_f32 v188, v188\n"
    "v_exp_f32 v189, v189\n"
    "v_cvt_pk_bf16_f32 v29, v186, v187\n"
    "v_exp_f32 v190, v190\n"
    "v_exp_f32 v191, v191\n"
    "v_cvt_pk_bf16_f32 v30, v188, v189\n"
    "v_cvt_pk_bf16_f32 v31, v190, v191\n"
    "v_mfma_f32_32x32x16_bf16 v[176:191], v[208:211], v[128:131], 0\n"
    "buffer_load_dwordx4 v[200:203], v1, s[12:15], s78 offen offset:2048 sc1\n"
    "v_exp_f32 v160, v160\n"
    "v_exp_f32 v161, v161\n"
    "v_exp_f32 v162, v162\n"
    "v_cvt_pk_bf16_f32 v24, v160, v161\n"
    "v_exp_f32 v163, v163\n"
    "v_mfma_f32_32x32x16_bf16 v[96:111], v[220:223], v[28:31], v[96:111]\n"
    "v_exp_f32 v164, v164\n"
    "v_exp_f32 v165, v165\n"
    "v_cvt_pk_bf16_f32 v25, v162, v163\n"
    "v_exp_f32 v166, v166\n"
    "v_exp_f32 v167, v167\n"
    "v_cvt_pk_bf16_f32 v26, v164, v165\n"
    "v_mfma_f32_32x32x16_bf16 v[176:191], v[212:215], v[132:135], v[176:191]\n"
    "v_exp_f32 v168, v168\n"
    "v_exp_f32 v169, v169\n"
    "v_cvt_pk_bf16_f32 v27, v166, v167\n"
    "v_exp_f32 v170, v170\n"
    "v_exp_f32 v171, v171\n"
    "v_cvt_pk_bf16_f32 v28, v168, v169\n"
    "v_mfma_f32_32x32x16_bf16 v[112:127], v[216:219], v[24:27], v[112:127]\n"
    "v_exp_f32 v172, v172\n"
    "v_exp_f32 v173, v173\n"
    "v_cvt_pk_bf16_f32 v29, v170, v171\n"
    "v_exp_f32 v174, v174\n"
    "v_exp_f32 v175, v175\n"
    "v_cvt_pk_bf16_f32 v30, v172, v173\n"
    "v_cvt_pk_bf16_f32 v31, v174, v175\n"
    "v_mfma_f32_32x32x16_bf16 v[160:175], v[224:227], v[136:139], 0\n"
    "buffer_load_dwordx4 v[204:207], v1, s[12:15], s78 offen offset:3072 sc1\n"
    "v_exp_f32 v176, v176\n"
    "v_exp_f32 v177, v177\n"
    "v_exp_f32 v178, v178\n"
    "v_cvt_pk_bf16_f32 v24, v176, v177\n"
    "v_exp_f32 v179, v179\n"
    "v_mfma_f32_32x32x16_bf16 v[112:127], v[220:223], v[28:31], v[112:127]\n"
    "v_exp_f32 v180, v180\n"
    "v_exp_f32 v181, v181\n"
    "v_cvt_pk_bf16_f32 v25, v178, v179\n"
    "v_exp_f32 v182, v182\n"
    "v_exp_f32 v183, v183\n"
    "v_cvt_pk_bf16_f32 v26, v180, v181\n"
    "v_mfma_f32_32x32x16_bf16 v[160:175], v[228:231], v[140:143], v[160:175]\n"
    "v_exp_f32 v184, v184\n"
    "v_exp_f32 v185, v185\n"
    "v_cvt_pk_bf16_f32 v27, v182, v183\n"
    "v_exp_f32 v186, v186\n"
    "v_exp_f32 v187, v187\n"
    "v_cvt_pk_bf16_f32 v28, v184, v185\n"
    "v_mfma_f32_32x32x16_bf16 v[64:79], v[216:219], v[24:27], v[64:79]\n"
    "v_exp_f32 v188, v188\n"
    "v_exp_f32 v189, v189\n"
    "v_cvt_pk_bf16_f32 v29, v186, v187\n"
    "v_exp_f32 v190, v190\n"
    "v_exp_f32 v191, v191\n"
    "v_cvt_pk_bf16_f32 v30, v188, v189\n"
    "v_cvt_pk_bf16_f32 v31, v190, v191\n"
    "v_mfma_f32_32x32x16_bf16 v[176:191], v[224:227], v[144:147], 0\n"
    "buffer_load_dwordx4 v[208:211], v1, s[12:15], s79 offen sc1\n"
    "s_waitcnt vmcnt(11)\n"
    "ds_read_b64_tr_b16 a[48:49], v20\n"
    "v_exp_f32 v160, v160\n"
    "v_exp_f32 v161, v161\n"
    "v_exp_f32 v162, v162\n"
    "v_cvt_pk_bf16_f32 v24, v160, v161\n"
    "v_exp_f32 v163, v163\n"
    "v_mfma_f32_32x32x16_bf16 v[64:79], v[220:223], v[28:31], v[64:79]\n"
    "ds_read_b64_tr_b16 a[50:51], v20 offset:1024\n"
    "v_exp_f32 v164, v164\n"
    "v_exp_f32 v165, v165\n"
    "v_cvt_pk_bf16_f32 v25, v162, v163\n"
    "v_exp_f32 v166, v166\n"
    "v_exp_f32 v167, v167\n"
    "v_cvt_pk_bf16_f32 v26, v164, v165\n"
    "v_mfma_f32_32x32x16_bf16 v[176:191], v[228:231], v[148:151], v[176:191]\n"
    "ds_read_b64_tr_b16 a[52:53], v21\n"
    "v_exp_f32 v168, v168\n"
    "v_exp_f32 v169, v169\n"
    "v_cvt_pk_bf16_f32 v27, v166, v167\n"
    "v_exp_f32 v170, v170\n"
    "v_exp_f32 v171, v171\n"
    "v_cvt_pk_bf16_f32 v28, v168, v169\n"
    "v_mfma_f32_32x32x16_bf16 v[80:95], v[232:235], v[24:27], v[80:95]\n"
    "ds_read_b64_tr_b16 a[54:55], v21 offset:1024\n"
    "v_exp_f32 v172, v172\n"
    "v_exp_f32 v173, v173\n"
    "v_cvt_pk_bf16_f32 v29, v170, v171\n"
    "v_exp_f32 v174, v174\n"
    "v_exp_f32 v175, v175\n"
    "v_cvt_pk_bf16_f32 v30, v172, v173\n"
    "v_cvt_pk_bf16_f32 v31, v174, v175\n"
    "v_mfma_f32_32x32x16_bf16 v[160:175], v[224:227], v[152:155], 0\n"
    "buffer_load_dwordx4 v[212:215], v1, s[12:15], s79 offen offset:1024 sc1\n"
    "ds_read_b64_tr_b16 a[56:57], v20 offset:2048\n"
    "v_exp_f32 v176, v176\n"
    "v_exp_f32 v177, v177\n"
    "v_exp_f32 v178, v178\n"
    "v_cvt_pk_bf16_f32 v24, v176, v177\n"
    "v_exp_f32 v179, v179\n"
    "v_mfma_f32_32x32x16_bf16 v[80:95], v[236:239], v[28:31], v[80:95]\n"
    "ds_read_b64_tr_b16 a[58:59], v20 offset:3072\n"
    "v_exp_f32 v180, v180\n"
    "v_exp_f32 v181, v181\n"
    "v_cvt_pk_bf16_f32 v25, v178, v179\n"
    "v_exp_f32 v182, v182\n"
    "v_exp_f32 v183, v183\n"
    "v_cvt_pk_bf16_f32 v26, v180, v181\n"
    "v_mfma_f32_32x32x16_bf16 v[160:175], v[228:231], v[156:159], v[160:175]\n"
    "ds_read_b64_tr_b16 a[60:61], v21 offset:2048\n"
    "v_exp_f32 v184, v184\n"
    "v_exp_f32 v185, v185\n"
    "v_cvt_pk_bf16_f32 v27, v182, v183\n"
    "v_exp_f32 v186, v186\n"
    "v_exp_f32 v187, v187\n"
    "v_cvt_pk_bf16_f32 v28, v184, v185\n"
    "v_mfma_f32_32x32x16_bf16 v[96:111], v[232:235], v[24:27], v[96:111]\n"
    "ds_read_b64_tr_b16 a[62:63], v21 offset:3072\n"
    "v_exp_f32 v188, v188\n"
    "v_exp_f32 v189, v189\n"
    "v_cvt_pk_bf16_f32 v29, v186, v187\n"
    "v_exp_f32 v190, v190\n"
    "v_exp_f32 v191, v191\n"
    "v_cvt_pk_bf16_f32 v30, v188, v189\n"
    "v_cvt_pk_bf16_f32 v31, v190, v191\n"
    "v_mfma_f32_32x32x16_bf16 v[176:191], v[224:227], v[128:131], 0\n"
    "buffer_load_dwordx4 v[216:219], v1, s[12:15], s79 offen offset:2048 sc1\n"
    "s_waitcnt vmcnt(7)\n"
    "ds_read_b64_tr_b16 a[64:65], v20 offset:4096\n"
    "s_waitcnt lgkmcnt(9)\n"
    "s_add_u32 m0, s67, 0x2000\n"
    "v_exp_f32 v160, v160\n"
    "v_exp_f32 v161, v161\n"
    "v_exp_f32 v162, v162\n"
    "v_cvt_pk_bf16_f32 v24, v160, v161\n"
    "v_exp_f32 v163, v163\n"
    "v_mfma_f32_32x32x16_bf16 v[96:111], v[236:239], v[28:31], v[96:111]\n"
    "ds_read_b64_tr_b16 a[66:67], v20 offset:5120\n"
    "buffer_load_dwordx4 v13, s[16:19], s91 offen lds\n"
    "v_exp_f32 v164, v164\n"
    "v_exp_f32 v165, v165\n"
    "v_cvt_pk_bf16_f32 v25, v162, v163\n"
    "v_exp_f32 v166, v166\n"
    "v_exp_f32 v167, v167\n"
    "v_cvt_pk_bf16_f32 v26, v164, v165\n"
    "v_mfma_f32_32x32x16_bf16 v[176:191], v[228:231], v[132:135], v[176:191]\n"
    "ds_read_b64_tr_b16 a[68:69], v21 offset:4096\n"
    "v_exp_f32 v168, v168\n"
    "v_exp_f32 v169, v169\n"
    "v_cvt_pk_bf16_f32 v27, v166, v167\n"
    "v_exp_f32 v170, v170\n"
    "v_exp_f32 v171, v171\n"
    "v_cvt_pk_bf16_f32 v28, v168, v169\n"
    "v_mfma_f32_32x32x16_bf16 v[112:127], v[232:235], v[24:27], v[112:127]\n"
    "ds_read_b64_tr_b16 a[70:71], v21 offset:5120\n"
    "buffer_load_dwordx4 v14, s[16:19], s91 offen offset:1024 lds\n"
    "v_exp_f32 v172, v172\n"
    "v_exp_f32 v173, v173\n"
    "v_cvt_pk_bf16_f32 v29, v170, v171\n"
    "v_exp_f32 v174, v174\n"
    "v_exp_f32 v175, v175\n"
    "v_cvt_pk_bf16_f32 v30, v172, v173\n"
    "v_cvt_pk_bf16_f32 v31, v174, v175\n"
    "v_mfma_f32_32x32x16_bf16 v[160:175], v[240:243], v[136:139], 0\n"
    "buffer_load_dwordx4 v[220:223], v1, s[12:15], s79 offen offset:3072 sc1\n"
    "ds_read_b64_tr_b16 a[72:73], v20 offset:6144\n"
    "v_exp_f32 v176, v176\n"
    "v_exp_f32 v177, v177\n"
    "v_exp_f32 v178, v178\n"
    "v_cvt_pk_bf16_f32 v24, v176, v177\n"
    "v_exp_f32 v179, v179\n"
    "v_mfma_f32_32x32x16_bf16 v[112:127], v[236:239], v[28:31], v[112:127]\n"
    "ds_read_b64_tr_b16 a[74:75], v20 offset:7168\n"
    "buffer_load_dwordx4 v15, s[16:19], s91 offen offset:2048 lds\n"
    "v_exp_f32 v180, v180\n"
    "v_exp_f32 v181, v181\n"
    "v_cvt_pk_bf16_f32 v25, v178, v179\n"
    "v_exp_f32 v182, v182\n"
    "v_exp_f32 v183, v183\n"
    "v_cvt_pk_bf16_f32 v26, v180, v181\n"
    "v_mfma_f32_32x32x16_bf16 v[160:175], v[244:247], v[140:143], v[160:175]\n"
    "ds_read_b64_tr_b16 a[76:77], v21 offset:6144\n"
    "v_exp_f32 v184, v184\n"
    "v_exp_f32 v185, v185\n"
    "v_cvt_pk_bf16_f32 v27, v182, v183\n"
    "v_exp_f32 v186, v186\n"
    "v_exp_f32 v187, v187\n"
    "v_cvt_pk_bf16_f32 v28, v184, v185\n"
    "v_mfma_f32_32x32x16_bf16 v[64:79], v[232:235], v[24:27], v[64:79]\n"
    "ds_read_b64_tr_b16 a[78:79], v21 offset:7168\n"
    "buffer_load_dwordx4 v16, s[16:19], s91 offen offset:3072 lds\n"
    "v_exp_f32 v188, v188\n"
    "v_exp_f32 v189, v189\n"
    "v_cvt_pk_bf16_f32 v29, v186, v187\n"
    "v_exp_f32 v190, v190\n"
    "v_exp_f32 v191, v191\n"
    "v_cvt_pk_bf16_f32 v30, v188, v189\n"
    "v_cvt_pk_bf16_f32 v31, v190, v191\n"
    "v_mfma_f32_32x32x16_bf16 v[176:191], v[240:243], v[144:147], 0\n"
    "buffer_load_dwordx4 v[224:227], v1, s[12:15], s80 offen sc1\n"
    "s_waitcnt lgkmcnt(8)\n"
    "s_add_u32 m0, s67, 0\n"
    "v_exp_f32 v160, v160\n"
    "v_exp_f32 v161, v161\n"
    "v_exp_f32 v162, v162\n"
    "v_cvt_pk_bf16_f32 v24, v160, v161\n"
    "v_exp_f32 v163, v163\n"
    "v_mfma_f32_32x32x16_bf16 v[64:79], v[236:239], v[28:31], v[64:79]\n"
    "buffer_load_dwordx4 v13, s[16:19], s92 offen lds\n"
    "v_exp_f32 v164, v164\n"
    "v_exp_f32 v165, v165\n"
    "v_cvt_pk_bf16_f32 v25, v162, v163\n"
    "v_exp_f32 v166, v166\n"
    "v_exp_f32 v167, v167\n"
    "v_cvt_pk_bf16_f32 v26, v164, v165\n"
    "v_mfma_f32_32x32x16_bf16 v[176:191], v[244:247], v[148:151], v[176:191]\n"
    "v_exp_f32 v168, v168\n"
    "v_exp_f32 v169, v169\n"
    "v_cvt_pk_bf16_f32 v27, v166, v167\n"
    "v_exp_f32 v170, v170\n"
    "v_exp_f32 v171, v171\n"
    "v_cvt_pk_bf16_f32 v28, v168, v169\n"
    "v_mfma_f32_32x32x16_bf16 v[80:95], v[248:251], v[24:27], v[80:95]\n"
    "buffer_load_dwordx4 v14, s[16:19], s92 offen offset:1024 lds\n"
    "v_exp_f32 v172, v172\n"
    "v_exp_f32 v173, v173\n"
    "v_cvt_pk_bf16_f32 v29, v170, v171\n"
    "v_exp_f32 v174, v174\n"
    "v_exp_f32 v175, v175\n"
    "v_cvt_pk_bf16_f32 v30, v172, v173\n"
    "v_cvt_pk_bf16_f32 v31, v174, v175\n"
    "v_mfma_f32_32x32x16_bf16 v[160:175], v[240:243], v[152:155], 0\n"
    "buffer_load_dwordx4 v[228:231], v1, s[12:15], s80 offen offset:1024 sc1\n"
    "v_exp_f32 v176, v176\n"
    "v_exp_f32 v177, v177\n"
    "v_exp_f32 v178, v178\n"
    "v_cvt_pk_bf16_f32 v24, v176, v177\n"
    "v_exp_f32 v179, v179\n"
    "v_mfma_f32_32x32x16_bf16 v[80:95], v[252:255], v[28:31], v[80:95]\n"
    "buffer_load_dwordx4 v15, s[16:19], s92 offen offset:2048 lds\n"
    "v_exp_f32 v180, v180\n"
    "v_exp_f32 v181, v181\n"
    "v_cvt_pk_bf16_f32 v25, v178, v179\n"
    "v_exp_f32 v182, v182\n"
    "v_exp_f32 v183, v183\n"
    "v_cvt_pk_bf16_f32 v26, v180, v181\n"
    "v_mfma_f32_32x32x16_bf16 v[160:175], v[244:247], v[156:159], v[160:175]\n"
    "v_exp_f32 v184, v184\n"
    "v_exp_f32 v185, v185\n"
    "v_cvt_pk_bf16_f32 v27, v182, v183\n"
    "v_exp_f32 v186, v186\n"
    "v_exp_f32 v187, v187\n"
    "v_cvt_pk_bf16_f32 v28, v184, v185\n"
    "v_mfma_f32_32x32x16_bf16 v[96:111], v[248:251], v[24:27], v[96:111]\n"
    "buffer_load_dwordx4 v16, s[16:19], s92 offen offset:3072 lds\n"
    "v_exp_f32 v188, v188\n"
    "v_exp_f32 v189, v189\n"
    "v_cvt_pk_bf16_f32 v29, v186, v187\n"
    "v_exp_f32 v190, v190\n"
    "v_exp_f32 v191, v191\n"
    "v_cvt_pk_bf16_f32 v30, v188, v189\n"
    "v_cvt_pk_bf16_f32 v31, v190, v191\n"
    "v_mfma_f32_32x32x16_bf16 v[176:191], v[240:243], v[128:131], 0\n"
    "buffer_load_dwordx4 v[232:235], v1, s[12:15], s80 offen offset:2048 sc1\n"
    "s_waitcnt lgkmcnt(0)\n"
    "s_add_u32 m0, s67, 0x1000\n"
    "v_exp_f32 v160, v160\n"
    "v_exp_f32 v161, v161\n"
    "v_exp_f32 v162, v162\n"
    "v_cvt_pk_bf16_f32 v24, v160, v161\n"
    "v_exp_f32 v163, v163\n"
    "v_mfma_f32_32x32x16_bf16 v[96:111], v[252:255], v[28:31], v[96:111]\n"
    "buffer_load_dwordx4 v13, s[16:19], s93 offen lds\n"
    "v_exp_f32 v164, v164\n"
    "v_exp_f32 v165, v165\n"
    "v_cvt_pk_bf16_f32 v25, v162, v163\n"
    "v_exp_f32 v166, v166\n"
    "v_exp_f32 v167, v167\n"
    "v_cvt_pk_bf16_f32 v26, v164, v165\n"
    "v_mfma_f32_32x32x16_bf16 v[176:191], v[244:247], v[132:135], v[176:191]\n"
    "v_exp_f32 v168, v168\n"
    "v_exp_f32 v169, v169\n"
    "v_cvt_pk_bf16_f32 v27, v166, v167\n"
    "v_exp_f32 v170, v170\n"
    "v_exp_f32 v171, v171\n"
    "v_cvt_pk_bf16_f32 v28, v168, v169\n"
    "v_mfma_f32_32x32x16_bf16 v[112:127], v[248:251], v[24:27], v[112:127]\n"
    "s_waitcnt vmcnt(16)\n"
    "buffer_load_dwordx4 v14, s[16:19], s93 offen offset:1024 lds\n"
    "v_exp_f32 v172, v172\n"
    "v_exp_f32 v173, v173\n"
    "v_cvt_pk_bf16_f32 v29, v170, v171\n"
    "v_exp_f32 v174, v174\n"
    "v_exp_f32 v175, v175\n"
    "v_cvt_pk_bf16_f32 v30, v172, v173\n"
    "v_cvt_pk_bf16_f32 v31, v174, v175\n"
    "v_mfma_f32_32x32x16_bf16 v[160:175], v[192:195], v[136:139], 0\n"
    "buffer_load_dwordx4 v[236:239], v1, s[12:15], s80 offen offset:3072 sc1\n"
    "v_exp_f32 v176, v176\n"
    "v_exp_f32 v177, v177\n"
    "v_exp_f32 v178, v178\n"
    "v_cvt_pk_bf16_f32 v24, v176, v177\n"
    "v_exp_f32 v179, v179\n"
    "v_mfma_f32_32x32x16_bf16 v[112:127], v[252:255], v[28:31], v[112:127]\n"
    "buffer_load_dwordx4 v15, s[16:19], s93 offen offset:2048 lds\n"
    "v_exp_f32 v180, v180\n"
    "v_exp_f32 v181, v181\n"
    "v_cvt_pk_bf16_f32 v25, v178, v179\n"
    "v_exp_f32 v182, v182\n"
    "v_exp_f32 v183, v183\n"
    "v_cvt_pk_bf16_f32 v26, v180, v181\n"
    "v_mfma_f32_32x32x16_bf16 v[160:175], v[196:199], v[140:143], v[160:175]\n"
    "v_exp_f32 v184, v184\n"
    "v_exp_f32 v185, v185\n"
    "v_cvt_pk_bf16_f32 v27, v182, v183\n"
    "v_exp_f32 v186, v186\n"
    "v_exp_f32 v187, v187\n"
    "v_cvt_pk_bf16_f32 v28, v184, v185\n"
    "v_mfma_f32_32x32x16_bf16 v[64:79], v[248:251], v[24:27], v[64:79]\n"
    "buffer_load_dwordx4 v16, s[16:19], s93 offen offset:3072 lds\n"
    "v_exp_f32 v188, v188\n"
    "v_exp_f32 v189, v189\n"
    "v_cvt_pk_bf16_f32 v29, v186, v187\n"
    "v_exp_f32 v190, v190\n"
    "v_exp_f32 v191, v191\n"
    "v_cvt_pk_bf16_f32 v30, v188, v189\n"
    "v_cvt_pk_bf16_f32 v31, v190, v191\n"
    "v_mfma_f32_32x32x16_bf16 v[176:191], v[192:195], v[144:147], 0\n"
    "buffer_load_dwordx4 v[240:243], v1, s[12:15], s81 offen sc1\n"
    "s_waitcnt vmcnt(13)\n"
    "ds_read_b64_tr_b16 a[80:81], v20 offset:8192\n"
    "s_add_u32 m0, s68, s1\n"
    "v_exp_f32 v160, v160\n"
    "v_exp_f32 v161, v161\n"
    "v_exp_f32 v162, v162\n"
    "v_cvt_pk_bf16_f32 v24, v160, v161\n"
    "v_exp_f32 v163, v163\n"
    "v_mfma_f32_32x32x16_bf16 v[64:79], v[252:255], v[28:31], v[64:79]\n"
    "ds_read_b64_tr_b16 a[82:83], v20 offset:9216\n"
    "buffer_load_dwordx4 v17, s[20:23], s66 offen lds\n"
    "v_exp_f32 v164, v164\n"
    "v_exp_f32 v165, v165\n"
    "v_cvt_pk_bf16_f32 v25, v162, v163\n"
    "v_exp_f32 v166, v166\n"
    "v_exp_f32 v167, v167\n"
    "v_cvt_pk_bf16_f32 v26, v164, v165\n"
    "v_mfma_f32_32x32x16_bf16 v[176:191], v[196:199], v[148:151], v[176:191]\n"
    "ds_read_b64_tr_b16 a[84:85], v21 offset:8192\n"
    "v_exp_f32 v168, v168\n"
    "v_exp_f32 v169, v169\n"
    "v_cvt_pk_bf16_f32 v27, v166, v167\n"
    "v_exp_f32 v170, v170\n"
    "v_exp_f32 v171, v171\n"
    "v_cvt_pk_bf16_f32 v28, v168, v169\n"
    "v_mfma_f32_32x32x16_bf16 v[80:95], v[200:203], v[24:27], v[80:95]\n"
    "ds_read_b64_tr_b16 a[86:87], v21 offset:9216\n"
    "buffer_load_dwordx4 v18, s[20:23], s66 offen offset:1024 lds\n"
    "v_exp_f32 v172, v172\n"
    "v_exp_f32 v173, v173\n"
    "v_cvt_pk_bf16_f32 v29, v170, v171\n"
    "v_exp_f32 v174, v174\n"
    "v_exp_f32 v175, v175\n"
    "v_cvt_pk_bf16_f32 v30, v172, v173\n"
    "v_cvt_pk_bf16_f32 v31, v174, v175\n"
    "v_mfma_f32_32x32x16_bf16 v[160:175], v[192:195], v[152:155], 0\n"
    "buffer_load_dwordx4 v[244:247], v1, s[12:15], s81 offen offset:1024 sc1\n"
    "ds_read_b64_tr_b16 a[88:89], v20 offset:10240\n"
    "v_exp_f32 v176, v176\n"
    "v_exp_f32 v177, v177\n"
    "v_exp_f32 v178, v178\n"
    "v_cvt_pk_bf16_f32 v24, v176, v177\n"
    "v_exp_f32 v179, v179\n"
    "v_mfma_f32_32x32x16_bf16 v[80:95], v[204:207], v[28:31], v[80:95]\n"
    "ds_read_b64_tr_b16 a[90:91], v20 offset:11264\n"
    "buffer_load_dwordx4 v19, s[20:23], s66 offen offset:2048 lds\n"
    "v_exp_f32 v180, v180\n"
    "v_exp_f32 v181, v181\n"
    "v_cvt_pk_bf16_f32 v25, v178, v179\n"
    "v_exp_f32 v182, v182\n"
    "v_exp_f32 v183, v183\n"
    "v_cvt_pk_bf16_f32 v26, v180, v181\n"
    "v_mfma_f32_32x32x16_bf16 v[160:175], v[196:199], v[156:159], v[160:175]\n"
    "ds_read_b64_tr_b16 a[92:93], v21 offset:10240\n"
    "v_exp_f32 v184, v184\n"
    "v_exp_f32 v185, v185\n"
    "v_cvt_pk_bf16_f32 v27, v182, v183\n"
    "v_exp_f32 v186, v186\n"
    "v_exp_f32 v187, v187\n"
    "v_cvt_pk_bf16_f32 v28, v184, v185\n"
    "v_mfma_f32_32x32x16_bf16 v[96:111], v[200:203], v[24:27], v[96:111]\n"
    "ds_read_b64_tr_b16 a[94:95], v21 offset:11264\n"
    "buffer_load_dwordx4 v10, s[20:23], s66 offen offset:3072 lds\n"
    "s_add_u32 s1, s1, 0x1000\n"
    "s_add_u32 s66, s66, 0x7000\n"
    "s_cmp_eq_u32 s1, 0x7000\n"
    "s_cselect_b32 s1, 0, s1\n"
    "s_cselect_b32 s66, 0, s66\n"
    "v_exp_f32 v188, v188\n"
    "v_exp_f32 v189, v189\n"
    "v_cvt_pk_bf16_f32 v29, v186, v187\n"
    "v_exp_f32 v190, v190\n"
    "v_exp_f32 v191, v191\n"
    "v_cvt_pk_bf16_f32 v30, v188, v189\n"
    "v_cvt_pk_bf16_f32 v31, v190, v191\n"
    "v_mfma_f32_32x32x16_bf16 v[176:191], v[192:195], v[128:131], 0\n"
    "buffer_load_dwordx4 v[248:251], v1, s[12:15], s81 offen offset:2048 sc1\n"
    "s_waitcnt vmcnt(13)\n"
    "ds_read_b64_tr_b16 a[96:97], v20\n"
    "s_waitcnt lgkmcnt(1)\n"
    "s_add_u32 m0, s67, 0x2000\n"
    "v_exp_f32 v160, v160\n"
    "v_exp_f32 v161, v161\n"
    "v_exp_f32 v162, v162\n"
    "v_cvt_pk_bf16_f32 v24, v160, v161\n"
    "v_exp_f32 v163, v163\n"
    "v_mfma_f32_32x32x16_bf16 v[96:111], v[204:207], v[28:31], v[96:111]\n"
    "ds_read_b64_tr_b16 a[98:99], v20 offset:1024\n"
    "buffer_load_dwordx4 v13, s[16:19], s94 offen lds\n"
    "v_exp_f32 v164, v164\n"
    "v_exp_f32 v165, v165\n"
    "v_cvt_pk_bf16_f32 v25, v162, v163\n"
    "v_exp_f32 v166, v166\n"
    "v_exp_f32 v167, v167\n"
    "v_cvt_pk_bf16_f32 v26, v164, v165\n"
    "v_mfma_f32_32x32x16_bf16 v[176:191], v[196:199], v[132:135], v[176:191]\n"
    "ds_read_b64_tr_b16 a[100:101], v21\n"
    "v_exp_f32 v168, v168\n"
    "v_exp_f32 v169, v169\n"
    "v_cvt_pk_bf16_f32 v27, v166, v167\n"
    "v_exp_f32 v170, v170\n"
    "v_exp_f32 v171, v171\n"
    "v_cvt_pk_bf16_f32 v28, v168, v169\n"
    "v_mfma_f32_32x32x16_bf16 v[112:127], v[200:203], v[24:27], v[112:127]\n"
    "ds_read_b64_tr_b16 a[102:103], v21 offset:1024\n"
    "buffer_load_dwordx4 v14, s[16:19], s94 offen offset:1024 lds\n"
    "v_exp_f32 v172, v172\n"
    "v_exp_f32 v173, v173\n"
    "v_cvt_pk_bf16_f32 v29, v170, v171\n"
    "v_exp_f32 v174, v174\n"
    "v_exp_f32 v175, v175\n"
    "v_cvt_pk_bf16_f32 v30, v172, v173\n"
    "v_cvt_pk_bf16_f32 v31, v174, v175\n"
    "v_mfma_f32_32x32x16_bf16 v[160:175], v[208:211], v[136:139], 0\n"
    "buffer_load_dwordx4 v[252:255], v1, s[12:15], s81 offen offset:3072 sc1\n"
    "ds_read_b64_tr_b16 a[104:105], v20 offset:2048\n"
    "v_exp_f32 v176, v176\n"
    "v_exp_f32 v177, v177\n"
    "v_exp_f32 v178, v178\n"
    "v_cvt_pk_bf16_f32 v24, v176, v177\n"
    "v_exp_f32 v179, v179\n"
    "v_mfma_f32_32x32x16_bf16 v[112:127], v[204:207], v[28:31], v[112:127]\n"
    "ds_read_b64_tr_b16 a[106:107], v20 offset:3072\n"
    "buffer_load_dwordx4 v15, s[16:19], s94 offen offset:2048 lds\n"
    "v_exp_f32 v180, v180\n"
    "v_exp_f32 v181, v181\n"
    "v_cvt_pk_bf16_f32 v25, v178, v179\n"
    "v_exp_f32 v182, v182\n"
    "v_exp_f32 v183, v183\n"
    "v_cvt_pk_bf16_f32 v26, v180, v181\n"
    "v_mfma_f32_32x32x16_bf16 v[160:175], v[212:215], v[140:143], v[160:175]\n"
    "ds_read_b64_tr_b16 a[108:109], v21 offset:2048\n"
    "v_exp_f32 v184, v184\n"
    "v_exp_f32 v185, v185\n"
    "v_cvt_pk_bf16_f32 v27, v182, v183\n"
    "v_exp_f32 v186, v186\n"
    "v_exp_f32 v187, v187\n"
    "v_cvt_pk_bf16_f32 v28, v184, v185\n"
    "v_mfma_f32_32x32x16_bf16 v[64:79], v[200:203], v[24:27], v[64:79]\n"
    "ds_read_b64_tr_b16 a[110:111], v21 offset:3072\n"
    "buffer_load_dwordx4 v16, s[16:19], s94 offen offset:3072 lds\n"
    "v_exp_f32 v188, v188\n"
    "v_exp_f32 v189, v189\n"
    "v_cvt_pk_bf16_f32 v29, v186, v187\n"
    "v_exp_f32 v190, v190\n"
    "v_exp_f32 v191, v191\n"
    "v_cvt_pk_bf16_f32 v30, v188, v189\n"
    "v_cvt_pk_bf16_f32 v31, v190, v191\n"
    "v_mfma_f32_32x32x16_bf16 v[176:191], v[208:211], v[144:147], 0\n"
    "buffer_load_dwordx4 v[192:195], v1, s[12:15], s82 offen sc1\n"
    "s_waitcnt vmcnt(13)\n"
    "ds_read_b64_tr_b16 a[112:113], v20 offset:4096\n"
    "s_waitcnt lgkmcnt(1)\n"
    "s_add_u32 m0, s67, 0\n"
    "v_exp_f32 v160, v160\n"
    "v_exp_f32 v161, v161\n"
    "v_exp_f32 v162, v162\n"
    "v_cvt_pk_bf16_f32 v24, v160, v161\n"
    "v_exp_f32 v163, v163\n"
    "v_mfma_f32_32x32x16_bf16 v[64:79], v[204:207], v[28:31], v[64:79]\n"
    "ds_read_b64_tr_b16 a[114:115], v20 offset:5120\n"
    "buffer_load_dwordx4 v13, s[16:19], s95 offen lds\n"
    "v_exp_f32 v164, v164\n"
    "v_exp_f32 v165, v165\n"
    "v_cvt_pk_bf16_f32 v25, v162, v163\n"
    "v_exp_f32 v166, v166\n"
    "v_exp_f32 v167, v167\n"
    "v_cvt_pk_bf16_f32 v26, v164, v165\n"
    "v_mfma_f32_32x32x16_bf16 v[176:191], v[212:215], v[148:151], v[176:191]\n"
    "ds_read_b64_tr_b16 a[116:117], v21 offset:4096\n"
    "v_exp_f32 v168, v168\n"
    "v_exp_f32 v169, v169\n"
    "v_cvt_pk_bf16_f32 v27, v166, v167\n"
    "v_exp_f32 v170, v170\n"
    "v_exp_f32 v171, v171\n"
    "v_cvt_pk_bf16_f32 v28, v168, v169\n"
    "v_mfma_f32_32x32x16_bf16 v[80:95], v[216:219], v[24:27], v[80:95]\n"
    "ds_read_b64_tr_b16 a[118:119], v21 offset:5120\n"
    "buffer_load_dwordx4 v14, s[16:19], s95 offen offset:1024 lds\n"
    "v_exp_f32 v172, v172\n"
    "v_exp_f32 v173, v173\n"
    "v_cvt_pk_bf16_f32 v29, v170, v171\n"
    "v_exp_f32 v174, v174\n"
    "v_exp_f32 v175, v175\n"
    "v_cvt_pk_bf16_f32 v30, v172, v173\n"
    "v_cvt_pk_bf16_f32 v31, v174, v175\n"
    "v_mfma_f32_32x32x16_bf16 v[160:175], v[208:211], v[152:155], 0\n"
    "buffer_load_dwordx4 v[196:199], v1, s[12:15], s82 offen offset:1024 sc1\n"
    "ds_read_b64_tr_b16 a[120:121], v20 offset:6144\n"
    "v_exp_f32 v176, v176\n"
    "v_exp_f32 v177, v177\n"
    "v_exp_f32 v178, v178\n"
    "v_cvt_pk_bf16_f32 v24, v176, v177\n"
    "v_exp_f32 v179, v179\n"
    "v_mfma_f32_32x32x16_bf16 v[80:95], v[220:223], v[28:31], v[80:95]\n"
    "ds_read_b64_tr_b16 a[122:123], v20 offset:7168\n"
    "buffer_load_dwordx4 v15, s[16:19], s95 offen offset:2048 lds\n"
    "v_exp_f32 v180, v180\n"
    "v_exp_f32 v181, v181\n"
    "v_cvt_pk_bf16_f32 v25, v178, v179\n"
    "v_exp_f32 v182, v182\n"
    "v_exp_f32 v183, v183\n"
    "v_cvt_pk_bf16_f32 v26, v180, v181\n"
    "v_mfma_f32_32x32x16_bf16 v[160:175], v[212:215], v[156:159], v[160:175]\n"
    "ds_read_b64_tr_b16 a[124:125], v21 offset:6144\n"
    "v_exp_f32 v184, v184\n"
    "v_exp_f32 v185, v185\n"
    "v_cvt_pk_bf16_f32 v27, v182, v183\n"
    "v_exp_f32 v186, v186\n"
    "v_exp_f32 v187, v187\n"
    "v_cvt_pk_bf16_f32 v28, v184, v185\n"
    "v_mfma_f32_32x32x16_bf16 v[96:111], v[216:219], v[24:27], v[96:111]\n"
    "ds_read_b64_tr_b16 a[126:127], v21 offset:7168\n"
    "buffer_load_dwordx4 v16, s[16:19], s95 offen offset:3072 lds\n"
    "v_exp_f32 v188, v188\n"
    "v_exp_f32 v189, v189\n"
    "v_cvt_pk_bf16_f32 v29, v186, v187\n"
    "v_exp_f32 v190, v190\n"
    "v_exp_f32 v191, v191\n"
    "v_cvt_pk_bf16_f32 v30, v188, v189\n"
    "v_cvt_pk_bf16_f32 v31, v190, v191\n"
    "v_mfma_f32_32x32x16_bf16 v[176:191], v[208:211], v[128:131], 0\n"
    "buffer_load_dwordx4 v[200:203], v1, s[12:15], s82 offen offset:2048 sc1\n"
    "s_waitcnt lgkmcnt(0)\n"
    "s_add_u32 m0, s67, 0x1000\n"
    "v_exp_f32 v160, v160\n"
    "v_exp_f32 v161, v161\n"
    "v_exp_f32 v162, v162\n"
    "v_cvt_pk_bf16_f32 v24, v160, v161\n"
    "v_exp_f32 v163, v163\n"
    "v_mfma_f32_32x32x16_bf16 v[96:111], v[220:223], v[28:31], v[96:111]\n"
    "buffer_load_dwordx4 v13, s[16:19], s96 offen lds\n"
    "v_exp_f32 v164, v164\n"
    "v_exp_f32 v165, v165\n"
    "v_cvt_pk_bf16_f32 v25, v162, v163\n"
    "v_exp_f32 v166, v166\n"
    "v_exp_f32 v167, v167\n"
    "v_cvt_pk_bf16_f32 v26, v164, v165\n"
    "v_mfma_f32_32x32x16_bf16 v[176:191], v[212:215], v[132:135], v[176:191]\n"
    "v_exp_f32 v168, v168\n"
    "v_exp_f32 v169, v169\n"
    "v_cvt_pk_bf16_f32 v27, v166, v167\n"
    "v_exp_f32 v170, v170\n"
    "v_exp_f32 v171, v171\n"
    "v_cvt_pk_bf16_f32 v28, v168, v169\n"
    "v_mfma_f32_32x32x16_bf16 v[112:127], v[216:219], v[24:27], v[112:127]\n"
    "buffer_load_dwordx4 v14, s[16:19], s96 offen offset:1024 lds\n"
    "v_exp_f32 v172, v172\n"
    "v_exp_f32 v173, v173\n"
    "v_cvt_pk_bf16_f32 v29, v170, v171\n"
    "v_exp_f32 v174, v174\n"
    "v_exp_f32 v175, v175\n"
    "v_cvt_pk_bf16_f32 v30, v172, v173\n"
    "v_cvt_pk_bf16_f32 v31, v174, v175\n"
    "v_mfma_f32_32x32x16_bf16 v[160:175], v[224:227], v[136:139], 0\n"
    "buffer_load_dwordx4 v[204:207], v1, s[12:15], s82 offen offset:3072 sc1\n"
    "v_exp_f32 v176, v176\n"
    "v_exp_f32 v177, v177\n"
    "v_exp_f32 v178, v178\n"
    "v_cvt_pk_bf16_f32 v24, v176, v177\n"
    "v_exp_f32 v179, v179\n"
    "v_mfma_f32_32x32x16_bf16 v[112:127], v[220:223], v[28:31], v[112:127]\n"
    "buffer_load_dwordx4 v15, s[16:19], s96 offen offset:2048 lds\n"
    "v_exp_f32 v180, v180\n"
    "v_exp_f32 v181, v181\n"
    "v_cvt_pk_bf16_f32 v25, v178, v179\n"
    "v_exp_f32 v182, v182\n"
    "v_exp_f32 v183, v183\n"
    "v_cvt_pk_bf16_f32 v26, v180, v181\n"
    "v_mfma_f32_32x32x16_bf16 v[160:175], v[228:231], v[140:143], v[160:175]\n"
    "v_exp_f32 v184, v184\n"
    "v_exp_f32 v185, v185\n"
    "v_cvt_pk_bf16_f32 v27, v182, v183\n"
    "v_exp_f32 v186, v186\n"
    "v_exp_f32 v187, v187\n"
    "v_cvt_pk_bf16_f32 v28, v184, v185\n"
    "v_mfma_f32_32x32x16_bf16 v[64:79], v[216:219], v[24:27], v[64:79]\n"
    "buffer_load_dwordx4 v16, s[16:19], s96 offen offset:3072 lds\n"
    "v_exp_f32 v188, v188\n"
    "v_exp_f32 v189, v189\n"
    "v_cvt_pk_bf16_f32 v29, v186, v187\n"
    "v_exp_f32 v190, v190\n"
    "v_exp_f32 v191, v191\n"
    "v_cvt_pk_bf16_f32 v30, v188, v189\n"
    "v_cvt_pk_bf16_f32 v31, v190, v191\n"
    "v_mfma_f32_32x32x16_bf16 v[176:191], v[224:227], v[144:147], 0\n"
    "buffer_load_dwordx4 v[208:211], v1, s[12:15], s83 offen sc1\n"
    "s_waitcnt vmcnt(13)\n"
    "ds_read_b64_tr_b16 a[128:129], v20 offset:8192\n"
    "s_add_u32 m0, s68, s1\n"
    "v_exp_f32 v160, v160\n"
    "v_exp_f32 v161, v161\n"
    "v_exp_f32 v162, v162\n"
    "v_cvt_pk_bf16_f32 v24, v160, v161\n"
    "v_exp_f32 v163, v163\n"
    "v_mfma_f32_32x32x16_bf16 v[64:79], v[220:223], v[28:31], v[64:79]\n"
    "ds_read_b64_tr_b16 a[130:131], v20 offset:9216\n"
    "buffer_load_dwordx4 v17, s[20:23], s66 offen lds\n"
    "v_exp_f32 v164, v164\n"
    "v_exp_f32 v165, v165\n"
    "v_cvt_pk_bf16_f32 v25, v162, v163\n"
    "v_exp_f32 v166, v166\n"
    "v_exp_f32 v167, v167\n"
    "v_cvt_pk_bf16_f32 v26, v164, v165\n"
    "v_mfma_f32_32x32x16_bf16 v[176:191], v[228:231], v[148:151], v[176:191]\n"
    "ds_read_b64_tr_b16 a[132:133], v21 offset:8192\n"
    "v_exp_f32 v168, v168\n"
    "v_exp_f32 v169, v169\n"
    "v_cvt_pk_bf16_f32 v27, v166, v167\n"
    "v_exp_f32 v170, v170\n"
    "v_exp_f32 v171, v171\n"
    "v_cvt_pk_bf16_f32 v28, v168, v169\n"
    "v_mfma_f32_32x32x16_bf16 v[80:95], v[232:235], v[24:27], v[80:95]\n"
    "ds_read_b64_tr_b16 a[134:135], v21 offset:9216\n"
    "buffer_load_dwordx4 v18, s[20:23], s66 offen offset:1024 lds\n"
    "v_exp_f32 v172, v172\n"
    "v_exp_f32 v173, v173\n"
    "v_cvt_pk_bf16_f32 v29, v170, v171\n"
    "v_exp_f32 v174, v174\n"
    "v_exp_f32 v175, v175\n"
    "v_cvt_pk_bf16_f32 v30, v172, v173\n"
    "v_cvt_pk_bf16_f32 v31, v174, v175\n"
    "v_mfma_f32_32x32x16_bf16 v[160:175], v[224:227], v[152:155], 0\n"
    "buffer_load_dwordx4 v[212:215], v1, s[12:15], s83 offen offset:1024 sc1\n"
    "ds_read_b64_tr_b16 a[136:137], v20 offset:10240\n"
    "v_exp_f32 v176, v176\n"
    "v_exp_f32 v177, v177\n"
    "v_exp_f32 v178, v178\n"
    "v_cvt_pk_bf16_f32 v24, v176, v177\n"
    "v_exp_f32 v179, v179\n"
    "v_mfma_f32_32x32x16_bf16 v[80:95], v[236:239], v[28:31], v[80:95]\n"
    "ds_read_b64_tr_b16 a[138:139], v20 offset:11264\n"
    "buffer_load_dwordx4 v19, s[20:23], s66 offen offset:2048 lds\n"
    "v_exp_f32 v180, v180\n"
    "v_exp_f32 v181, v181\n"
    "v_cvt_pk_bf16_f32 v25, v178, v179\n"
    "v_exp_f32 v182, v182\n"
    "v_exp_f32 v183, v183\n"
    "v_cvt_pk_bf16_f32 v26, v180, v181\n"
    "v_mfma_f32_32x32x16_bf16 v[160:175], v[228:231], v[156:159], v[160:175]\n"
    "ds_read_b64_tr_b16 a[140:141], v21 offset:10240\n"
    "v_exp_f32 v184, v184\n"
    "v_exp_f32 v185, v185\n"
    "v_cvt_pk_bf16_f32 v27, v182, v183\n"
    "v_exp_f32 v186, v186\n"
    "v_exp_f32 v187, v187\n"
    "v_cvt_pk_bf16_f32 v28, v184, v185\n"
    "v_mfma_f32_32x32x16_bf16 v[96:111], v[232:235], v[24:27], v[96:111]\n"
    "ds_read_b64_tr_b16 a[142:143], v21 offset:11264\n"
    "buffer_load_dwordx4 v10, s[20:23], s66 offen offset:3072 lds\n"
    "s_add_u32 s1, s1, 0x1000\n"
    "s_add_u32 s66, s66, 0x7000\n"
    "s_cmp_eq_u32 s1, 0x7000\n"
    "s_cselect_b32 s1, 0, s1\n"
    "s_cselect_b32 s66, 0, s66\n"
    "v_exp_f32 v188, v188\n"
    "v_exp_f32 v189, v189\n"
    "v_cvt_pk_bf16_f32 v29, v186, v187\n"
    "v_exp_f32 v190, v190\n"
    "v_exp_f32 v191, v191\n"
    "v_cvt_pk_bf16_f32 v30, v188, v189\n"
    "v_cvt_pk_bf16_f32 v31, v190, v191\n"
    "v_mfma_f32_32x32x16_bf16 v[176:191], v[224:227], v[128:131], 0\n"
    "buffer_load_dwordx4 v[216:219], v1, s[12:15], s83 offen offset:2048 sc1\n"
    "s_waitcnt vmcnt(13)\n"
    "ds_read_b64_tr_b16 a[144:145], v20\n"
    "s_waitcnt lgkmcnt(1)\n"
    "s_add_u32 m0, s67, 0x2000\n"
    "v_exp_f32 v160, v160\n"
    "v_exp_f32 v161, v161\n"
    "v_exp_f32 v162, v162\n"
    "v_cvt_pk_bf16_f32 v24, v160, v161\n"
    "v_exp_f32 v163, v163\n"
    "v_mfma_f32_32x32x16_bf16 v[96:111], v[236:239], v[28:31], v[96:111]\n"
    "ds_read_b64_tr_b16 a[146:147], v20 offset:1024\n"
    "buffer_load_dwordx4 v13, s[16:19], s97 offen lds\n"
    "v_exp_f32 v164, v164\n"
    "v_exp_f32 v165, v165\n"
    "v_cvt_pk_bf16_f32 v25, v162, v163\n"
    "v_exp_f32 v166, v166\n"
    "v_exp_f32 v167, v167\n"
    "v_cvt_pk_bf16_f32 v26, v164, v165\n"
    "v_mfma_f32_32x32x16_bf16 v[176:191], v[228:231], v[132:135], v[176:191]\n"
    "ds_read_b64_tr_b16 a[148:149], v21\n"
    "v_exp_f32 v168, v168\n"
    "v_exp_f32 v169, v169\n"
    "v_cvt_pk_bf16_f32 v27, v166, v167\n"
    "v_exp_f32 v170, v170\n"
    "v_exp_f32 v171, v171\n"
    "v_cvt_pk_bf16_f32 v28, v168, v169\n"
    "v_mfma_f32_32x32x16_bf16 v[112:127], v[232:235], v[24:27], v[112:127]\n"
    "ds_read_b64_tr_b16 a[150:151], v21 offset:1024\n"
    "buffer_load_dwordx4 v14, s[16:19], s97 offen offset:1024 lds\n"
    "v_exp_f32 v172, v172\n"
    "v_exp_f32 v173, v173\n"
    "v_cvt_pk_bf16_f32 v29, v170, v171\n"
    "v_exp_f32 v174, v174\n"
    "v_exp_f32 v175, v175\n"
    "v_cvt_pk_bf16_f32 v30, v172, v173\n"
    "v_cvt_pk_bf16_f32 v31, v174, v175\n"
    "v_mfma_f32_32x32x16_bf16 v[160:175], v[240:243], v[136:139], 0\n"
    "buffer_load_dwordx4 v[220:223], v1, s[12:15], s83 offen offset:3072 sc1\n"
    "ds_read_b64_tr_b16 a[152:153], v20 offset:2048\n"
    "v_exp_f32 v176, v176\n"
    "v_exp_f32 v177, v177\n"
    "v_exp_f32 v178, v178\n"
    "v_cvt_pk_bf16_f32 v24, v176, v177\n"
    "v_exp_f32 v179, v179\n"
    "v_mfma_f32_32x32x16_bf16 v[112:127], v[236:239], v[28:31], v[112:127]\n"
    "ds_read_b64_tr_b16 a[154:155], v20 offset:3072\n"
    "buffer_load_dwordx4 v15, s[16:19], s97 offen offset:2048 lds\n"
    "v_exp_f32 v180, v180\n"
    "v_exp_f32 v181, v181\n"
    "v_cvt_pk_bf16_f32 v25, v178, v179\n"
    "v_exp_f32 v182, v182\n"
    "v_exp_f32 v183, v183\n"
    "v_cvt_pk_bf16_f32 v26, v180, v181\n"
    "v_mfma_f32_32x32x16_bf16 v[160:175], v[244:247], v[140:143], v[160:175]\n"
    "ds_read_b64_tr_b16 a[156:157], v21 offset:2048\n"
    "v_exp_f32 v184, v184\n"
    "v_exp_f32 v185, v185\n"
    "v_cvt_pk_bf16_f32 v27, v182, v183\n"
    "v_exp_f32 v186, v186\n"
    "v_exp_f32 v187, v187\n"
    "v_cvt_pk_bf16_f32 v28, v184, v185\n"
    "v_mfma_f32_32x32x16_bf16 v[64:79], v[232:235], v[24:27], v[64:79]\n"
    "ds_read_b64_tr_b16 a[158:159], v21 offset:3072\n"
    "buffer_load_dwordx4 v16, s[16:19], s97 offen offset:3072 lds\n"
    "v_exp_f32 v188, v188\n"
    "v_exp_f32 v189, v189\n"
    "v_cvt_pk_bf16_f32 v29, v186, v187\n"
    "v_exp_f32 v190, v190\n"
    "v_exp_f32 v191, v191\n"
    "v_cvt_pk_bf16_f32 v30, v188, v189\n"
    "v_cvt_pk_bf16_f32 v31, v190, v191\n"
    "v_mfma_f32_32x32x16_bf16 v[176:191], v[240:243], v[144:147], 0\n"
    "buffer_load_dwordx4 v[224:227], v1, s[12:15], s84 offen sc1\n"
    "s_waitcnt vmcnt(13)\n"
    "ds_read_b64_tr_b16 a[160:161], v20 offset:4096\n"
    "s_waitcnt lgkmcnt(1)\n"
    "s_add_u32 m0, s67, 0\n"
    "v_exp_f32 v160, v160\n"
    "v_exp_f32 v161, v161\n"
    "v_exp_f32 v162, v162\n"
    "v_cvt_pk_bf16_f32 v24, v160, v161\n"
    "v_exp_f32 v163, v163\n"
    "v_mfma_f32_32x32x16_bf16 v[64:79], v[236:239], v[28:31], v[64:79]\n"
    "ds_read_b64_tr_b16 a[162:163], v20 offset:5120\n"
    "buffer_load_dwordx4 v13, s[16:19], s98 offen lds\n"
    "v_exp_f32 v164, v164\n"
    "v_exp_f32 v165, v165\n"
    "v_cvt_pk_bf16_f32 v25, v162, v163\n"
    "v_exp_f32 v166, v166\n"
    "v_exp_f32 v167, v167\n"
    "v_cvt_pk_bf16_f32 v26, v164, v165\n"
    "v_mfma_f32_32x32x16_bf16 v[176:191], v[244:247], v[148:151], v[176:191]\n"
    "ds_read_b64_tr_b16 a[164:165], v21 offset:4096\n"
    "v_exp_f32 v168, v168\n"
    "v_exp_f32 v169, v169\n"
    "v_cvt_pk_bf16_f32 v27, v166, v167\n"
    "v_exp_f32 v170, v170\n"
    "v_exp_f32 v171, v171\n"
    "v_cvt_pk_bf16_f32 v28, v168, v169\n"
    "v_mfma_f32_32x32x16_bf16 v[80:95], v[248:251], v[24:27], v[80:95]\n"
    "ds_read_b64_tr_b16 a[166:167], v21 offset:5120\n"
    "buffer_load_dwordx4 v14, s[16:19], s98 offen offset:1024 lds\n"
    "v_exp_f32 v172, v172\n"
    "v_exp_f32 v173, v173\n"
    "v_cvt_pk_bf16_f32 v29, v170, v171\n"
    "v_exp_f32 v174, v174\n"
    "v_exp_f32 v175, v175\n"
    "v_cvt_pk_bf16_f32 v30, v172, v173\n"
    "v_cvt_pk_bf16_f32 v31, v174, v175\n"
    "v_mfma_f32_32x32x16_bf16 v[160:175], v[240:243], v[152:155], 0\n"
    "buffer_load_dwordx4 v[228:231], v1, s[12:15], s84 offen offset:1024 sc1\n"
    "ds_read_b64_tr_b16 a[168:169], v20 offset:6144\n"
    "v_exp_f32 v176, v176\n"
    "v_exp_f32 v177, v177\n"
    "v_exp_f32 v178, v178\n"
    "v_cvt_pk_bf16_f32 v24, v176, v177\n"
    "v_exp_f32 v179, v179\n"
    "v_mfma_f32_32x32x16_bf16 v[80:95], v[252:255], v[28:31], v[80:95]\n"
    "ds_read_b64_tr_b16 a[170:171], v20 offset:7168\n"
    "buffer_load_dwordx4 v15, s[16:19], s98 offen offset:2048 lds\n"
    "v_exp_f32 v180, v180\n"
    "v_exp_f32 v181, v181\n"
    "v_cvt_pk_bf16_f32 v25, v178, v179\n"
    "v_exp_f32 v182, v182\n"
    "v_exp_f32 v183, v183\n"
    "v_cvt_pk_bf16_f32 v26, v180, v181\n"
    "v_mfma_f32_32x32x16_bf16 v[160:175], v[244:247], v[156:159], v[160:175]\n"
    "ds_read_b64_tr_b16 a[172:173], v21 offset:6144\n"
    "buffer_load_dwordx2 v[42:43], v41, s[12:15], 0 offen sc1\n"
    "v_exp_f32 v184, v184\n"
    "v_exp_f32 v185, v185\n"
    "v_cvt_pk_bf16_f32 v27, v182, v183\n"
    "v_exp_f32 v186, v186\n"
    "v_exp_f32 v187, v187\n"
    "v_cvt_pk_bf16_f32 v28, v184, v185\n"
    "v_mfma_f32_32x32x16_bf16 v[96:111], v[248:251], v[24:27], v[96:111]\n"
    "ds_read_b64_tr_b16 a[174:175], v21 offset:7168\n"
    "buffer_load_dwordx4 v16, s[16:19], s98 offen offset:3072 lds\n"
    "v_exp_f32 v188, v188\n"
    "v_exp_f32 v189, v189\n"
    "v_cvt_pk_bf16_f32 v29, v186, v187\n"
    "v_exp_f32 v190, v190\n"
    "v_exp_f32 v191, v191\n"
    "v_cvt_pk_bf16_f32 v30, v188, v189\n"
    "v_cvt_pk_bf16_f32 v31, v190, v191\n"
    "v_mfma_f32_32x32x16_bf16 v[176:191], v[240:243], v[128:131], 0\n"
    "buffer_load_dwordx4 v[232:235], v1, s[12:15], s84 offen offset:2048 sc1\n"
    "s_waitcnt lgkmcnt(0)\n"
    "s_add_u32 m0, s67, 0x1000\n"
    "v_exp_f32 v160, v160\n"
    "v_exp_f32 v161, v161\n"
    "v_exp_f32 v162, v162\n"
    "v_cvt_pk_bf16_f32 v24, v160, v161\n"
    "v_exp_f32 v163, v163\n"
    "v_mfma_f32_32x32x16_bf16 v[96:111], v[252:255], v[28:31], v[96:111]\n"
    "buffer_load_dwordx4 v13, s[16:19], s99 offen lds\n"
    "v_exp_f32 v164, v164\n"
    "v_exp_f32 v165, v165\n"
    "v_cvt_pk_bf16_f32 v25, v162, v163\n"
    "v_exp_f32 v166, v166\n"
    "v_exp_f32 v167, v167\n"
    "v_cvt_pk_bf16_f32 v26, v164, v165\n"
    "v_mfma_f32_32x32x16_bf16 v[176:191], v[244:247], v[132:135], v[176:191]\n"
    "v_exp_f32 v168, v168\n"
    "v_exp_f32 v169, v169\n"
    "v_cvt_pk_bf16_f32 v27, v166, v167\n"
    "v_exp_f32 v170, v170\n"
    "v_exp_f32 v171, v171\n"
    "v_cvt_pk_bf16_f32 v28, v168, v169\n"
    "v_mfma_f32_32x32x16_bf16 v[112:127], v[248:251], v[24:27], v[112:127]\n"
    "buffer_load_dwordx4 v14, s[16:19], s99 offen offset:1024 lds\n"
    "v_exp_f32 v172, v172\n"
    "v_exp_f32 v173, v173\n"
    "v_cvt_pk_bf16_f32 v29, v170, v171\n"
    "v_exp_f32 v174, v174\n"
    "v_exp_f32 v175, v175\n"
    "v_cvt_pk_bf16_f32 v30, v172, v173\n"
    "v_cvt_pk_bf16_f32 v31, v174, v175\n"
    "v_mfma_f32_32x32x16_bf16 v[160:175], v[192:195], v[136:139], 0\n"
    "buffer_load_dwordx4 v[236:239], v1, s[12:15], s84 offen offset:3072 sc1\n"
    "v_exp_f32 v176, v176\n"
    "v_exp_f32 v177, v177\n"
    "v_exp_f32 v178, v178\n"
    "v_cvt_pk_bf16_f32 v24, v176, v177\n"
    "v_exp_f32 v179, v179\n"
    "v_mfma_f32_32x32x16_bf16 v[112:127], v[252:255], v[28:31], v[112:127]\n"
    "buffer_load_dwordx4 v15, s[16:19], s99 offen offset:2048 lds\n"
    "v_exp_f32 v180, v180\n"
    "v_exp_f32 v181, v181\n"
    "v_cvt_pk_bf16_f32 v25, v178, v179\n"
    "v_exp_f32 v182, v182\n"
    "v_exp_f32 v183, v183\n"
    "v_cvt_pk_bf16_f32 v26, v180, v181\n"
    "v_mfma_f32_32x32x16_bf16 v[160:175], v[196:199], v[140:143], v[160:175]\n"
    "v_exp_f32 v184, v184\n"
    "v_exp_f32 v185, v185\n"
    "v_cvt_pk_bf16_f32 v27, v182, v183\n"
    "v_exp_f32 v186, v186\n"
    "v_exp_f32 v187, v187\n"
    "v_cvt_pk_bf16_f32 v28, v184, v185\n"
    "v_mfma_f32_32x32x16_bf16 v[64:79], v[248:251], v[24:27], v[64:79]\n"
    "buffer_load_dwordx4 v16, s[16:19], s99 offen offset:3072 lds\n"
    "v_exp_f32 v188, v188\n"
    "v_exp_f32 v189, v189\n"
    "v_cvt_pk_bf16_f32 v29, v186, v187\n"
    "v_exp_f32 v190, v190\n"
    "v_exp_f32 v191, v191\n"
    "v_cvt_pk_bf16_f32 v30, v188, v189\n"
    "v_cvt_pk_bf16_f32 v31, v190, v191\n"
    "v_mfma_f32_32x32x16_bf16 v[176:191], v[192:195], v[144:147], 0\n"
    "buffer_load_dwordx4 v[240:243], v1, s[12:15], s85 offen sc1\n"
    "s_waitcnt vmcnt(14)\n"
    "ds_read_b64_tr_b16 a[176:177], v20 offset:8192\n"
    "v_exp_f32 v160, v160\n"
    "v_exp_f32 v161, v161\n"
    "v_exp_f32 v162, v162\n"
    "v_cvt_pk_bf16_f32 v24, v160, v161\n"
    "v_exp_f32 v163, v163\n"
    "v_mfma_f32_32x32x16_bf16 v[64:79], v[252:255], v[28:31], v[64:79]\n"
    "ds_read_b64_tr_b16 a[178:179], v20 offset:9216\n"
    "v_exp_f32 v164, v164\n"
    "v_exp_f32 v165, v165\n"
    "v_cvt_pk_bf16_f32 v25, v162, v163\n"
    "v_exp_f32 v166, v166\n"
    "v_exp_f32 v167, v167\n"
    "v_cvt_pk_bf16_f32 v26, v164, v165\n"
    "v_mfma_f32_32x32x16_bf16 v[176:191], v[196:199], v[148:151], v[176:191]\n"
    "ds_read_b64_tr_b16 a[180:181], v21 offset:8192\n"
    "v_exp_f32 v168, v168\n"
    "v_exp_f32 v169, v169\n"
    "v_cvt_pk_bf16_f32 v27, v166, v167\n"
    "v_exp_f32 v170, v170\n"
    "v_exp_f32 v171, v171\n"
    "v_cvt_pk_bf16_f32 v28, v168, v169\n"
    "v_mfma_f32_32x32x16_bf16 v[80:95], v[200:203], v[24:27], v[80:95]\n"
    "ds_read_b64_tr_b16 a[182:183], v21 offset:9216\n"
    "v_exp_f32 v172, v172\n"
    "v_exp_f32 v173, v173\n"
    "v_cvt_pk_bf16_f32 v29, v170, v171\n"
    "v_exp_f32 v174, v174\n"
    "v_exp_f32 v175, v175\n"
    "v_cvt_pk_bf16_f32 v30, v172, v173\n"
    "v_cvt_pk_bf16_f32 v31, v174, v175\n"
    "v_mfma_f32_32x32x16_bf16 v[160:175], v[192:195], v[152:155], 0\n"
    "buffer_load_dwordx4 v[244:247], v1, s[12:15], s85 offen offset:1024 sc1\n"
    "ds_read_b64_tr_b16 a[184:185], v20 offset:10240\n"
    "v_exp_f32 v176, v176\n"
    "v_exp_f32 v177, v177\n"
    "v_exp_f32 v178, v178\n"
    "v_cvt_pk_bf16_f32 v24, v176, v177\n"
    "v_exp_f32 v179, v179\n"
    "v_mfma_f32_32x32x16_bf16 v[80:95], v[204:207], v[28:31], v[80:95]\n"
    "ds_read_b64_tr_b16 a[186:187], v20 offset:11264\n"
    "v_exp_f32 v180, v180\n"
    "v_exp_f32 v181, v181\n"
    "v_cvt_pk_bf16_f32 v25, v178, v179\n"
    "v_exp_f32 v182, v182\n"
    "v_exp_f32 v183, v183\n"
    "v_cvt_pk_bf16_f32 v26, v180, v181\n"
    "v_mfma_f32_32x32x16_bf16 v[160:175], v[196:199], v[156:159], v[160:175]\n"
    "ds_read_b64_tr_b16 a[188:189], v21 offset:10240\n"
    "v_exp_f32 v184, v184\n"
    "v_exp_f32 v185, v185\n"
    "v_cvt_pk_bf16_f32 v27, v182, v183\n"
    "v_exp_f32 v186, v186\n"
    "v_exp_f32 v187, v187\n"
    "v_cvt_pk_bf16_f32 v28, v184, v185\n"
    "v_mfma_f32_32x32x16_bf16 v[96:111], v[200:203], v[24:27], v[96:111]\n"
    "ds_read_b64_tr_b16 a[190:191], v21 offset:11264\n"
    "v_exp_f32 v188, v188\n"
    "v_exp_f32 v189, v189\n"
    "v_cvt_pk_bf16_f32 v29, v186, v187\n"
    "v_exp_f32 v190, v190\n"
    "v_exp_f32 v191, v191\n"
    "v_cvt_pk_bf16_f32 v30, v188, v189\n"
    "v_cvt_pk_bf16_f32 v31, v190, v191\n"
    "v_mfma_f32_32x32x16_bf16 v[176:191], v[192:195], v[128:131], 0\n"
    "buffer_load_dwordx4 v[248:251], v1, s[12:15], s85 offen offset:2048 sc1\n"
    "s_waitcnt vmcnt(9)\n"
    "ds_read_b64_tr_b16 a[192:193], v20\n"
    "v_exp_f32 v160, v160\n"
    "v_exp_f32 v161, v161\n"
    "v_exp_f32 v162, v162\n"
    "v_cvt_pk_bf16_f32 v24, v160, v161\n"
    "v_exp_f32 v163, v163\n"
    "v_mfma_f32_32x32x16_bf16 v[96:111], v[204:207], v[28:31], v[96:111]\n"
    "ds_read_b64_tr_b16 a[194:195], v20 offset:1024\n"
    "s_waitcnt lgkmcnt(2)\n"
    "s_add_u32 m0, s67, 0x2000\n"
    "s_nop 0\n"
    "buffer_load_dwordx4 v13, s[16:19], s100 offen lds\n"
    "v_exp_f32 v164, v164\n"
    "v_exp_f32 v165, v165\n"
    "v_cvt_pk_bf16_f32 v25, v162, v163\n"
    "v_exp_f32 v166, v166\n"
    "v_exp_f32 v167, v167\n"
    "v_cvt_pk_bf16_f32 v26, v164, v165\n"
    "v_mfma_f32_32x32x16_bf16 v[176:191], v[196:199], v[132:135], v[176:191]\n"
    "ds_read_b64_tr_b16 a[196:197], v21\n"
    "buffer_load_dwordx4 v14, s[16:19], s100 offen offset:1024 lds\n"
    "v_exp_f32 v168, v168\n"
    "v_exp_f32 v169, v169\n"
    "v_cvt_pk_bf16_f32 v27, v166, v167\n"
    "v_exp_f32 v170, v170\n"
    "v_exp_f32 v171, v171\n"
    "v_cvt_pk_bf16_f32 v28, v168, v169\n"
    "v_mfma_f32_32x32x16_bf16 v[112:127], v[200:203], v[24:27], v[112:127]\n"
    "ds_read_b64_tr_b16 a[198:199], v21 offset:1024\n"
    "buffer_load_dwordx4 v15, s[16:19], s100 offen offset:2048 lds\n"
    "v_exp_f32 v172, v172\n"
    "v_exp_f32 v173, v173\n"
    "v_cvt_pk_bf16_f32 v29, v170, v171\n"
    "v_exp_f32 v174, v174\n"
    "v_exp_f32 v175, v175\n"
    "v_cvt_pk_bf16_f32 v30, v172, v173\n"
    "v_cvt_pk_bf16_f32 v31, v174, v175\n"
    "v_mfma_f32_32x32x16_bf16 v[160:175], v[208:211], v[136:139], 0\n"
    "buffer_load_dwordx4 v[252:255], v1, s[12:15], s85 offen offset:3072 sc1\n"
    "ds_read_b64_tr_b16 a[200:201], v20 offset:2048\n"
    "v_exp_f32 v176, v176\n"
    "v_exp_f32 v177, v177\n"
    "v_exp_f32 v178, v178\n"
    "v_cvt_pk_bf16_f32 v24, v176, v177\n"
    "v_exp_f32 v179, v179\n"
    "v_mfma_f32_32x32x16_bf16 v[112:127], v[204:207], v[28:31], v[112:127]\n"
    "ds_read_b64_tr_b16 a[202:203], v20 offset:3072\n"
    "buffer_load_dwordx4 v16, s[16:19], s100 offen offset:3072 lds\n"
    "v_exp_f32 v180, v180\n"
    "v_exp_f32 v181, v181\n"
    "v_cvt_pk_bf16_f32 v25, v178, v179\n"
    "v_exp_f32 v182, v182\n"
    "v_exp_f32 v183, v183\n"
    "v_cvt_pk_bf16_f32 v26, v180, v181\n"
    "v_mfma_f32_32x32x16_bf16 v[160:175], v[212:215], v[140:143], v[160:175]\n"
    "ds_read_b64_tr_b16 a[204:205], v21 offset:2048\n"
    "v_exp_f32 v184, v184\n"
    "v_exp_f32 v185, v185\n"
    "v_cvt_pk_bf16_f32 v27, v182, v183\n"
    "v_exp_f32 v186, v186\n"
    "v_exp_f32 v187, v187\n"
    "v_cvt_pk_bf16_f32 v28, v184, v185\n"
    "v_mfma_f32_32x32x16_bf16 v[64:79], v[200:203], v[24:27], v[64:79]\n"
    "ds_read_b64_tr_b16 a[206:207], v21 offset:3072\n"
    "v_exp_f32 v188, v188\n"
    "v_exp_f32 v189, v189\n"
    "v_cvt_pk_bf16_f32 v29, v186, v187\n"
    "v_exp_f32 v190, v190\n"
    "v_exp_f32 v191, v191\n"
    "v_cvt_pk_bf16_f32 v30, v188, v189\n"
    "v_cvt_pk_bf16_f32 v31, v190, v191\n"
    "v_mfma_f32_32x32x16_bf16 v[176:191], v[208:211], v[144:147], 0\n"
    "s_waitcnt vmcnt(8)\n"
    "ds_read_b64_tr_b16 a[208:209], v20 offset:4096\n"
    "v_exp_f32 v160, v160\n"
    "v_exp_f32 v161, v161\n"
    "v_exp_f32 v162, v162\n"
    "v_cvt_pk_bf16_f32 v24, v160, v161\n"
    "v_exp_f32 v163, v163\n"
    "v_mfma_f32_32x32x16_bf16 v[64:79], v[204:207], v[28:31], v[64:79]\n"
    "ds_read_b64_tr_b16 a[210:211], v20 offset:5120\n"
    "s_waitcnt lgkmcnt(2)\n"
    "s_add_u32 m0, s67, 0\n"
    "s_nop 0\n"
    "buffer_load_dwordx4 v13, s[16:19], s101 offen lds\n"
    "v_exp_f32 v164, v164\n"
    "v_exp_f32 v165, v165\n"
    "v_cvt_pk_bf16_f32 v25, v162, v163\n"
    "v_exp_f32 v166, v166\n"
    "v_exp_f32 v167, v167\n"
    "v_cvt_pk_bf16_f32 v26, v164, v165\n"
    "v_mfma_f32_32x32x16_bf16 v[176:191], v[212:215], v[148:151], v[176:191]\n"
    "ds_read_b64_tr_b16 a[212:213], v21 offset:4096\n"
    "buffer_load_dwordx4 v14, s[16:19], s101 offen offset:1024 lds\n"
    "v_exp_f32 v168, v168\n"
    "v_exp_f32 v169, v169\n"
    "v_cvt_pk_bf16_f32 v27, v166, v167\n"
    "v_exp_f32 v170, v170\n"
    "v_exp_f32 v171, v171\n"
    "v_cvt_pk_bf16_f32 v28, v168, v169\n"
    "v_mfma_f32_32x32x16_bf16 v[80:95], v[216:219], v[24:27], v[80:95]\n"
    "ds_read_b64_tr_b16 a[214:215], v21 offset:5120\n"
    "buffer_load_dwordx4 v15, s[16:19], s101 offen offset:2048 lds\n"
    "v_exp_f32 v172, v172\n"
    "v_exp_f32 v173, v173\n"
    "v_cvt_pk_bf16_f32 v29, v170, v171\n"
    "v_exp_f32 v174, v174\n"
    "v_exp_f32 v175, v175\n"
    "v_cvt_pk_bf16_f32 v30, v172, v173\n"
    "v_cvt_pk_bf16_f32 v31, v174, v175\n"
    "v_mfma_f32_32x32x16_bf16 v[160:175], v[208:211], v[152:155], 0\n"
    "ds_read_b64_tr_b16 a[216:217], v20 offset:6144\n"
    "v_exp_f32 v176, v176\n"
    "v_exp_f32 v177, v177\n"
    "v_exp_f32 v178, v178\n"
    "v_cvt_pk_bf16_f32 v24, v176, v177\n"
    "v_exp_f32 v179, v179\n"
    "v_mfma_f32_32x32x16_bf16 v[80:95], v[220:223], v[28:31], v[80:95]\n"
    "ds_read_b64_tr_b16 a[218:219], v20 offset:7168\n"
    "buffer_load_dwordx4 v16, s[16:19], s101 offen offset:3072 lds\n"
    "v_exp_f32 v180, v180\n"
    "v_exp_f32 v181, v181\n"
    "v_cvt_pk_bf16_f32 v25, v178, v179\n"
    "v_exp_f32 v182, v182\n"
    "v_exp_f32 v183, v183\n"
    "v_cvt_pk_bf16_f32 v26, v180, v181\n"
    "v_mfma_f32_32x32x16_bf16 v[160:175], v[212:215], v[156:159], v[160:175]\n"
    "ds_read_b64_tr_b16 a[220:221], v21 offset:6144\n"
    "v_exp_f32 v184, v184\n"
    "v_exp_f32 v185, v185\n"
    "v_cvt_pk_bf16_f32 v27, v182, v183\n"
    "v_exp_f32 v186, v186\n"
    "v_exp_f32 v187, v187\n"
    "v_cvt_pk_bf16_f32 v28, v184, v185\n"
    "v_mfma_f32_32x32x16_bf16 v[96:111], v[216:219], v[24:27], v[96:111]\n"
    "ds_read_b64_tr_b16 a[222:223], v21 offset:7168\n"
    "buffer_load_dword v36, v12, s[24:27], s64 offen\n"
    "v_exp_f32 v188, v188\n"
    "v_exp_f32 v189, v189\n"
    "v_cvt_pk_bf16_f32 v29, v186, v187\n"
    "v_exp_f32 v190, v190\n"
    "v_exp_f32 v191, v191\n"
    "v_cvt_pk_bf16_f32 v30, v188, v189\n"
    "v_cvt_pk_bf16_f32 v31, v190, v191\n"
    "v_mfma_f32_32x32x16_bf16 v[176:191], v[208:211], v[128:131], 0\n"
    "v_exp_f32 v160, v160\n"
    "v_exp_f32 v161, v161\n"
    "v_exp_f32 v162, v162\n"
    "v_cvt_pk_bf16_f32 v24, v160, v161\n"
    "v_exp_f32 v163, v163\n"
    "v_mfma_f32_32x32x16_bf16 v[96:111], v[220:223], v[28:31], v[96:111]\n"
    "v_exp_f32 v164, v164\n"
    "v_exp_f32 v165, v165\n"
    "v_cvt_pk_bf16_f32 v25, v162, v163\n"
    "v_exp_f32 v166, v166\n"
    "v_exp_f32 v167, v167\n"
    "v_cvt_pk_bf16_f32 v26, v164, v165\n"
    "v_mfma_f32_32x32x16_bf16 v[176:191], v[212:215], v[132:135], v[176:191]\n"
    "v_cmp_ne_u64 vcc, v[42:43], v[22:23]\n"
    "s_nop 0\n"
    "s_cbranch_vccnz L_fx\n"
    "L_fx_back:\n"
    "v_exp_f32 v168, v168\n"
    "v_exp_f32 v169, v169\n"
    "v_cvt_pk_bf16_f32 v27, v166, v167\n"
    "v_exp_f32 v170, v170\n"
    "v_exp_f32 v171, v171\n"
    "v_cvt_pk_bf16_f32 v28, v168, v169\n"
    "v_mfma_f32_32x32x16_bf16 v[112:127], v[216:219], v[24:27], v[112:127]\n"
    "v_exp_f32 v172, v172\n"
    "v_exp_f32 v173, v173\n"
    "v_cvt_pk_bf16_f32 v29, v170, v171\n"
    "v_exp_f32 v174, v174\n"
    "v_exp_f32 v175, v175\n"
    "v_cvt_pk_bf16_f32 v30, v172, v173\n"
    "v_cvt_pk_bf16_f32 v31, v174, v175\n"
    "v_mfma_f32_32x32x16_bf16 v[160:175], v[224:227], v[136:139], 0\n"
    "v_exp_f32 v176, v176\n"
    "v_exp_f32 v177, v177\n"
    "v_exp_f32 v178, v178\n"
    "v_cvt_pk_bf16_f32 v24, v176, v177\n"
    "v_exp_f32 v179, v179\n"
    "v_mfma_f32_32x32x16_bf16 v[112:127], v[220:223], v[28:31], v[112:127]\n"
    "v_exp_f32 v180, v180\n"
    "v_exp_f32 v181, v181\n"
    "v_cvt_pk_bf16_f32 v25, v178, v179\n"
    "v_exp_f32 v182, v182\n"
    "v_exp_f32 v183, v183\n"
    "v_cvt_pk_bf16_f32 v26, v180, v181\n"
    "v_mfma_f32_32x32x16_bf16 v[160:175], v[228:231], v[140:143], v[160:175]\n"
    "v_exp_f32 v184, v184\n"
    "v_exp_f32 v185, v185\n"
    "v_cvt_pk_bf16_f32 v27, v182, v183\n"
    "v_exp_f32 v186, v186\n"
    "v_exp_f32 v187, v187\n"
    "v_cvt_pk_bf16_f32 v28, v184, v185\n"
    "v_mfma_f32_32x32x16_bf16 v[64:79], v[216:219], v[24:27], v[64:79]\n"
    "v_exp_f32 v188, v188\n"
    "v_exp_f32 v189, v189\n"
    "v_cvt_pk_bf16_f32 v29, v186, v187\n"
    "v_exp_f32 v190, v190\n"
    "v_exp_f32 v191, v191\n"
    "v_cvt_pk_bf16_f32 v30, v188, v189\n"
    "v_cvt_pk_bf16_f32 v31, v190, v191\n"
    "v_mfma_f32_32x32x16_bf16 v[176:191], v[224:227], v[144:147], 0\n"
    "s_waitcnt vmcnt(5)\n"
    "ds_read_b64_tr_b16 a[224:225], v20 offset:8192\n"
    "v_exp_f32 v160, v160\n"
    "v_exp_f32 v161, v161\n"
    "v_exp_f32 v162, v162\n"
    "v_cvt_pk_bf16_f32 v24, v160, v161\n"
    "v_exp_f32 v163, v163\n"
    "v_mfma_f32_32x32x16_bf16 v[64:79], v[220:223], v[28:31], v[64:79]\n"
    "ds_read_b64_tr_b16 a[226:227], v20 offset:9216\n"
    "v_exp_f32 v164, v164\n"
    "v_exp_f32 v165, v165\n"
    "v_cvt_pk_bf16_f32 v25, v162, v163\n"
    "v_exp_f32 v166, v166\n"
    "v_exp_f32 v167, v167\n"
    "v_cvt_pk_bf16_f32 v26, v164, v165\n"
    "v_mfma_f32_32x32x16_bf16 v[176:191], v[228:231], v[148:151], v[176:191]\n"
    "ds_read_b64_tr_b16 a[228:229], v21 offset:8192\n"
    "v_exp_f32 v168, v168\n"
    "v_exp_f32 v169, v169\n"
    "v_cvt_pk_bf16_f32 v27, v166, v167\n"
    "v_exp_f32 v170, v170\n"
    "v_exp_f32 v171, v171\n"
    "v_cvt_pk_bf16_f32 v28, v168, v169\n"
    "v_mfma_f32_32x32x16_bf16 v[80:95], v[232:235], v[24:27], v[80:95]\n"
    "ds_read_b64_tr_b16 a[230:231], v21 offset:9216\n"
    "v_exp_f32 v172, v172\n"
    "v_exp_f32 v173, v173\n"
    "v_cvt_pk_bf16_f32 v29, v170, v171\n"
    "v_exp_f32 v174, v174\n"
    "v_exp_f32 v175, v175\n"
    "v_cvt_pk_bf16_f32 v30, v172, v173\n"
    "v_cvt_pk_bf16_f32 v31, v174, v175\n"
    "v_mfma_f32_32x32x16_bf16 v[160:175], v[224:227], v[152:155], 0\n"
    "ds_read_b64_tr_b16 a[232:233], v20 offset:10240\n"
    "v_exp_f32 v176, v176\n"
    "v_exp_f32 v177, v177\n"
    "v_exp_f32 v178, v178\n"
    "v_cvt_pk_bf16_f32 v24, v176, v177\n"
    "v_exp_f32 v179, v179\n"
    "v_mfma_f32_32x32x16_bf16 v[80:95], v[236:239], v[28:31], v[80:95]\n"
    "ds_read_b64_tr_b16 a[234:235], v20 offset:11264\n"
    "v_exp_f32 v180, v180\n"
    "v_exp_f32 v181, v181\n"
    "v_cvt_pk_bf16_f32 v25, v178, v179\n"
    "v_exp_f32 v182, v182\n"
    "v_exp_f32 v183, v183\n"
    "v_cvt_pk_bf16_f32 v26, v180, v181\n"
    "v_mfma_f32_32x32x16_bf16 v[160:175], v[228:231], v[156:159], v[160:175]\n"
    "ds_read_b64_tr_b16 a[236:237], v21 offset:10240\n"
    "v_exp_f32 v184, v184\n"
    "v_exp_f32 v185, v185\n"
    "v_cvt_pk_bf16_f32 v27, v182, v183\n"
    "v_exp_f32 v186, v186\n"
    "v_exp_f32 v187, v187\n"
    "v_cvt_pk_bf16_f32 v28, v184, v185\n"
    "v_mfma_f32_32x32x16_bf16 v[96:111], v[232:235], v[24:27], v[96:111]\n"
    "ds_read_b64_tr_b16 a[238:239], v21 offset:11264\n"
    "v_exp_f32 v188, v188\n"
    "v_exp_f32 v189, v189\n"
    "v_cvt_pk_bf16_f32 v29, v186, v187\n"
    "v_exp_f32 v190, v190\n"
    "v_exp_f32 v191, v191\n"
    "v_cvt_pk_bf16_f32 v30, v188, v189\n"
    "v_cvt_pk_bf16_f32 v31, v190, v191\n"
    "v_mfma_f32_32x32x16_bf16 v[176:191], v[224:227], v[128:131], 0\n"
    "s_waitcnt vmcnt(1)\n"
    "ds_read_b64_tr_b16 a[240:241], v20\n"
    "v_exp_f32 v160, v160\n"
    "v_exp_f32 v161, v161\n"
    "v_exp_f32 v162, v162\n"
    "v_cvt_pk_bf16_f32 v24, v160, v161\n"
    "v_exp_f32 v163, v163\n"
    "v_mfma_f32_32x32x16_bf16 v[96:111], v[236:239], v[28:31], v[96:111]\n"
    "ds_read_b64_tr_b16 a[242:243], v20 offset:1024\n"
    "v_exp_f32 v164, v164\n"
    "v_exp_f32 v165, v165\n"
    "v_cvt_pk_bf16_f32 v25, v162, v163\n"
    "v_exp_f32 v166, v166\n"
    "v_exp_f32 v167, v167\n"
    "v_cvt_pk_bf16_f32 v26, v164, v165\n"
    "v_mfma_f32_32x32x16_bf16 v[176:191], v[228:231], v[132:135], v[176:191]\n"
    "ds_read_b64_tr_b16 a[244:245], v21\n"
    "v_exp_f32 v168, v168\n"
    "v_exp_f32 v169, v169\n"
    "v_cvt_pk_bf16_f32 v27, v166, v167\n"
    "v_exp_f32 v170, v170\n"
    "v_exp_f32 v171, v171\n"
    "v_cvt_pk_bf16_f32 v28, v168, v169\n"
    "v_mfma_f32_32x32x16_bf16 v[112:127], v[232:235], v[24:27], v[112:127]\n"
    "ds_read_b64_tr_b16 a[246:247], v21 offset:1024\n"
    "v_exp_f32 v172, v172\n"
    "v_exp_f32 v173, v173\n"
    "v_cvt_pk_bf16_f32 v29, v170, v171\n"
    "v_exp_f32 v174, v174\n"
    "v_exp_f32 v175, v175\n"
    "v_cvt_pk_bf16_f32 v30, v172, v173\n"
    "v_cvt_pk_bf16_f32 v31, v174, v175\n"
    "v_mfma_f32_32x32x16_bf16 v[160:175], v[240:243], v[136:139], 0\n"
    "ds_read_b64_tr_b16 a[248:249], v20 offset:2048\n"
    "v_exp_f32 v176, v176\n"
    "v_exp_f32 v177, v177\n"
    "v_exp_f32 v178, v178\n"
    "v_cvt_pk_bf16_f32 v24, v176, v177\n"
    "v_exp_f32 v179, v179\n"
    "v_mfma_f32_32x32x16_bf16 v[112:127], v[236:239], v[28:31], v[112:127]\n"
    "ds_read_b64_tr_b16 a[250:251], v20 offset:3072\n"
    "v_exp_f32 v180, v180\n"
    "v_exp_f32 v181, v181\n"
    "v_cvt_pk_bf16_f32 v25, v178, v179\n"
    "v_exp_f32 v182, v182\n"
    "v_exp_f32 v183, v183\n"
    "v_cvt_pk_bf16_f32 v26, v180, v181\n"
    "v_mfma_f32_32x32x16_bf16 v[160:175], v[244:247], v[140:143], v[160:175]\n"
    "ds_read_b64_tr_b16 a[252:253], v21 offset:2048\n"
    "v_exp_f32 v184, v184\n"
    "v_exp_f32 v185, v185\n"
    "v_cvt_pk_bf16_f32 v27, v182, v183\n"
    "v_exp_f32 v186, v186\n"
    "v_exp_f32 v187, v187\n"
    "v_cvt_pk_bf16_f32 v28, v184, v185\n"
    "v_mfma_f32_32x32x16_bf16 v[64:79], v[232:235], v[24:27], v[64:79]\n"
    "ds_read_b64_tr_b16 a[254:255], v21 offset:3072\n"
    "v_exp_f32 v188, v188\n"
    "v_exp_f32 v189, v189\n"
    "v_cvt_pk_bf16_f32 v29, v186, v187\n"
    "v_exp_f32 v190, v190\n"
    "v_exp_f32 v191, v191\n"
    "v_cvt_pk_bf16_f32 v30, v188, v189\n"
    "v_cvt_pk_bf16_f32 v31, v190, v191\n"
    "v_mfma_f32_32x32x16_bf16 v[176:191], v[240:243], v[144:147], 0\n"
    "v_exp_f32 v160, v160\n"
    "v_exp_f32 v161, v161\n"
    "v_exp_f32 v162, v162\n"
    "v_cvt_pk_bf16_f32 v24, v160, v161\n"
    "v_exp_f32 v163, v163\n"
    "v_mfma_f32_32x32x16_bf16 v[64:79], v[236:239], v[28:31], v[64:79]\n"
    "v_exp_f32 v164, v164\n"
    "v_exp_f32 v165, v165\n"
    "v_cvt_pk_bf16_f32 v25, v162, v163\n"
    "v_exp_f32 v166, v166\n"
    "v_exp_f32 v167, v167\n"
    "v_cvt_pk_bf16_f32 v26, v164, v165\n"
    "v_mfma_f32_32x32x16_bf16 v[176:191], v[244:247], v[148:151], v[176:191]\n"
    "s_waitcnt lgkmcnt(0)\n"
    "s_barrier\n"
    "v_exp_f32 v168, v168\n"
    "v_exp_f32 v169, v169\n"
    "v_cvt_pk_bf16_f32 v27, v166, v167\n"
    "v_exp_f32 v170, v170\n"
    "v_exp_f32 v171, v171\n"
    "v_cvt_pk_bf16_f32 v28, v168, v169\n"
    "v_mfma_f32_32x32x16_bf16 v[80:95], v[248:251], v[24:27], v[80:95]\n"
    "v_exp_f32 v172, v172\n"
    "v_exp_f32 v173, v173\n"
    "v_cvt_pk_bf16_f32 v29, v170, v171\n"
    "v_exp_f32 v174, v174\n"
    "v_exp_f32 v175, v175\n"
    "v_cvt_pk_bf16_f32 v30, v172, v173\n"
    "v_cvt_pk_bf16_f32 v31, v174, v175\n"
    "v_mfma_f32_32x32x16_bf16 v[160:175], v[240:243], v[152:155], 0\n"
    "v_exp_f32 v176, v176\n"
    "v_exp_f32 v177, v177\n"
    "v_exp_f32 v178, v178\n"
    "v_cvt_pk_bf16_f32 v24, v176, v177\n"
    "v_exp_f32 v179, v179\n"
    "v_mfma_f32_32x32x16_bf16 v[80:95], v[252:255], v[28:31], v[80:95]\n"
    "v_exp_f32 v180, v180\n"
    "v_exp_f32 v181, v181\n"
    "v_cvt_pk_bf16_f32 v25, v178, v179\n"
    "v_exp_f32 v182, v182\n"
    "v_exp_f32 v183, v183\n"
    "v_cvt_pk_bf16_f32 v26, v180, v181\n"
    "v_mfma_f32_32x32x16_bf16 v[160:175], v[244:247], v[156:159], v[160:175]\n"
    "v_exp_f32 v184, v184\n"
    "v_exp_f32 v185, v185\n"
    "v_cvt_pk_bf16_f32 v27, v182, v183\n"
    "v_exp_f32 v186, v186\n"
    "v_exp_f32 v187, v187\n"
    "v_cvt_pk_bf16_f32 v28, v184, v185\n"
    "v_mfma_f32_32x32x16_bf16 v[96:111], v[248:251], v[24:27], v[96:111]\n"
    "v_exp_f32 v188, v188\n"
    "v_exp_f32 v189, v189\n"
    "v_cvt_pk_bf16_f32 v29, v186, v187\n"
    "v_exp_f32 v190, v190\n"
    "v_exp_f32 v191, v191\n"
    "v_cvt_pk_bf16_f32 v30, v188, v189\n"
    "v_cvt_pk_bf16_f32 v31, v190, v191\n"
    "v_mfma_f32_32x32x16_bf16 v[176:191], v[240:243], v[128:131], 0\n"
    "ds_write_b128 v6, v[80:83]\n"
    "s_lshl2_add_u32 s74, s50, s44\n"
    "s_lshl_b32 s74, s74, 13\n"
    "v_exp_f32 v160, v160\n"
    "v_exp_f32 v161, v161\n"
    "v_exp_f32 v162, v162\n"
    "v_cvt_pk_bf16_f32 v24, v160, v161\n"
    "v_exp_f32 v163, v163\n"
    "v_mfma_f32_32x32x16_bf16 v[96:111], v[252:255], v[28:31], v[96:111]\n"
    "ds_write_b128 v6, v[84:87] offset:1024\n"
    "s_add_u32 s74, s74, 0x500000\n"
    "s_add_u32 s75, s74, 0x1000\n"
    "v_exp_f32 v164, v164\n"
    "v_exp_f32 v165, v165\n"
    "v_cvt_pk_bf16_f32 v25, v162, v163\n"
    "v_exp_f32 v166, v166\n"
    "v_exp_f32 v167, v167\n"
    "v_cvt_pk_bf16_f32 v26, v164, v165\n"
    "v_mfma_f32_32x32x16_bf16 v[176:191], v[244:247], v[132:135], v[176:191]\n"
    "ds_write_b128 v6, v[88:91] offset:2048\n"
    "v_and_b32 v18, 3, v0\n"
    "s_add_u32 s60, s74, 0x7f0\n"
    "v_exp_f32 v168, v168\n"
    "v_exp_f32 v169, v169\n"
    "v_cvt_pk_bf16_f32 v27, v166, v167\n"
    "v_exp_f32 v170, v170\n"
    "v_exp_f32 v171, v171\n"
    "v_cvt_pk_bf16_f32 v28, v168, v169\n"
    "v_mfma_f32_32x32x16_bf16 v[112:127], v[248:251], v[24:27], v[112:127]\n"
    "ds_write_b128 v6, v[92:95] offset:3072\n"
    "v_lshl_add_u32 v18, v18, 11, s60\n"
    "s_lshl_b32 s76, s44, 13\n"
    "v_exp_f32 v172, v172\n"
    "v_exp_f32 v173, v173\n"
    "v_cvt_pk_bf16_f32 v29, v170, v171\n"
    "v_exp_f32 v174, v174\n"
    "v_exp_f32 v175, v175\n"
    "v_cvt_pk_bf16_f32 v30, v172, v173\n"
    "v_cvt_pk_bf16_f32 v31, v174, v175\n"
    "ds_write_b128 v7, v[96:99]\n"
    "s_add_u32 s76, s76, 0x1c000\n"
    "v_exp_f32 v176, v176\n"
    "v_exp_f32 v177, v177\n"
    "v_exp_f32 v178, v178\n"
    "v_cvt_pk_bf16_f32 v24, v176, v177\n"
    "v_exp_f32 v179, v179\n"
    "v_mfma_f32_32x32x16_bf16 v[112:127], v[252:255], v[28:31], v[112:127]\n"
    "ds_write_b128 v7, v[100:103] offset:1024\n"
    "v_exp_f32 v180, v180\n"
    "v_exp_f32 v181, v181\n"
    "v_cvt_pk_bf16_f32 v25, v178, v179\n"
    "v_exp_f32 v182, v182\n"
    "v_exp_f32 v183, v183\n"
    "v_cvt_pk_bf16_f32 v26, v180, v181\n"
    "ds_write_b128 v7, v[104:107] offset:2048\n"
    "v_exp_f32 v184, v184\n"
    "v_exp_f32 v185, v185\n"
    "v_cvt_pk_bf16_f32 v27, v182, v183\n"
    "v_exp_f32 v186, v186\n"
    "v_exp_f32 v187, v187\n"
    "v_cvt_pk_bf16_f32 v28, v184, v185\n"
    "v_mfma_f32_32x32x16_bf16 v[64:79], v[248:251], v[24:27], v[64:79]\n"
    "ds_write_b128 v7, v[108:111] offset:3072\n"
    "buffer_load_dword v56, v18, s[12:15], 0 offen sc1\n"
    "v_exp_f32 v188, v188\n"
    "v_exp_f32 v189, v189\n"
    "v_cvt_pk_bf16_f32 v29, v186, v187\n"
    "v_exp_f32 v190, v190\n"
    "v_exp_f32 v191, v191\n"
    "v_cvt_pk_bf16_f32 v30, v188, v189\n"
    "v_cvt_pk_bf16_f32 v31, v190, v191\n"
    "ds_write_b128 v8, v[112:115]\n"
    "ds_write_b128 v8, v[116:119] offset:1024\n"
    "v_mfma_f32_32x32x16_bf16 v[64:79], v[252:255], v[28:31], v[64:79]\n"
    "ds_write_b128 v8, v[120:123] offset:2048\n"
    "ds_write_b128 v8, v[124:127] offset:3072\n"
    "s_waitcnt lgkmcnt(0)\n"
    "s_barrier\n"
    "ds_read_b128 v[160:163], v9\n"
    "ds_read_b128 v[164:167], v9 offset:1024\n"
    "ds_read_b128 v[168:171], v9 offset:2048\n"
    "ds_read_b128 v[172:175], v9 offset:3072\n"
    "ds_read_b128 v[176:179], v9 offset:4096\n"
    "ds_read_b128 v[180:183], v9 offset:5120\n"
    "ds_read_b128 v[184:187], v9 offset:6144\n"
    "ds_read_b128 v[188:191], v9 offset:7168\n"
    "ds_read_b128 v[192:195], v9 offset:8192\n"
    "ds_read_b128 v[196:199], v9 offset:9216\n"
    "ds_read_b128 v[200:203], v9 offset:10240\n"
    "ds_read_b128 v[204:207], v9 offset:11264\n"
    "s_lshl2_add_u32 s60, s49, s44\n"
    "s_lshl4_add_u32 s60, s60, s48\n"
    "s_lshl_b32 s61, s60, 11\n"
    "s_add_u32 s61, s61, 0x500000\n"
    "s_mov_b32 s62, 0x1f800000\n"
    "s_mov_b32 s63, 0x5f800000\n"
    "s_waitcnt lgkmcnt(10)\n"
    "v_pk_add_f32 v[64:65], v[64:65], v[160:161]\n"
    "v_pk_add_f32 v[66:67], v[66:67], v[162:163]\n"
    "v_pk_add_f32 v[68:69], v[68:69], v[164:165]\n"
    "v_pk_add_f32 v[70:71], v[70:71], v[166:167]\n"
    "s_waitcnt lgkmcnt(8)\n"
    "v_pk_add_f32 v[72:73], v[72:73], v[168:169]\n"
    "v_pk_add_f32 v[74:75], v[74:75], v[170:171]\n"
    "v_pk_add_f32 v[76:77], v[76:77], v[172:173]\n"
    "v_pk_add_f32 v[78:79], v[78:79], v[174:175]\n"
    "s_waitcnt lgkmcnt(6)\n"
    "v_pk_add_f32 v[64:65], v[64:65], v[176:177]\n"
    "v_pk_add_f32 v[66:67], v[66:67], v[178:179]\n"
    "v_pk_add_f32 v[68:69], v[68:69], v[180:181]\n"
    "v_pk_add_f32 v[70:71], v[70:71], v[182:183]\n"
    "s_waitcnt lgkmcnt(4)\n"
    "v_pk_add_f32 v[72:73], v[72:73], v[184:185]\n"
    "v_pk_add_f32 v[74:75], v[74:75], v[186:187]\n"
    "v_pk_add_f32 v[76:77], v[76:77], v[188:189]\n"
    "v_pk_add_f32 v[78:79], v[78:79], v[190:191]\n"
    "s_waitcnt lgkmcnt(2)\n"
    "v_pk_add_f32 v[64:65], v[64:65], v[192:193]\n"
    "v_pk_add_f32 v[66:67], v[66:67], v[194:195]\n"
    "v_pk_add_f32 v[68:69], v[68:69], v[196:197]\n"
    "v_pk_add_f32 v[70:71], v[70:71], v[198:199]\n"
    "s_waitcnt lgkmcnt(0)\n"
    "v_pk_add_f32 v[72:73], v[72:73], v[200:201]\n"
    "v_pk_add_f32 v[74:75], v[74:75], v[202:203]\n"
    "v_pk_add_f32 v[76:77], v[76:77], v[204:205]\n"
    "v_pk_add_f32 v[78:79], v[78:79], v[206:207]\n"
    "v_mov_b32 v24, v76\n"
    "v_mov_b32 v25, v76\n"
    "s_nop 1\n"
    "v_permlane32_swap_b32 v24, v25\n"
    "v_rcp_f32 v24, v25\n"
    "v_cmp_lt_f32 vcc, s62, v25\n"
    "v_cmp_gt_f32 s[58:59], s63, v25\n"
    "v_pk_mul_f32 v[64:65], v[64:65], v[24:25] op_sel_hi:[1,0]\n"
    "v_pk_mul_f32 v[66:67], v[66:67], v[24:25] op_sel_hi:[1,0]\n"
    "v_pk_mul_f32 v[68:69], v[68:69], v[24:25] op_sel_hi:[1,0]\n"
    "v_pk_mul_f32 v[70:71], v[70:71], v[24:25] op_sel_hi:[1,0]\n"
    "v_pk_mul_f32 v[72:73], v[72:73], v[24:25] op_sel_hi:[1,0]\n"
    "v_pk_mul_f32 v[74:75], v[74:75], v[24:25] op_sel_hi:[1,0]\n"
    "v_pk_mul_f32 v[76:77], v[76:77], v[24:25] op_sel_hi:[1,0]\n"
    "v_pk_mul_f32 v[78:79], v[78:79], v[24:25] op_sel_hi:[1,0]\n"
    "s_and_b64 vcc, vcc, s[58:59]\n"
    "v_pk_mul_f32 v[26:27], v[64:65], 0 op_sel_hi:[1,0]\n"
    "v_pk_fma_f32 v[26:27], v[66:67], 0, v[26:27] op_sel_hi:[1,0,1]\n"
    "v_pk_fma_f32 v[26:27], v[68:69], 0, v[26:27] op_sel_hi:[1,0,1]\n"
    "v_pk_fma_f32 v[26:27], v[70:71], 0, v[26:27] op_sel_hi:[1,0,1]\n"
    "v_pk_fma_f32 v[26:27], v[72:73], 0, v[26:27] op_sel_hi:[1,0,1]\n"
    "v_pk_fma_f32 v[26:27], v[74:75], 0, v[26:27] op_sel_hi:[1,0,1]\n"
    "v_pk_fma_f32 v[26:27], v[76:77], 0, v[26:27] op_sel_hi:[1,0,1]\n"
    "v_pk_fma_f32 v[26:27], v[78:79], 0, v[26:27] op_sel_hi:[1,0,1]\n"
    "v_cmp_o_f32 s[58:59], v26, v27\n"
    "v_cvt_pk_bf16_f32 v28, v64, v65\n"
    "v_cvt_pk_bf16_f32 v29, v66, v67\n"
    "v_cvt_pk_bf16_f32 v30, v68, v69\n"
    "v_cvt_pk_bf16_f32 v31, v70, v71\n"
    "v_cvt_pk_bf16_f32 v32, v72, v73\n"
    "v_cvt_pk_bf16_f32 v33, v74, v75\n"
    "v_cvt_pk_bf16_f32 v34, v76, v77\n"
    "v_cvt_pk_bf16_f32 v35, v78, v79\n"
    "s_mov_b32 exec_lo, 0\n"
    "v_mov_b32 v34, 0\n"
    "v_mov_b32 v35, 0\n"
    "s_mov_b32 exec_lo, -1\n"
    "s_and_b64 vcc, vcc, s[58:59]\n"
    "s_cmp_eq_u64 vcc, -1\n"
    "s_cbranch_scc0 L_slow\n"
    "L_slow_back:\n"
    "buffer_store_dwordx4 v[28:31], v1, s[12:15], s61 offen sc1\n"
    "buffer_store_dwordx4 v[32:35], v1, s[12:15], s61 offen offset:1024 sc1\n"
    "s_barrier\n"
    "s_waitcnt vmcnt(2)\n"
    "v_cmp_eq_u32 s[58:59], -1, v56\n"
    "s_nop 0\n"
    "s_cmp_eq_u64 s[58:59], 0\n"
    "s_cselect_b32 s77, 1, 0\n"
    "s_cbranch_scc0 L_o1\n"
    "s_mov_b32 m0, s76\n"
    "s_nop 0\n"
    "buffer_load_dwordx4 v1, s[12:15], s74 offen sc1 lds\n"
    "buffer_load_dwordx4 v1, s[12:15], s74 offen offset:1024 sc1 lds\n"
    "buffer_load_dwordx4 v1, s[12:15], s74 offen offset:2048 sc1 lds\n"
    "buffer_load_dwordx4 v1, s[12:15], s74 offen offset:3072 sc1 lds\n"
    "s_add_u32 m0, s76, 0x1000\n"
    "s_nop 0\n"
    "buffer_load_dwordx4 v1, s[12:15], s75 offen sc1 lds\n"
    "buffer_load_dwordx4 v1, s[12:15], s75 offen offset:1024 sc1 lds\n"
    "buffer_load_dwordx4 v1, s[12:15], s75 offen offset:2048 sc1 lds\n"
    "buffer_load_dwordx4 v1, s[12:15], s75 offen offset:3072 sc1 lds\n"
    "L_o1:\n"
    "v_mov_b64 v[80:81], 0\n"
    "v_mov_b64 v[82:83], 0\n"
    "v_mov_b64 v[84:85], 0\n"
    "v_mov_b64 v[86:87], 0\n"
    "v_mov_b64 v[88:89], 0\n"
    "v_mov_b64 v[90:91], 0\n"
    "v_mov_b64 v[92:93], 0\n"
    "v_mov_b64 v[94:95], 0\n"
    "s_mul_i32 s62, s50, 0x7000\n"
    "s_lshl_b32 s63, s52, 6\n"
    "s_add_u32 s62, s62, s63\n"
    "v_mul_u32_u24 v112, 0xe00, v60\n"
    "v_lshl_add_u32 v112, v61, 1, v112\n"
    "buffer_load_short_d16_hi v80, v112, s[4:7], s62 offen\n"
    "buffer_load_short_d16_hi v81, v112, s[4:7], s62 offen offset:896\n"
    "buffer_load_short_d16_hi v82, v112, s[4:7], s62 offen offset:1792\n"
    "buffer_load_short_d16_hi v83, v112, s[4:7], s62 offen offset:2688\n"
    "s_add_u32 s62, s62, 0x1c00\n"
    "buffer_load_short_d16_hi v84, v112, s[4:7], s62 offen\n"
    "buffer_load_short_d16_hi v85, v112, s[4:7], s62 offen offset:896\n"
    "buffer_load_short_d16_hi v86, v112, s[4:7], s62 offen offset:1792\n"
    "buffer_load_short_d16_hi v87, v112, s[4:7], s62 offen offset:2688\n"
    "s_add_u32 s62, s62, 0x1c00\n"
    "buffer_load_short_d16_hi v88, v112, s[4:7], s62 offen\n"
    "buffer_load_short_d16_hi v89, v112, s[4:7], s62 offen offset:896\n"
    "buffer_load_short_d16_hi v90, v112, s[4:7], s62 offen offset:1792\n"
    "buffer_load_short_d16_hi v91, v112, s[4:7], s62 offen offset:2688\n"
    "s_add_u32 s62, s62, 0x1c00\n"
    "buffer_load_short_d16_hi v92, v112, s[4:7], s62 offen\n"
    "buffer_load_short_d16_hi v93, v112, s[4:7], s62 offen offset:896\n"
    "buffer_load_short_d16_hi v94, v112, s[4:7], s62 offen offset:1792\n"
    "buffer_load_short_d16_hi v95, v112, s[4:7], s62 offen offset:2688\n"
    "s_lshr_b32 s60, s44, 1\n"
    "s_lshl1_add_u32 s60, s53, s60\n"
    "s_mul_i32 s72, s60, 0x70\n"
    "s_lshl2_add_u32 s72, s54, s72\n"
    "s_and_b32 s65, s44, 1\n"
    "s_lshl1_add_u32 s72, s65, s72\n"
    "s_lshl_b32 s72, s72, 10\n"
    "s_add_u32 s72, s72, 0xb00000\n"
    "s_cmp_lt_u32 s56, 7\n"
    "s_cselect_b32 s72, s72, 0x70000000\n"
    "buffer_store_dwordx4 v[44:47], v1, s[12:15], s72 offen sc1\n"
    "s_lshl_b32 s60, s52, 6\n"
    "buffer_store_dwordx4 v[44:47], v1, s[12:15], s72 offen offset:1024 sc1\n"
    "s_add_u32 s60, s60, s50\n"
    "s_lshl_b32 s60, s60, 11\n"
    "s_add_u32 s82, s60, 0x900000\n"
    "s_cmp_lt_u32 s52, 14\n"
    "s_cselect_b32 s83, -1, 0\n"
    "s_cselect_b32 s82, s82, 0x70000000\n"
    "buffer_store_dwordx4 v[44:47], v1, s[12:15], s82 offen sc1\n"
    "s_sub_u32 s81, s82, 0x200000\n"
    "buffer_store_dwordx4 v[44:47], v1, s[12:15], s82 offen offset:1024 sc1\n"
    "s_lshl2_add_u32 s79, s50, s51\n"
    "s_lshl_b32 s79, s79, 9\n"
    "s_add_u32 s79, s79, 0x80e00\n"
    "s_cmp_eq_u32 s44, 0\n"
    "s_cselect_b32 s79, s79, 0x70000000\n"
    "s_mov_b32 exec_lo, 0\n"
    "buffer_store_dwordx4 v[44:47], v1, s[12:15], s79 offen sc1\n"
    "s_mov_b32 exec_lo, -1\n"
    "s_add_u32 s79, s79, 0x200\n"
    "v_lshlrev_b32 v112, 9, v60\n"
    "v_sub_u32 v112, v4, v112\n"
    "s_lshr_b32 s66, s44, 1\n"
    "s_mul_i32 s66, s66, 0xe000\n"
    "s_xor_b32 s60, s44, 0\n"
    "s_mul_i32 s61, s60, 0x3800\n"
    "s_add_u32 s61, s61, s66\n"
    "v_add_u32 v10, s61, v4\n"
    "v_add_u32 v14, s61, v112\n"
    "s_lshl_b32 s60, s60, 13\n"
    "s_add_u32 s60, s60, 0x1c000\n"
    "v_add_u32 v56, s60, v1\n"
    "ds_read_b64_tr_b16 v[128:129], v10\n"
    "ds_read_b64_tr_b16 v[130:131], v10 offset:1024\n"
    "ds_read_b64_tr_b16 v[132:133], v10 offset:2048\n"
    "ds_read_b64_tr_b16 v[134:135], v14 offset:3072\n"
    "ds_read_b64_tr_b16 v[136:137], v10 offset:3584\n"
    "ds_read_b64_tr_b16 v[138:139], v10 offset:4608\n"
    "ds_read_b64_tr_b16 v[140:141], v10 offset:5632\n"
    "ds_read_b64_tr_b16 v[142:143], v14 offset:6656\n"
    "ds_read_b64_tr_b16 v[144:145], v10 offset:7168\n"
    "ds_read_b64_tr_b16 v[146:147], v10 offset:8192\n"
    "ds_read_b64_tr_b16 v[148:149], v10 offset:9216\n"
    "ds_read_b64_tr_b16 v[150:151], v14 offset:10240\n"
    "ds_read_b64_tr_b16 v[152:153], v10 offset:10752\n"
    "ds_read_b64_tr_b16 v[154:155], v10 offset:11776\n"
    "ds_read_b64_tr_b16 v[156:157], v10 offset:12800\n"
    "ds_read_b64_tr_b16 v[158:159], v14 offset:13824\n"
    "s_xor_b32 s60, s44, 1\n"
    "s_mul_i32 s61, s60, 0x3800\n"
    "s_add_u32 s61, s61, s66\n"
    "v_add_u32 v11, s61, v4\n"
    "v_add_u32 v15, s61, v112\n"
    "s_lshl_b32 s60, s60, 13\n"
    "s_add_u32 s60, s60, 0x1c000\n"
    "v_add_u32 v57, s60, v1\n"
    "ds_read_b64_tr_b16 v[160:161], v11\n"
    "ds_read_b64_tr_b16 v[162:163], v11 offset:1024\n"
    "ds_read_b64_tr_b16 v[164:165], v11 offset:2048\n"
    "ds_read_b64_tr_b16 v[166:167], v15 offset:3072\n"
    "ds_read_b64_tr_b16 v[168:169], v11 offset:3584\n"
    "ds_read_b64_tr_b16 v[170:171], v11 offset:4608\n"
    "ds_read_b64_tr_b16 v[172:173], v11 offset:5632\n"
    "ds_read_b64_tr_b16 v[174:175], v15 offset:6656\n"
    "ds_read_b64_tr_b16 v[176:177], v11 offset:7168\n"
    "ds_read_b64_tr_b16 v[178:179], v11 offset:8192\n"
    "ds_read_b64_tr_b16 v[180:181], v11 offset:9216\n"
    "ds_read_b64_tr_b16 v[182:183], v15 offset:10240\n"
    "ds_read_b64_tr_b16 v[184:185], v11 offset:10752\n"
    "ds_read_b64_tr_b16 v[186:187], v11 offset:11776\n"
    "ds_read_b64_tr_b16 v[188:189], v11 offset:12800\n"
    "ds_read_b64_tr_b16 v[190:191], v15 offset:13824\n"
    "s_xor_b32 s60, s44, 2\n"
    "s_mul_i32 s61, s60, 0x3800\n"
    "s_add_u32 s61, s61, s66\n"
    "v_add_u32 v12, s61, v4\n"
    "v_add_u32 v16, s61, v112\n"
    "s_lshl_b32 s60, s60, 13\n"
    "s_add_u32 s60, s60, 0x1c000\n"
    "v_add_u32 v58, s60, v1\n"
    "ds_read_b64_tr_b16 v[192:193], v12\n"
    "ds_read_b64_tr_b16 v[194:195], v12 offset:1024\n"
    "ds_read_b64_tr_b16 v[196:197], v12 offset:2048\n"
    "ds_read_b64_tr_b16 v[198:199], v16 offset:3072\n"
    "ds_read_b64_tr_b16 v[200:201], v12 offset:3584\n"
    "ds_read_b64_tr_b16 v[202:203], v12 offset:4608\n"
    "ds_read_b64_tr_b16 v[204:205], v12 offset:5632\n"
    "ds_read_b64_tr_b16 v[206:207], v16 offset:6656\n"
    "ds_read_b64_tr_b16 v[208:209], v12 offset:7168\n"
    "ds_read_b64_tr_b16 v[210:211], v12 offset:8192\n"
    "ds_read_b64_tr_b16 v[212:213], v12 offset:9216\n"
    "ds_read_b64_tr_b16 v[214:215], v16 offset:10240\n"
    "ds_read_b64_tr_b16 v[216:217], v12 offset:10752\n"
    "ds_read_b64_tr_b16 v[218:219], v12 offset:11776\n"
    "ds_read_b64_tr_b16 v[220:221], v12 offset:12800\n"
    "ds_read_b64_tr_b16 v[222:223], v16 offset:13824\n"
    "s_xor_b32 s60, s44, 3\n"
    "s_mul_i32 s61, s60, 0x3800\n"
    "s_add_u32 s61, s61, s66\n"
    "v_add_u32 v13, s61, v4\n"
    "v_add_u32 v17, s61, v112\n"
    "s_lshl_b32 s60, s60, 13\n"
    "s_add_u32 s60, s60, 0x1c000\n"
    "v_add_u32 v59, s60, v1\n"
    "ds_read_b64_tr_b16 v[224:225], v13\n"
    "ds_read_b64_tr_b16 v[226:227], v13 offset:1024\n"
    "ds_read_b64_tr_b16 v[228:229], v13 offset:2048\n"
    "ds_read_b64_tr_b16 v[230:231], v17 offset:3072\n"
    "ds_read_b64_tr_b16 v[232:233], v13 offset:3584\n"
    "ds_read_b64_tr_b16 v[234:235], v13 offset:4608\n"
    "ds_read_b64_tr_b16 v[236:237], v13 offset:5632\n"
    "ds_read_b64_tr_b16 v[238:239], v17 offset:6656\n"
    "ds_read_b64_tr_b16 v[240:241], v13 offset:7168\n"
    "ds_read_b64_tr_b16 v[242:243], v13 offset:8192\n"
    "ds_read_b64_tr_b16 v[244:245], v13 offset:9216\n"
    "ds_read_b64_tr_b16 v[246:247], v17 offset:10240\n"
    "ds_read_b64_tr_b16 v[248:249], v13 offset:10752\n"
    "ds_read_b64_tr_b16 v[250:251], v13 offset:11776\n"
    "ds_read_b64_tr_b16 v[252:253], v13 offset:12800\n"
    "ds_read_b64_tr_b16 v[254:255], v17 offset:13824\n"
    "s_cmp_eq_u32 s77, 1\n"
    "s_cbranch_scc1 L_o2\n"
    "s_mov_b32 m0, s76\n"
    "s_nop 0\n"
    "buffer_load_dwordx4 v1, s[12:15], s74 offen sc1 lds\n"
    "buffer_load_dwordx4 v1, s[12:15], s74 offen offset:1024 sc1 lds\n"
    "buffer_load_dwordx4 v1, s[12:15], s74 offen offset:2048 sc1 lds\n"
    "buffer_load_dwordx4 v1, s[12:15], s74 offen offset:3072 sc1 lds\n"
    "s_add_u32 m0, s76, 0x1000\n"
    "s_nop 0\n"
    "buffer_load_dwordx4 v1, s[12:15], s75 offen sc1 lds\n"
    "buffer_load_dwordx4 v1, s[12:15], s75 offen offset:1024 sc1 lds\n"
    "buffer_load_dwordx4 v1, s[12:15], s75 offen offset:2048 sc1 lds\n"
    "buffer_load_dwordx4 v1, s[12:15], s75 offen offset:3072 sc1 lds\n"
    "L_o2:\n"
    "s_mov_b32 s58, 0xcccccccc\n"
    "s_mov_b32 s59, 0xcccccccc\n"
    "s_mul_i32 s60, s44, 0x1c0000\n"
    "s_lshl_b32 s61, s55, 7\n"
    "s_add_u32 s60, s60, s61\n"
    "v_lshlrev_b32 v117, 12, v62\n"
    "v_add3_u32 v10, v117, v63, s60\n"
    "v_add_u32 v11, 0x7c00, v10\n"
    "v_add_u32 v12, 0x7c00, v11\n"
    "v_add_u32 v13, 0x7c00, v12\n"
    "v_add_u32 v18, s68, v4\n"
    "v_add_u32 v19, s68, v5\n"
    "s_lshl_b32 s2, s2, 11\n"
    "s_waitcnt lgkmcnt(0)\n"
    "s_barrier\n"
    "s_add_u32 m0, s68, 0\n"
    "s_mov_b32 s71, 0\n"
    "buffer_load_dwordx4 v10, s[24:27], s71 offen lds\n"
    "buffer_load_dwordx4 v11, s[24:27], s71 offen offset:1024 lds\n"
    "buffer_load_dwordx4 v12, s[24:27], s71 offen offset:2048 lds\n"
    "buffer_load_dwordx4 v13, s[24:27], s71 offen offset:3072 lds\n"
    "s_lshl_b32 s60, s44, 2\n"
    "s_add_u32 s60, s60, 0x24000\n"
    "v_add_u32 v96, s60, v1\n"
    "v_add_u32 v97, 0x24000, v1\n"
    "s_lshl_b32 s80, s50, 11\n"
    "s_add_u32 s80, s80, 0x81000\n"
    "v_lshlrev_b32 v98, 3, v0\n"
    "s_lshl_b32 s60, s44, 8\n"
    "s_add_u32 s60, s60, 0x24400\n"
    "v_lshl_add_u32 v100, v60, 7, s60\n"
    "v_and_b32 v101, 1, v0\n"
    "v_lshl_add_u32 v99, v101, 6, v100\n"
    "v_bfe_u32 v101, v0, 1, 4\n"
    "v_lshl_add_u32 v99, v101, 2, v99\n"
    "s_lshr_b32 s60, s44, 1\n"
    "s_lshl1_add_u32 s46, s53, s60\n"
    "s_xor_b32 s47, s46, 1\n"
    "v_and_b32 v14, 7, v0\n"
    "s_lshl4_add_u32 s60, s53, s44\n"
    "s_lshl_b32 s60, s60, 14\n"
    "s_add_u32 s60, s60, 0x7207f0\n"
    "s_and_b32 s61, s55, 7\n"
    "s_lshl_b32 s61, s61, 7\n"
    "s_sub_u32 s60, s60, s61\n"
    "v_lshl_add_u32 v14, v14, 11, s60\n"
    "s_lshl3_add_u32 s60, s46, s44\n"
    "s_lshl_b32 s60, s60, 14\n"
    "s_add_u32 s60, s60, 0x700000\n"
    "s_cmp_lt_u32 s56, 7\n"
    "s_cselect_b32 s60, s60, 0x70000000\n"
    "v_add_u32 v20, s60, v1\n"
    "s_lshl3_add_u32 s60, s47, s44\n"
    "s_lshl_b32 s60, s60, 14\n"
    "s_add_u32 s60, s60, 0x700000\n"
    "s_cmp_lt_u32 s56, 7\n"
    "s_cselect_b32 s60, s60, 0x70000000\n"
    "v_add_u32 v21, s60, v1\n"
    "s_lshl_b32 s67, s54, 1\n"
    "s_add_u32 s67, s67, s65\n"
    "s_lshl_b32 s67, s67, 6\n"
    "v_lshlrev_b32 v15, 3, v60\n"
    "v_mov_b32 v16, 0xc0135761\n"
    "v_mov_b32 v17, 0xbdd2d3e8\n"
    "s_cmp_eq_u32 s77, 1\n"
    "s_cbranch_scc1 L_o3\n"
    "s_waitcnt vmcnt(4)\n"
    "L_o3:\n"
    "s_waitcnt vmcnt(9)\n"
    "ds_read_b128 v[24:27], v56\n"
    "ds_read_b128 v[28:31], v56 offset:1024\n"
    "ds_read_b128 v[32:35], v56 offset:2048\n"
    "ds_read_b128 v[36:39], v56 offset:3072\n"
    "ds_read_b128 v[40:43], v56 offset:4096\n"
    "ds_read_b128 v[44:47], v56 offset:5120\n"
    "ds_read_b128 v[48:51], v56 offset:6144\n"
    "ds_read_b128 v[52:55], v56 offset:7168\n"
    "s_waitcnt lgkmcnt(0)\n"
    "v_max3_u32 v101, v24, v28, v32\n"
    "v_max3_u32 v101, v101, v36, v40\n"
    "v_max3_u32 v101, v101, v44, v48\n"
    "v_max_u32 v101, v101, v52\n"
    "v_cmp_eq_u32 vcc, -1, v101\n"
    "s_nop 0\n"
    "s_cbranch_vccnz Lagain_4\n"
    "Lback_5:\n"
    "s_mov_b32 vcc_lo, 0xaaaaaaaa\n"
    "s_mov_b32 vcc_hi, 0xaaaaaaaa\n"
    "v_mfma_f32_32x32x16_bf16 v[80:95], v[24:27], v[128:131], v[80:95]\n"
    "v_mfma_f32_32x32x16_bf16 v[80:95], v[28:31], v[132:135], v[80:95]\n"
    "v_mfma_f32_32x32x16_bf16 v[80:95], v[32:35], v[136:139], v[80:95]\n"
    "v_mfma_f32_32x32x16_bf16 v[80:95], v[36:39], v[140:143], v[80:95]\n"
    "s_barrier\n"
    "ds_read_b128 v[24:27], v57\n"
    "ds_read_b128 v[28:31], v57 offset:1024\n"
    "ds_read_b128 v[32:35], v57 offset:2048\n"
    "v_mfma_f32_32x32x16_bf16 v[80:95], v[40:43], v[144:147], v[80:95]\n"
    "ds_read_b128 v[36:39], v57 offset:3072\n"
    "s_add_u32 m0, s68, 0x1000\n"
    "s_mov_b32 s71, 0x20000\n"
    "buffer_load_dwordx4 v10, s[24:27], s71 offen lds\n"
    "v_mfma_f32_32x32x16_bf16 v[80:95], v[44:47], v[148:151], v[80:95]\n"
    "ds_read_b128 v[40:43], v57 offset:4096\n"
    "v_mfma_f32_32x32x16_bf16 v[80:95], v[48:51], v[152:155], v[80:95]\n"
    "ds_read_b128 v[44:47], v57 offset:5120\n"
    "buffer_load_dwordx4 v11, s[24:27], s71 offen offset:1024 lds\n"
    "v_mfma_f32_32x32x16_bf16 v[80:95], v[52:55], v[156:159], v[80:95]\n"
    "ds_read_b128 v[48:51], v57 offset:6144\n"
    "s_waitcnt lgkmcnt(6)\n"
    "v_mfma_f32_32x32x16_bf16 v[80:95], v[24:27], v[160:163], v[80:95]\n"
    "ds_read_b128 v[52:55], v57 offset:7168\n"
    "buffer_load_dwordx4 v12, s[24:27], s71 offen offset:2048 lds\n"
    "s_waitcnt lgkmcnt(6)\n"
    "v_mfma_f32_32x32x16_bf16 v[80:95], v[28:31], v[164:167], v[80:95]\n"
    "ds_read_b128 v[24:27], v58\n"
    "s_waitcnt lgkmcnt(6)\n"
    "v_mfma_f32_32x32x16_bf16 v[80:95], v[32:35], v[168:171], v[80:95]\n"
    "ds_read_b128 v[28:31], v58 offset:1024\n"
    "buffer_load_dwordx4 v13, s[24:27], s71 offen offset:3072 lds\n"
    "s_waitcnt lgkmcnt(6)\n"
    "v_mfma_f32_32x32x16_bf16 v[80:95], v[36:39], v[172:175], v[80:95]\n"
    "ds_read_b128 v[32:35], v58 offset:2048\n"
    "s_waitcnt lgkmcnt(6)\n"
    "v_mfma_f32_32x32x16_bf16 v[80:95], v[40:43], v[176:179], v[80:95]\n"
    "ds_read_b128 v[36:39], v58 offset:3072\n"
    "s_add_u32 m0, s68, 0x2000\n"
    "s_mov_b32 s71, 0x40000\n"
    "buffer_load_dwordx4 v10, s[24:27], s71 offen lds\n"
    "s_waitcnt lgkmcnt(6)\n"
    "v_mfma_f32_32x32x16_bf16 v[80:95], v[44:47], v[180:183], v[80:95]\n"
    "ds_read_b128 v[40:43], v58 offset:4096\n"
    "s_waitcnt lgkmcnt(6)\n"
    "v_mfma_f32_32x32x16_bf16 v[80:95], v[48:51], v[184:187], v[80:95]\n"
    "ds_read_b128 v[44:47], v58 offset:5120\n"
    "buffer_load_dwordx4 v11, s[24:27], s71 offen offset:1024 lds\n"
    "s_waitcnt lgkmcnt(6)\n"
    "v_mfma_f32_32x32x16_bf16 v[80:95], v[52:55], v[188:191], v[80:95]\n"
    "ds_read_b128 v[48:51], v58 offset:6144\n"
    "s_waitcnt lgkmcnt(6)\n"
    "v_mfma_f32_32x32x16_bf16 v[80:95], v[24:27], v[192:195], v[80:95]\n"
    "ds_read_b128 v[52:55], v58 offset:7168\n"
    "buffer_load_dwordx4 v12, s[24:27], s71 offen offset:2048 lds\n"
    "s_waitcnt lgkmcnt(6)\n"
    "v_mfma_f32_32x32x16_bf16 v[80:95], v[28:31], v[196:199], v[80:95]\n"
    "ds_read_b128 v[24:27], v59\n"
    "s_waitcnt lgkmcnt(6)\n"
    "v_mfma_f32_32x32x16_bf16 v[80:95], v[32:35], v[200:203], v[80:95]\n"
    "ds_read_b128 v[28:31], v59 offset:1024\n"
    "buffer_load_dwordx4 v13, s[24:27], s71 offen offset:3072 lds\n"
    "s_waitcnt lgkmcnt(6)\n"
    "v_mfma_f32_32x32x16_bf16 v[80:95], v[36:39], v[204:207], v[80:95]\n"
    "ds_read_b128 v[32:35], v59 offset:2048\n"
    "s_waitcnt lgkmcnt(6)\n"
    "v_mfma_f32_32x32x16_bf16 v[80:95], v[40:43], v[208:211], v[80:95]\n"
    "ds_read_b128 v[36:39], v59 offset:3072\n"
    "s_add_u32 m0, s68, 0x3000\n"
    "s_mov_b32 s71, 0x60000\n"
    "buffer_load_dwordx4 v10, s[24:27], s71 offen lds\n"
    "s_waitcnt lgkmcnt(6)\n"
    "v_mfma_f32_32x32x16_bf16 v[80:95], v[44:47], v[212:215], v[80:95]\n"
    "ds_read_b128 v[40:43], v59 offset:4096\n"
    "s_waitcnt lgkmcnt(6)\n"
    "v_mfma_f32_32x32x16_bf16 v[80:95], v[48:51], v[216:219], v[80:95]\n"
    "ds_read_b128 v[44:47], v59 offset:5120\n"
    "buffer_load_dwordx4 v11, s[24:27], s71 offen offset:1024 lds\n"
    "s_waitcnt lgkmcnt(6)\n"
    "v_mfma_f32_32x32x16_bf16 v[80:95], v[52:55], v[220:223], v[80:95]\n"
    "ds_read_b128 v[48:51], v59 offset:6144\n"
    "s_waitcnt lgkmcnt(6)\n"
    "v_mfma_f32_32x32x16_bf16 v[80:95], v[24:27], v[224:227], v[80:95]\n"
    "ds_read_b128 v[52:55], v59 offset:7168\n"
    "buffer_load_dwordx4 v12, s[24:27], s71 offen offset:2048 lds\n"
    "s_waitcnt lgkmcnt(6)\n"
    "v_mfma_f32_32x32x16_bf16 v[80:95], v[28:31], v[228:231], v[80:95]\n"
    "s_waitcnt lgkmcnt(5)\n"
    "v_mfma_f32_32x32x16_bf16 v[80:95], v[32:35], v[232:235], v[80:95]\n"
    "buffer_load_dwordx4 v13, s[24:27], s71 offen offset:3072 lds\n"
    "s_waitcnt lgkmcnt(4)\n"
    "v_mfma_f32_32x32x16_bf16 v[80:95], v[36:39], v[236:239], v[80:95]\n"
    "s_waitcnt lgkmcnt(3)\n"
    "v_mfma_f32_32x32x16_bf16 v[80:95], v[40:43], v[240:243], v[80:95]\n"
    "s_add_u32 m0, s68, 0x4000\n"
    "s_mov_b32 s71, 0x80000\n"
    "buffer_load_dwordx4 v10, s[24:27], s71 offen lds\n"
    "s_waitcnt lgkmcnt(2)\n"
    "v_mfma_f32_32x32x16_bf16 v[80:95], v[44:47], v[244:247], v[80:95]\n"
    "s_waitcnt lgkmcnt(1)\n"
    "v_mfma_f32_32x32x16_bf16 v[80:95], v[48:51], v[248:251], v[80:95]\n"
    "buffer_load_dwordx4 v11, s[24:27], s71 offen offset:1024 lds\n"
    "s_waitcnt lgkmcnt(0)\n"
    "v_mfma_f32_32x32x16_bf16 v[80:95], v[52:55], v[252:255], v[80:95]\n"
    "s_nop 11\n"
    "v_pk_mul_f32 v[144:145], v[80:81], v[80:81]\n"
    "v_pk_mul_f32 v[146:147], v[82:83], v[82:83]\n"
    "v_pk_mul_f32 v[148:149], v[84:85], v[84:85]\n"
    "v_pk_mul_f32 v[150:151], v[86:87], v[86:87]\n"
    "v_pk_mul_f32 v[152:153], v[88:89], v[88:89]\n"
    "v_pk_mul_f32 v[154:155], v[90:91], v[90:91]\n"
    "v_pk_mul_f32 v[156:157], v[92:93], v[92:93]\n"
    "v_pk_mul_f32 v[158:159], v[94:95], v[94:95]\n"
    "v_mov_b64 v[128:129], v[80:81]\n"
    "v_mov_b64 v[130:131], v[82:83]\n"
    "v_mov_b64 v[132:133], v[84:85]\n"
    "v_mov_b64 v[134:135], v[86:87]\n"
    "v_mov_b64 v[136:137], v[88:89]\n"
    "v_mov_b64 v[138:139], v[90:91]\n"
    "v_mov_b64 v[140:141], v[92:93]\n"
    "v_mov_b64 v[142:143], v[94:95]\n"
    "v_permlane16_swap_b32 v144, v152\n"
    "v_permlane16_swap_b32 v145, v153\n"
    "v_permlane16_swap_b32 v146, v154\n"
    "v_permlane16_swap_b32 v147, v155\n"
    "v_permlane16_swap_b32 v148, v156\n"
    "v_permlane16_swap_b32 v149, v157\n"
    "v_permlane16_swap_b32 v150, v158\n"
    "v_permlane16_swap_b32 v151, v159\n"
    "v_permlane16_swap_b32 v128, v136\n"
    "v_permlane16_swap_b32 v129, v137\n"
    "v_permlane16_swap_b32 v130, v138\n"
    "v_permlane16_swap_b32 v131, v139\n"
    "v_permlane16_swap_b32 v132, v140\n"
    "v_permlane16_swap_b32 v133, v141\n"
    "v_permlane16_swap_b32 v134, v142\n"
    "v_permlane16_swap_b32 v135, v143\n"
    "v_pk_add_f32 v[144:145], v[144:145], v[152:153]\n"
    "v_pk_add_f32 v[146:147], v[146:147], v[154:155]\n"
    "v_pk_add_f32 v[148:149], v[148:149], v[156:157]\n"
    "v_pk_add_f32 v[150:151], v[150:151], v[158:159]\n"
    "v_pk_add_f32 v[128:129], v[128:129], v[136:137]\n"
    "v_pk_add_f32 v[130:131], v[130:131], v[138:139]\n"
    "v_pk_add_f32 v[132:133], v[132:133], v[140:141]\n"
    "v_pk_add_f32 v[134:135], v[134:135], v[142:143]\n"
    "v_add_f32_dpp v128, v128, v128 row_mirror row_mask:0xf bank_mask:0x3\n"
    "v_add_f32_dpp v144, v144, v144 row_mirror row_mask:0xf bank_mask:0x3\n"
    "v_add_f32_dpp v129, v129, v129 row_mirror row_mask:0xf bank_mask:0x3\n"
    "v_add_f32_dpp v145, v145, v145 row_mirror row_mask:0xf bank_mask:0x3\n"
    "v_add_f32_dpp v130, v130, v130 row_mirror row_mask:0xf bank_mask:0x3\n"
    "v_add_f32_dpp v146, v146, v146 row_mirror row_mask:0xf bank_mask:0x3\n"
    "v_add_f32_dpp v131, v131, v131 row_mirror row_mask:0xf bank_mask:0x3\n"
    "v_add_f32_dpp v147, v147, v147 row_mirror row_mask:0xf bank_mask:0x3\n"
    "v_add_f32_dpp v128, v132, v132 row_mirror row_mask:0xf bank_mask:0xc\n"
    "v_add_f32_dpp v144, v148, v148 row_mirror row_mask:0xf bank_mask:0xc\n"
    "v_add_f32_dpp v129, v133, v133 row_mirror row_mask:0xf bank_mask:0xc\n"
    "v_add_f32_dpp v145, v149, v149 row_mirror row_mask:0xf bank_mask:0xc\n"
    "v_add_f32_dpp v130, v134, v134 row_mirror row_mask:0xf bank_mask:0xc\n"
    "v_add_f32_dpp v146, v150, v150 row_mirror row_mask:0xf bank_mask:0xc\n"
    "v_add_f32_dpp v131, v135, v135 row_mirror row_mask:0xf bank_mask:0xc\n"
    "v_add_f32_dpp v147, v151, v151 row_mirror row_mask:0xf bank_mask:0xc\n"
    "v_add_f32_dpp v128, v128, v128 row_half_mirror row_mask:0xf bank_mask:0x5\n"
    "v_add_f32_dpp v144, v144, v144 row_half_mirror row_mask:0xf bank_mask:0x5\n"
    "v_add_f32_dpp v129, v129, v129 row_half_mirror row_mask:0xf bank_mask:0x5\n"
    "v_add_f32_dpp v145, v145, v145 row_half_mirror row_mask:0xf bank_mask:0x5\n"
    "v_add_f32_dpp v128, v130, v130 row_half_mirror row_mask:0xf bank_mask:0xa\n"
    "v_add_f32_dpp v144, v146, v146 row_half_mirror row_mask:0xf bank_mask:0xa\n"
    "v_add_f32_dpp v129, v131, v131 row_half_mirror row_mask:0xf bank_mask:0xa\n"
    "v_add_f32_dpp v145, v147, v147 row_half_mirror row_mask:0xf bank_mask:0xa\n"
    "v_add_f32_dpp v128, v128, v128 quad_perm:[2,3,0,1] row_mask:0xf bank_mask:0xf\n"
    "v_add_f32_dpp v144, v144, v144 quad_perm:[2,3,0,1] row_mask:0xf bank_mask:0xf\n"
    "v_add_f32_dpp v129, v129, v129 quad_perm:[2,3,0,1] row_mask:0xf bank_mask:0xf\n"
    "v_add_f32_dpp v145, v145, v145 quad_perm:[2,3,0,1] row_mask:0xf bank_mask:0xf\n"
    "v_cndmask_b32 v128, v128, v129, s[58:59]\n"
    "v_cndmask_b32 v144, v144, v145, s[58:59]\n"
    "s_nop 0\n"
    "v_add_f32_dpp v128, v128, v128 quad_perm:[1,0,3,2] row_mask:0xf bank_mask:0xf\n"
    "v_add_f32_dpp v144, v144, v144 quad_perm:[1,0,3,2] row_mask:0xf bank_mask:0xf\n"
    "v_cndmask_b32 v128, v128, v144, vcc\n"
    "v_and_b32 v128, s83, v128\n"
    "ds_write_b32 v96, v128\n"
    "s_waitcnt vmcnt(18) lgkmcnt(0)\n"
    "s_barrier\n"
    "ds_read_b128 v[104:107], v97\n"
    "s_waitcnt lgkmcnt(0)\n"
    "v_pk_add_f32 v[104:105], v[104:105], v[106:107]\n"
    "v_add_f32 v104, v104, v105\n"
    "v_mov_b32 v105, v22\n"
    "buffer_store_dwordx2 v[104:105], v98, s[12:15], s79 offen sc1\n"
    "v_cvt_pk_bf16_f32 v168, v80, v81\n"
    "v_cvt_pk_bf16_f32 v169, v82, v83\n"
    "v_cvt_pk_bf16_f32 v170, v84, v85\n"
    "v_cvt_pk_bf16_f32 v171, v86, v87\n"
    "v_cvt_pk_bf16_f32 v172, v88, v89\n"
    "v_cvt_pk_bf16_f32 v173, v90, v91\n"
    "v_cvt_pk_bf16_f32 v174, v92, v93\n"
    "v_cvt_pk_bf16_f32 v175, v94, v95\n"
    "s_mov_b32 s60, 0x100000\n"
    "s_sleep 4\n"
    "buffer_load_dwordx2 v[108:109], v98, s[12:15], s80 offen sc1\n"
    "buffer_load_dwordx2 v[110:111], v98, s[12:15], s80 offen offset:512 sc1\n"
    "buffer_load_dwordx2 v[112:113], v98, s[12:15], s80 offen offset:1024 sc1\n"
    "buffer_load_dwordx2 v[114:115], v98, s[12:15], s80 offen offset:1536 sc1\n"
    "s_waitcnt vmcnt(0)\n"
    "s_branch L_schk\n"
    "Lspoll_6:\n"
    "buffer_load_dwordx2 v[108:109], v98, s[12:15], s80 offen sc1\n"
    "buffer_load_dwordx2 v[110:111], v98, s[12:15], s80 offen offset:512 sc1\n"
    "buffer_load_dwordx2 v[112:113], v98, s[12:15], s80 offen offset:1024 sc1\n"
    "buffer_load_dwordx2 v[114:115], v98, s[12:15], s80 offen offset:1536 sc1\n"
    "s_waitcnt vmcnt(0)\n"
    "L_schk:\n"
    "v_xor_b32 v116, v109, v22\n"
    "v_xor_b32 v117, v111, v22\n"
    "v_xor_b32 v118, v113, v22\n"
    "v_xor_b32 v119, v115, v22\n"
    "v_or3_b32 v116, v116, v117, v118\n"
    "v_or_b32 v116, v116, v119\n"
    "v_cmp_ne_u32 s[58:59], 0, v116\n"
    "v_add_f32 v120, v108, v110\n"
    "v_add_f32 v121, v112, v114\n"
    "v_add_f32 v120, v120, v121\n"
    "s_cmp_eq_u64 s[58:59], 0\n"
    "s_cbranch_scc1 Lsdone_7\n"
    "s_sub_u32 s60, s60, 1\n"
    "s_sleep 1\n"
    "s_cbranch_scc0 Lspoll_6\n"
    "Lsdone_7:\n"
    "v_mov_b32_dpp v121, v120 quad_perm:[1,0,3,2] row_mask:0xf bank_mask:0xf\n"
    "v_cndmask_b32 v122, v120, v121, vcc\n"
    "v_cndmask_b32 v123, v121, v120, vcc\n"
    "v_mul_f32 v122, 0x3b124925, v122\n"
    "v_mul_f32 v123, 0x3b124925, v123\n"
    "v_fma_f32 v123, -v122, v122, v123\n"
    "v_max_f32 v123, 0, v123\n"
    "v_add_f32 v120, 0x3727c5ac, v123\n"
    "v_rsq_f32 v120, v120\n"
    "v_mov_b32 v121, 0x3727c5ac\n"
    "v_mul_f32 v123, v123, v120\n"
    "v_fma_f32 v123, v123, v120, v121\n"
    "v_rsq_f32 v123, v123\n"
    "v_mul_f32 v122, v122, v120\n"
    "v_cndmask_b32 v120, v120, v122, vcc\n"
    "v_mul_f32 v120, v120, v123\n"
    "ds_write_b32 v99, v120\n"
    "ds_read_b128 v[128:131], v100\n"
    "ds_read_b128 v[144:147], v100 offset:64\n"
    "ds_read_b128 v[132:135], v100 offset:16\n"
    "ds_read_b128 v[148:151], v100 offset:80\n"
    "ds_read_b128 v[136:139], v100 offset:32\n"
    "ds_read_b128 v[152:155], v100 offset:96\n"
    "ds_read_b128 v[140:143], v100 offset:48\n"
    "ds_read_b128 v[156:159], v100 offset:112\n"
    "s_waitcnt lgkmcnt(4)\n"
    "v_pk_fma_f32 v[128:129], v[80:81], v[128:129], v[144:145] neg_lo:[0,0,1] neg_hi:[0,0,1]\n"
    "v_pk_fma_f32 v[130:131], v[82:83], v[130:131], v[146:147] neg_lo:[0,0,1] neg_hi:[0,0,1]\n"
    "v_pk_fma_f32 v[132:133], v[84:85], v[132:133], v[148:149] neg_lo:[0,0,1] neg_hi:[0,0,1]\n"
    "v_pk_fma_f32 v[134:135], v[86:87], v[134:135], v[150:151] neg_lo:[0,0,1] neg_hi:[0,0,1]\n"
    "v_cvt_pk_bf16_f32 v160, v128, v129\n"
    "v_cvt_pk_bf16_f32 v161, v130, v131\n"
    "v_cvt_pk_bf16_f32 v162, v132, v133\n"
    "v_cvt_pk_bf16_f32 v163, v134, v135\n"
    "buffer_store_dwordx4 v[160:163], v1, s[12:15], s81 offen sc1\n"
    "s_waitcnt lgkmcnt(0)\n"
    "v_pk_fma_f32 v[136:137], v[88:89], v[136:137], v[152:153] neg_lo:[0,0,1] neg_hi:[0,0,1]\n"
    "v_pk_fma_f32 v[138:139], v[90:91], v[138:139], v[154:155] neg_lo:[0,0,1] neg_hi:[0,0,1]\n"
    "v_pk_fma_f32 v[140:141], v[92:93], v[140:141], v[156:157] neg_lo:[0,0,1] neg_hi:[0,0,1]\n"
    "v_pk_fma_f32 v[142:143], v[94:95], v[142:143], v[158:159] neg_lo:[0,0,1] neg_hi:[0,0,1]\n"
    "v_cvt_pk_bf16_f32 v164, v136, v137\n"
    "v_cvt_pk_bf16_f32 v165, v138, v139\n"
    "v_cvt_pk_bf16_f32 v166, v140, v141\n"
    "v_cvt_pk_bf16_f32 v167, v142, v143\n"
    "buffer_store_dwordx4 v[164:167], v1, s[12:15], s81 offen offset:1024 sc1\n"
    "buffer_store_dwordx4 v[168:171], v1, s[12:15], s82 offen sc1\n"
    "buffer_store_dwordx4 v[172:175], v1, s[12:15], s82 offen offset:1024 sc1\n"
    "s_cmp_lt_u32 s56, 8\n"
    "s_cbranch_scc0 L_idle\n"
    "s_cmp_eq_u32 s77, 1\n"
    "s_cbranch_scc0 L_h1\n"
    "buffer_load_dword v27, v14, s[12:15], 0 offen sc1\n"
    "L_h1:\n"
    "buffer_load_dwordx2 v[28:29], v15, s[28:31], s67 offen\n"
    "buffer_load_dwordx2 v[30:31], v15, s[28:31], s67 offen offset:16\n"
    "buffer_load_dwordx2 v[32:33], v15, s[28:31], s67 offen offset:32\n"
    "buffer_load_dwordx2 v[34:35], v15, s[28:31], s67 offen offset:48\n"
    "buffer_load_dwordx4 v12, s[24:27], s71 offen offset:2048 lds\n"
    "buffer_load_dwordx4 v13, s[24:27], s71 offen offset:3072 lds\n"
    "s_add_u32 m0, s68, 0x5000\n"
    "s_mov_b32 s71, 0xa0000\n"
    "buffer_load_dwordx4 v10, s[24:27], s71 offen lds\n"
    "buffer_load_dwordx4 v11, s[24:27], s71 offen offset:1024 lds\n"
    "buffer_load_dwordx4 v12, s[24:27], s71 offen offset:2048 lds\n"
    "buffer_load_dwordx4 v13, s[24:27], s71 offen offset:3072 lds\n"
    "s_add_u32 m0, s68, 0x6000\n"
    "s_mov_b32 s71, 0xc0000\n"
    "buffer_load_dwordx4 v10, s[24:27], s71 offen lds\n"
    "buffer_load_dwordx4 v11, s[24:27], s71 offen offset:1024 lds\n"
    "buffer_load_dwordx4 v12, s[24:27], s71 offen offset:2048 lds\n"
    "buffer_load_dwordx4 v13, s[24:27], s71 offen offset:3072 lds\n"
    "s_mov_b32 s73, 0x2000\n"
    "s_mov_b32 s74, 0x10000\n"
    "s_mov_b32 s75, 0x12000\n"
    "s_barrier\n"
    "s_cmp_eq_u32 s77, 1\n"
    "s_cbranch_scc0 L_h2\n"
    "s_waitcnt vmcnt(14)\n"
    "v_cmp_eq_u32 vcc, -1, v27\n"
    "s_nop 0\n"
    "s_cbranch_vccz L_h3\n"
    "L_h2:\n"
    "s_mov_b32 s60, 0x100000\n"
    "Lpoll_8:\n"
    "buffer_load_dword v24, v14, s[12:15], 0 offen sc1\n"
    "s_waitcnt vmcnt(0)\n"
    "v_cmp_eq_u32 vcc, -1, v24\n"
    "s_nop 0\n"
    "s_cbranch_vccz Ldone_9\n"
    "s_sub_u32 s60, s60, 1\n"
    "s_sleep 1\n"
    "s_cbranch_scc0 Lpoll_8\n"
    "Ldone_9:\n"
    "s_mov_b32 s77, 0\n"
    "L_h3:\n"
    "s_xor_b32 s78, s2, 0\n"
    "buffer_load_dwordx4 v[128:131], v20, s[12:15], s78 offen sc1\n"
    "buffer_load_dwordx4 v[132:135], v21, s[12:15], s78 offen sc1\n"
    "buffer_load_dwordx4 v[136:139], v20, s[12:15], s78 offen offset:1024 sc1\n"
    "buffer_load_dwordx4 v[140:143], v21, s[12:15], s78 offen offset:1024 sc1\n"
    "s_xor_b32 s78, s2, 0x800\n"
    "buffer_load_dwordx4 v[144:147], v20, s[12:15], s78 offen sc1\n"
    "buffer_load_dwordx4 v[148:151], v21, s[12:15], s78 offen sc1\n"
    "buffer_load_dwordx4 v[152:155], v20, s[12:15], s78 offen offset:1024 sc1\n"
    "buffer_load_dwordx4 v[156:159], v21, s[12:15], s78 offen offset:1024 sc1\n"
    "s_xor_b32 s78, s2, 0x1000\n"
    "buffer_load_dwordx4 v[160:163], v20, s[12:15], s78 offen sc1\n"
    "buffer_load_dwordx4 v[164:167], v21, s[12:15], s78 offen sc1\n"
    "buffer_load_dwordx4 v[168:171], v20, s[12:15], s78 offen offset:1024 sc1\n"
    "buffer_load_dwordx4 v[172:175], v21, s[12:15], s78 offen offset:1024 sc1\n"
    "s_xor_b32 s78, s2, 0x1800\n"
    "buffer_load_dwordx4 v[176:179], v20, s[12:15], s78 offen sc1\n"
    "buffer_load_dwordx4 v[180:183], v21, s[12:15], s78 offen sc1\n"
    "s_waitcnt vmcnt(24)\n"
    "v_lshlrev_b32 v64, 16, v28\n"
    "v_and_b32 v65, 0xffff0000, v28\n"
    "v_lshlrev_b32 v66, 16, v29\n"
    "v_and_b32 v67, 0xffff0000, v29\n"
    "v_lshlrev_b32 v68, 16, v30\n"
    "v_and_b32 v69, 0xffff0000, v30\n"
    "v_lshlrev_b32 v70, 16, v31\n"
    "v_and_b32 v71, 0xffff0000, v31\n"
    "v_lshlrev_b32 v72, 16, v32\n"
    "v_and_b32 v73, 0xffff0000, v32\n"
    "v_lshlrev_b32 v74, 16, v33\n"
    "v_and_b32 v75, 0xffff0000, v33\n"
    "v_lshlrev_b32 v76, 16, v34\n"
    "v_and_b32 v77, 0xffff0000, v34\n"
    "v_lshlrev_b32 v78, 16, v35\n"
    "v_and_b32 v79, 0xffff0000, v35\n"
    "s_waitcnt vmcnt(12)\n"
    "v_max_u32 v26, v128, v132\n"
    "v_cmp_eq_u32 vcc, -1, v26\n"
    "s_nop 0\n"
    "s_cbranch_vccnz Lagain_11\n"
    "Lback_12:\n"
    "v_mfma_f32_32x32x16_bf16 v[64:79], a[0:3], v[128:131], v[64:79]\n"
    "buffer_load_dwordx4 v[184:187], v20, s[12:15], s78 offen offset:1024 sc1\n"
    "v_mfma_f32_32x32x16_bf16 v[80:95], a[4:7], v[128:131], 0\n"
    "buffer_load_dwordx4 v[188:191], v21, s[12:15], s78 offen offset:1024 sc1\n"
    "v_mfma_f32_32x32x16_bf16 v[96:111], a[0:3], v[132:135], 0\n"
    "s_waitcnt vmcnt(12)\n"
    "v_max_u32 v26, v136, v140\n"
    "v_cmp_eq_u32 vcc, -1, v26\n"
    "v_mfma_f32_32x32x16_bf16 v[112:127], a[4:7], v[132:135], 0\n"
    "s_cbranch_vccnz Lagain_13\n"
    "Lback_14:\n"
    "v_mfma_f32_32x32x16_bf16 v[64:79], a[8:11], v[136:139], v[64:79]\n"
    "s_xor_b32 s78, s2, 0x2000\n"
    "buffer_load_dwordx4 v[192:195], v20, s[12:15], s78 offen sc1\n"
    "v_mfma_f32_32x32x16_bf16 v[80:95], a[12:15], v[136:139], v[80:95]\n"
    "buffer_load_dwordx4 v[196:199], v21, s[12:15], s78 offen sc1\n"
    "v_mfma_f32_32x32x16_bf16 v[96:111], a[8:11], v[140:143], v[96:111]\n"
    "s_waitcnt vmcnt(12)\n"
    "v_max_u32 v26, v144, v148\n"
    "v_cmp_eq_u32 vcc, -1, v26\n"
    "v_mfma_f32_32x32x16_bf16 v[112:127], a[12:15], v[140:143], v[112:127]\n"
    "s_cbranch_vccnz Lagain_15\n"
    "Lback_16:\n"
    "v_mfma_f32_32x32x16_bf16 v[64:79], a[16:19], v[144:147], v[64:79]\n"
    "buffer_load_dwordx4 v[200:203], v20, s[12:15], s78 offen offset:1024 sc1\n"
    "v_mfma_f32_32x32x16_bf16 v[80:95], a[20:23], v[144:147], v[80:95]\n"
    "buffer_load_dwordx4 v[204:207], v21, s[12:15], s78 offen offset:1024 sc1\n"
    "v_mfma_f32_32x32x16_bf16 v[96:111], a[16:19], v[148:151], v[96:111]\n"
    "ds_read_b64_tr_b16 a[0:1], v18\n"
    "ds_read_b64_tr_b16 a[2:3], v18 offset:1024\n"
    "ds_read_b64_tr_b16 a[4:5], v19\n"
    "ds_read_b64_tr_b16 a[6:7], v19 offset:1024\n"
    "s_waitcnt vmcnt(12)\n"
    "v_max_u32 v26, v152, v156\n"
    "v_cmp_eq_u32 vcc, -1, v26\n"
    "v_mfma_f32_32x32x16_bf16 v[112:127], a[20:23], v[148:151], v[112:127]\n"
    "s_cbranch_vccnz Lagain_17\n"
    "Lback_18:\n"
    "v_mfma_f32_32x32x16_bf16 v[64:79], a[24:27], v[152:155], v[64:79]\n"
    "s_xor_b32 s78, s2, 0x2800\n"
    "buffer_load_dwordx4 v[208:211], v20, s[12:15], s78 offen sc1\n"
    "v_mfma_f32_32x32x16_bf16 v[80:95], a[28:31], v[152:155], v[80:95]\n"
    "buffer_load_dwordx4 v[212:215], v21, s[12:15], s78 offen sc1\n"
    "v_mfma_f32_32x32x16_bf16 v[96:111], a[24:27], v[156:159], v[96:111]\n"
    "ds_read_b64_tr_b16 a[8:9], v18 offset:2048\n"
    "ds_read_b64_tr_b16 a[10:11], v18 offset:3072\n"
    "ds_read_b64_tr_b16 a[12:13], v19 offset:2048\n"
    "ds_read_b64_tr_b16 a[14:15], v19 offset:3072\n"
    "s_waitcnt vmcnt(12)\n"
    "v_max_u32 v26, v160, v164\n"
    "v_cmp_eq_u32 vcc, -1, v26\n"
    "v_mfma_f32_32x32x16_bf16 v[112:127], a[28:31], v[156:159], v[112:127]\n"
    "s_cbranch_vccnz Lagain_19\n"
    "Lback_20:\n"
    "v_mfma_f32_32x32x16_bf16 v[64:79], a[32:35], v[160:163], v[64:79]\n"
    "buffer_load_dwordx4 v[216:219], v20, s[12:15], s78 offen offset:1024 sc1\n"
    "v_mfma_f32_32x32x16_bf16 v[80:95], a[36:39], v[160:163], v[80:95]\n"
    "buffer_load_dwordx4 v[220:223], v21, s[12:15], s78 offen offset:1024 sc1\n"
    "v_mfma_f32_32x32x16_bf16 v[96:111], a[32:35], v[164:167], v[96:111]\n"
    "ds_read_b64_tr_b16 a[16:17], v18 offset:4096\n"
    "ds_read_b64_tr_b16 a[18:19], v18 offset:5120\n"
    "ds_read_b64_tr_b16 a[20:21], v19 offset:4096\n"
    "ds_read_b64_tr_b16 a[22:23], v19 offset:5120\n"
    "buffer_load_dword v25, v14, s[12:15], s74 offen sc1\n"
    "s_waitcnt vmcnt(13)\n"
    "v_max_u32 v26, v168, v172\n"
    "v_cmp_eq_u32 vcc, -1, v26\n"
    "v_mfma_f32_32x32x16_bf16 v[112:127], a[36:39], v[164:167], v[112:127]\n"
    "s_cbranch_vccnz Lagain_21\n"
    "Lback_22:\n"
    "v_mfma_f32_32x32x16_bf16 v[64:79], a[40:43], v[168:171], v[64:79]\n"
    "s_xor_b32 s78, s2, 0x3000\n"
    "buffer_load_dwordx4 v[224:227], v20, s[12:15], s78 offen sc1\n"
    "v_mfma_f32_32x32x16_bf16 v[80:95], a[44:47], v[168:171], v[80:95]\n"
    "buffer_load_dwordx4 v[228:231], v21, s[12:15], s78 offen sc1\n"
    "v_mfma_f32_32x32x16_bf16 v[96:111], a[40:43], v[172:175], v[96:111]\n"
    "ds_read_b64_tr_b16 a[24:25], v18 offset:6144\n"
    "ds_read_b64_tr_b16 a[26:27], v18 offset:7168\n"
    "ds_read_b64_tr_b16 a[28:29], v19 offset:6144\n"
    "ds_read_b64_tr_b16 a[30:31], v19 offset:7168\n"
    "s_waitcnt vmcnt(13)\n"
    "v_max_u32 v26, v176, v180\n"
    "v_cmp_eq_u32 vcc, -1, v26\n"
    "v_mfma_f32_32x32x16_bf16 v[112:127], a[44:47], v[172:175], v[112:127]\n"
    "s_cbranch_vccnz Lagain_23\n"
    "Lback_24:\n"
    "v_mfma_f32_32x32x16_bf16 v[64:79], a[48:51], v[176:179], v[64:79]\n"
    "buffer_load_dwordx4 v[232:235], v20, s[12:15], s78 offen offset:1024 sc1\n"
    "v_mfma_f32_32x32x16_bf16 v[80:95], a[52:55], v[176:179], v[80:95]\n"
    "buffer_load_dwordx4 v[236:239], v21, s[12:15], s78 offen offset:1024 sc1\n"
    "v_mfma_f32_32x32x16_bf16 v[96:111], a[48:51], v[180:183], v[96:111]\n"
    "ds_read_b64_tr_b16 a[32:33], v18 offset:8192\n"
    "ds_read_b64_tr_b16 a[34:35], v18 offset:9216\n"
    "ds_read_b64_tr_b16 a[36:37], v19 offset:8192\n"
    "ds_read_b64_tr_b16 a[38:39], v19 offset:9216\n"
    "s_waitcnt vmcnt(13)\n"
    "v_max_u32 v26, v184, v188\n"
    "v_cmp_eq_u32 vcc, -1, v26\n"
    "v_mfma_f32_32x32x16_bf16 v[112:127], a[52:55], v[180:183], v[112:127]\n"
    "s_cbranch_vccnz Lagain_25\n"
    "Lback_26:\n"
    "v_mfma_f32_32x32x16_bf16 v[64:79], a[56:59], v[184:187], v[64:79]\n"
    "s_xor_b32 s78, s2, 0x3800\n"
    "buffer_load_dwordx4 v[240:243], v20, s[12:15], s78 offen sc1\n"
    "v_mfma_f32_32x32x16_bf16 v[80:95], a[60:63], v[184:187], v[80:95]\n"
    "buffer_load_dwordx4 v[244:247], v21, s[12:15], s78 offen sc1\n"
    "v_mfma_f32_32x32x16_bf16 v[96:111], a[56:59], v[188:191], v[96:111]\n"
    "ds_read_b64_tr_b16 a[40:41], v18 offset:10240\n"
    "ds_read_b64_tr_b16 a[42:43], v18 offset:11264\n"
    "ds_read_b64_tr_b16 a[44:45], v19 offset:10240\n"
    "ds_read_b64_tr_b16 a[46:47], v19 offset:11264\n"
    "s_waitcnt vmcnt(13)\n"
    "v_max_u32 v26, v192, v196\n"
    "v_cmp_eq_u32 vcc, -1, v26\n"
    "v_mfma_f32_32x32x16_bf16 v[112:127], a[60:63], v[188:191], v[112:127]\n"
    "s_cbranch_vccnz Lagain_27\n"
    "Lback_28:\n"
    "v_mfma_f32_32x32x16_bf16 v[64:79], a[64:67], v[192:195], v[64:79]\n"
    "buffer_load_dwordx4 v[248:251], v20, s[12:15], s78 offen offset:1024 sc1\n"
    "v_mfma_f32_32x32x16_bf16 v[80:95], a[68:71], v[192:195], v[80:95]\n"
    "buffer_load_dwordx4 v[252:255], v21, s[12:15], s78 offen offset:1024 sc1\n"
    "v_mfma_f32_32x32x16_bf16 v[96:111], a[64:67], v[196:199], v[96:111]\n"
    "ds_read_b64_tr_b16 a[48:49], v18 offset:12288\n"
    "ds_read_b64_tr_b16 a[50:51], v18 offset:13312\n"
    "ds_read_b64_tr_b16 a[52:53], v19 offset:12288\n"
    "ds_read_b64_tr_b16 a[54:55], v19 offset:13312\n"
    "s_waitcnt vmcnt(8)\n"
    "v_cmp_eq_u32 vcc, -1, v25\n"
    "s_nop 0\n"
    "s_cbranch_vccnz Lagain_29\n"
    "Lback_30:\n"
    "v_max_u32 v26, v200, v204\n"
    "v_cmp_eq_u32 vcc, -1, v26\n"
    "v_mfma_f32_32x32x16_bf16 v[112:127], a[68:71], v[196:199], v[112:127]\n"
    "s_cbranch_vccnz Lagain_31\n"
    "Lback_32:\n"
    "v_mfma_f32_32x32x16_bf16 v[64:79], a[72:75], v[200:203], v[64:79]\n"
    "s_xor_b32 s78, s2, 0x10000\n"
    "buffer_load_dwordx4 v[128:131], v20, s[12:15], s78 offen sc1\n"
    "v_mfma_f32_32x32x16_bf16 v[80:95], a[76:79], v[200:203], v[80:95]\n"
    "buffer_load_dwordx4 v[132:135], v21, s[12:15], s78 offen sc1\n"
    "v_mfma_f32_32x32x16_bf16 v[96:111], a[72:75], v[204:207], v[96:111]\n"
    "ds_read_b64_tr_b16 a[56:57], v18 offset:14336\n"
    "ds_read_b64_tr_b16 a[58:59], v18 offset:15360\n"
    "ds_read_b64_tr_b16 a[60:61], v19 offset:14336\n"
    "ds_read_b64_tr_b16 a[62:63], v19 offset:15360\n"
    "v_max_u32 v26, v208, v212\n"
    "v_cmp_eq_u32 vcc, -1, v26\n"
    "v_mfma_f32_32x32x16_bf16 v[112:127], a[76:79], v[204:207], v[112:127]\n"
    "s_cbranch_vccnz Lagain_33\n"
    "Lback_34:\n"
    "v_mfma_f32_32x32x16_bf16 v[64:79], a[80:83], v[208:211], v[64:79]\n"
    "buffer_load_dwordx4 v[136:139], v20, s[12:15], s78 offen offset:1024 sc1\n"
    "v_mfma_f32_32x32x16_bf16 v[80:95], a[84:87], v[208:211], v[80:95]\n"
    "buffer_load_dwordx4 v[140:143], v21, s[12:15], s78 offen offset:1024 sc1\n"
    "v_mfma_f32_32x32x16_bf16 v[96:111], a[80:83], v[212:215], v[96:111]\n"
    "ds_read_b64_tr_b16 a[64:65], v18 offset:16384\n"
    "ds_read_b64_tr_b16 a[66:67], v18 offset:17408\n"
    "ds_read_b64_tr_b16 a[68:69], v19 offset:16384\n"
    "ds_read_b64_tr_b16 a[70:71], v19 offset:17408\n"
    "v_max_u32 v26, v216, v220\n"
    "v_cmp_eq_u32 vcc, -1, v26\n"
    "v_mfma_f32_32x32x16_bf16 v[112:127], a[84:87], v[212:215], v[112:127]\n"
    "s_cbranch_vccnz Lagain_35\n"
    "Lback_36:\n"
    "v_mfma_f32_32x32x16_bf16 v[64:79], a[88:91], v[216:219], v[64:79]\n"
    "s_xor_b32 s78, s2, 0x10800\n"
    "buffer_load_dwordx4 v[144:147], v20, s[12:15], s78 offen sc1\n"
    "v_mfma_f32_32x32x16_bf16 v[80:95], a[92:95], v[216:219], v[80:95]\n"
    "buffer_load_dwordx4 v[148:151], v21, s[12:15], s78 offen sc1\n"
    "v_mfma_f32_32x32x16_bf16 v[96:111], a[88:91], v[220:223], v[96:111]\n"
    "ds_read_b64_tr_b16 a[72:73], v18 offset:18432\n"
    "ds_read_b64_tr_b16 a[74:75], v18 offset:19456\n"
    "ds_read_b64_tr_b16 a[76:77], v19 offset:18432\n"
    "ds_read_b64_tr_b16 a[78:79], v19 offset:19456\n"
    "s_waitcnt vmcnt(12)\n"
    "v_max_u32 v26, v224, v228\n"
    "v_cmp_eq_u32 vcc, -1, v26\n"
    "v_mfma_f32_32x32x16_bf16 v[112:127], a[92:95], v[220:223], v[112:127]\n"
    "s_cbranch_vccnz Lagain_37\n"
    "Lback_38:\n"
    "v_mfma_f32_32x32x16_bf16 v[64:79], a[96:99], v[224:227], v[64:79]\n"
    "buffer_load_dwordx4 v[152:155], v20, s[12:15], s78 offen offset:1024 sc1\n"
    "v_mfma_f32_32x32x16_bf16 v[80:95], a[100:103], v[224:227], v[80:95]\n"
    "buffer_load_dwordx4 v[156:159], v21, s[12:15], s78 offen offset:1024 sc1\n"
    "v_mfma_f32_32x32x16_bf16 v[96:111], a[96:99], v[228:231], v[96:111]\n"
    "ds_read_b64_tr_b16 a[80:81], v18 offset:20480\n"
    "ds_read_b64_tr_b16 a[82:83], v18 offset:21504\n"
    "ds_read_b64_tr_b16 a[84:85], v19 offset:20480\n"
    "ds_read_b64_tr_b16 a[86:87], v19 offset:21504\n"
    "s_waitcnt vmcnt(12)\n"
    "v_max_u32 v26, v232, v236\n"
    "v_cmp_eq_u32 vcc, -1, v26\n"
    "v_mfma_f32_32x32x16_bf16 v[112:127], a[100:103], v[228:231], v[112:127]\n"
    "s_cbranch_vccnz Lagain_39\n"
    "Lback_40:\n"
    "v_mfma_f32_32x32x16_bf16 v[64:79], a[104:107], v[232:235], v[64:79]\n"
    "s_xor_b32 s78, s2, 0x11000\n"
    "buffer_load_dwordx4 v[160:163], v20, s[12:15], s78 offen sc1\n"
    "v_mfma_f32_32x32x16_bf16 v[80:95], a[108:111], v[232:235], v[80:95]\n"
    "buffer_load_dwordx4 v[164:167], v21, s[12:15], s78 offen sc1\n"
    "v_mfma_f32_32x32x16_bf16 v[96:111], a[104:107], v[236:239], v[96:111]\n"
    "ds_read_b64_tr_b16 a[88:89], v18 offset:22528\n"
    "ds_read_b64_tr_b16 a[90:91], v18 offset:23552\n"
    "ds_read_b64_tr_b16 a[92:93], v19 offset:22528\n"
    "ds_read_b64_tr_b16 a[94:95], v19 offset:23552\n"
    "s_waitcnt vmcnt(12)\n"
    "v_max_u32 v26, v240, v244\n"
    "v_cmp_eq_u32 vcc, -1, v26\n"
    "v_mfma_f32_32x32x16_bf16 v[112:127], a[108:111], v[236:239], v[112:127]\n"
    "s_cbranch_vccnz Lagain_41\n"
    "Lback_42:\n"
    "v_mfma_f32_32x32x16_bf16 v[64:79], a[112:115], v[240:243], v[64:79]\n"
    "buffer_load_dwordx4 v[168:171], v20, s[12:15], s78 offen offset:1024 sc1\n"
    "v_mfma_f32_32x32x16_bf16 v[80:95], a[116:119], v[240:243], v[80:95]\n"
    "buffer_load_dwordx4 v[172:175], v21, s[12:15], s78 offen offset:1024 sc1\n"
    "v_mfma_f32_32x32x16_bf16 v[96:111], a[112:115], v[244:247], v[96:111]\n"
    "ds_read_b64_tr_b16 a[96:97], v18 offset:24576\n"
    "ds_read_b64_tr_b16 a[98:99], v18 offset:25600\n"
    "ds_read_b64_tr_b16 a[100:101], v19 offset:24576\n"
    "ds_read_b64_tr_b16 a[102:103], v19 offset:25600\n"
    "s_waitcnt vmcnt(12)\n"
    "v_max_u32 v26, v248, v252\n"
    "v_cmp_eq_u32 vcc, -1, v26\n"
    "v_mfma_f32_32x32x16_bf16 v[112:127], a[116:119], v[244:247], v[112:127]\n"
    "s_cbranch_vccnz Lagain_43\n"
    "Lback_44:\n"
    "v_mfma_f32_32x32x16_bf16 v[64:79], a[120:123], v[248:251], v[64:79]\n"
    "s_xor_b32 s78, s2, 0x11800\n"
    "buffer_load_dwordx4 v[176:179], v20, s[12:15], s78 offen sc1\n"
    "v_mfma_f32_32x32x16_bf16 v[80:95], a[124:127], v[248:251], v[80:95]\n"
    "buffer_load_dwordx4 v[180:183], v21, s[12:15], s78 offen sc1\n"
    "v_mfma_f32_32x32x16_bf16 v[96:111], a[120:123], v[252:255], v[96:111]\n"
    "ds_read_b64_tr_b16 a[104:105], v18 offset:26624\n"
    "ds_read_b64_tr_b16 a[106:107], v18 offset:27648\n"
    "ds_read_b64_tr_b16 a[108:109], v19 offset:26624\n"
    "ds_read_b64_tr_b16 a[110:111], v19 offset:27648\n"
    "s_waitcnt vmcnt(12)\n"
    "v_max_u32 v26, v128, v132\n"
    "v_cmp_eq_u32 vcc, -1, v26\n"
    "v_mfma_f32_32x32x16_bf16 v[112:127], a[124:127], v[252:255], v[112:127]\n"
    "s_cbranch_vccnz Lagain_45\n"
    "Lback_46:\n"
    "v_mfma_f32_32x32x16_bf16 v[64:79], a[128:131], v[128:131], v[64:79]\n"
    "buffer_load_dwordx4 v[184:187], v20, s[12:15], s78 offen offset:1024 sc1\n"
    "v_mfma_f32_32x32x16_bf16 v[80:95], a[132:135], v[128:131], v[80:95]\n"
    "buffer_load_dwordx4 v[188:191], v21, s[12:15], s78 offen offset:1024 sc1\n"
    "v_mfma_f32_32x32x16_bf16 v[96:111], a[128:131], v[132:135], v[96:111]\n"
    "s_waitcnt vmcnt(12)\n"
    "v_max_u32 v26, v136, v140\n"
    "v_cmp_eq_u32 vcc, -1, v26\n"
    "v_mfma_f32_32x32x16_bf16 v[112:127], a[132:135], v[132:135], v[112:127]\n"
    "s_cbranch_vccnz Lagain_47\n"
    "Lback_48:\n"
    "v_mfma_f32_32x32x16_bf16 v[64:79], a[136:139], v[136:139], v[64:79]\n"
    "s_xor_b32 s78, s2, 0x12000\n"
    "buffer_load_dwordx4 v[192:195], v20, s[12:15], s78 offen sc1\n"
    "v_mfma_f32_32x32x16_bf16 v[80:95], a[140:143], v[136:139], v[80:95]\n"
    "buffer_load_dwordx4 v[196:199], v21, s[12:15], s78 offen sc1\n"
    "v_mfma_f32_32x32x16_bf16 v[96:111], a[136:139], v[140:143], v[96:111]\n"
    "s_waitcnt vmcnt(12)\n"
    "v_max_u32 v26, v144, v148\n"
    "v_cmp_eq_u32 vcc, -1, v26\n"
    "v_mfma_f32_32x32x16_bf16 v[112:127], a[140:143], v[140:143], v[112:127]\n"
    "s_cbranch_vccnz Lagain_49\n"
    "Lback_50:\n"
    "v_mfma_f32_32x32x16_bf16 v[64:79], a[144:147], v[144:147], v[64:79]\n"
    "buffer_load_dwordx4 v[200:203], v20, s[12:15], s78 offen offset:1024 sc1\n"
    "v_mfma_f32_32x32x16_bf16 v[80:95], a[148:151], v[144:147], v[80:95]\n"
    "buffer_load_dwordx4 v[204:207], v21, s[12:15], s78 offen offset:1024 sc1\n"
    "v_mfma_f32_32x32x16_bf16 v[96:111], a[144:147], v[148:151], v[96:111]\n"
    "s_waitcnt vmcnt(12)\n"
    "v_max_u32 v26, v152, v156\n"
    "v_cmp_eq_u32 vcc, -1, v26\n"
    "v_mfma_f32_32x32x16_bf16 v[112:127], a[148:151], v[148:151], v[112:127]\n"
    "s_cbranch_vccnz Lagain_51\n"
    "Lback_52:\n"
    "v_mfma_f32_32x32x16_bf16 v[64:79], a[152:155], v[152:155], v[64:79]\n"
    "s_xor_b32 s78, s2, 0x12800\n"
    "buffer_load_dwordx4 v[212:215], v21, s[12:15], s78 offen sc1\n"
    "v_mfma_f32_32x32x16_bf16 v[80:95], a[156:159], v[152:155], v[80:95]\n"
    "buffer_load_dwordx4 v[220:223], v21, s[12:15], s78 offen offset:1024 sc1\n"
    "v_mfma_f32_32x32x16_bf16 v[96:111], a[152:155], v[156:159], v[96:111]\n"
    "s_waitcnt vmcnt(12)\n"
    "v_max_u32 v26, v160, v164\n"
    "v_cmp_eq_u32 vcc, -1, v26\n"
    "v_mfma_f32_32x32x16_bf16 v[112:127], a[156:159], v[156:159], v[112:127]\n"
    "s_cbranch_vccnz Lagain_53\n"
    "Lback_54:\n"
    "v_mfma_f32_32x32x16_bf16 v[64:79], a[160:163], v[160:163], v[64:79]\n"
    "s_xor_b32 s78, s2, 0x13000\n"
    "buffer_load_dwordx4 v[228:231], v21, s[12:15], s78 offen sc1\n"
    "v_mfma_f32_32x32x16_bf16 v[80:95], a[164:167], v[160:163], v[80:95]\n"
    "buffer_load_dwordx4 v[236:239], v21, s[12:15], s78 offen offset:1024 sc1\n"
    "v_mfma_f32_32x32x16_bf16 v[96:111], a[160:163], v[164:167], v[96:111]\n"
    "s_waitcnt vmcnt(12)\n"
    "v_max_u32 v26, v168, v172\n"
    "v_cmp_eq_u32 vcc, -1, v26\n"
    "v_mfma_f32_32x32x16_bf16 v[112:127], a[164:167], v[164:167], v[112:127]\n"
    "s_cbranch_vccnz Lagain_55\n"
    "Lback_56:\n"
    "v_mfma_f32_32x32x16_bf16 v[64:79], a[168:171], v[168:171], v[64:79]\n"
    "s_xor_b32 s78, s2, 0x13800\n"
    "buffer_load_dwordx4 v[244:247], v21, s[12:15], s78 offen sc1\n"
    "v_mfma_f32_32x32x16_bf16 v[80:95], a[172:175], v[168:171], v[80:95]\n"
    "buffer_load_dwordx4 v[252:255], v21, s[12:15], s78 offen offset:1024 sc1\n"
    "v_mfma_f32_32x32x16_bf16 v[96:111], a[168:171], v[172:175], v[96:111]\n"
    "s_waitcnt vmcnt(12)\n"
    "v_max_u32 v26, v176, v180\n"
    "v_cmp_eq_u32 vcc, -1, v26\n"
    "v_mfma_f32_32x32x16_bf16 v[112:127], a[172:175], v[172:175], v[112:127]\n"
    "s_cbranch_vccnz Lagain_57\n"
    "Lback_58:\n"
    "v_mfma_f32_32x32x16_bf16 v[64:79], a[176:179], v[176:179], v[64:79]\n"
    "s_xor_b32 s78, s2, 0x12800\n"
    "buffer_load_dwordx4 v[208:211], v20, s[12:15], s78 offen sc1\n"
    "v_mfma_f32_32x32x16_bf16 v[80:95], a[180:183], v[176:179], v[80:95]\n"
    "buffer_load_dwordx4 v[216:219], v20, s[12:15], s78 offen offset:1024 sc1\n"
    "v_mfma_f32_32x32x16_bf16 v[96:111], a[176:179], v[180:183], v[96:111]\n"
    "s_waitcnt vmcnt(12)\n"
    "v_max_u32 v26, v184, v188\n"
    "v_cmp_eq_u32 vcc, -1, v26\n"
    "v_mfma_f32_32x32x16_bf16 v[112:127], a[180:183], v[180:183], v[112:127]\n"
    "s_cbranch_vccnz Lagain_59\n"
    "Lback_60:\n"
    "v_mfma_f32_32x32x16_bf16 v[64:79], a[184:187], v[184:187], v[64:79]\n"
    "s_xor_b32 s78, s2, 0x13000\n"
    "buffer_load_dwordx4 v[224:227], v20, s[12:15], s78 offen sc1\n"
    "v_mfma_f32_32x32x16_bf16 v[80:95], a[188:191], v[184:187], v[80:95]\n"
    "buffer_load_dwordx4 v[232:235], v20, s[12:15], s78 offen offset:1024 sc1\n"
    "v_mfma_f32_32x32x16_bf16 v[96:111], a[184:187], v[188:191], v[96:111]\n"
    "s_waitcnt vmcnt(12)\n"
    "v_max_u32 v26, v192, v196\n"
    "v_cmp_eq_u32 vcc, -1, v26\n"
    "v_mfma_f32_32x32x16_bf16 v[112:127], a[188:191], v[188:191], v[112:127]\n"
    "s_cbranch_vccnz Lagain_61\n"
    "Lback_62:\n"
    "v_mfma_f32_32x32x16_bf16 v[64:79], a[192:195], v[192:195], v[64:79]\n"
    "s_xor_b32 s78, s2, 0x13800\n"
    "buffer_load_dwordx4 v[240:243], v20, s[12:15], s78 offen sc1\n"
    "v_mfma_f32_32x32x16_bf16 v[80:95], a[196:199], v[192:195], v[80:95]\n"
    "buffer_load_dwordx4 v[248:251], v20, s[12:15], s78 offen offset:1024 sc1\n"
    "v_mfma_f32_32x32x16_bf16 v[96:111], a[192:195], v[196:199], v[96:111]\n"
    "s_waitcnt vmcnt(12)\n"
    "v_max_u32 v26, v200, v204\n"
    "v_cmp_eq_u32 vcc, -1, v26\n"
    "v_mfma_f32_32x32x16_bf16 v[112:127], a[196:199], v[196:199], v[112:127]\n"
    "s_cbranch_vccnz Lagain_63\n"
    "Lback_64:\n"
    "v_mfma_f32_32x32x16_bf16 v[64:79], a[200:203], v[200:203], v[64:79]\n"
    "s_waitcnt lgkmcnt(0)\n"
    "s_add_u32 m0, s68, 0\n"
    "s_mov_b32 s71, 0xe0000\n"
    "buffer_load_dwordx4 v10, s[24:27], s71 offen lds\n"
    "v_mfma_f32_32x32x16_bf16 v[80:95], a[204:207], v[200:203], v[80:95]\n"
    "buffer_load_dwordx4 v11, s[24:27], s71 offen offset:1024 lds\n"
    "v_mfma_f32_32x32x16_bf16 v[96:111], a[200:203], v[204:207], v[96:111]\n"
    "s_waitcnt vmcnt(13)\n"
    "v_cmp_eq_u32 vcc, -1, v212\n"
    "v_mfma_f32_32x32x16_bf16 v[112:127], a[204:207], v[204:207], v[112:127]\n"
    "s_cbranch_vccnz Lagain_65\n"
    "Lback_66:\n"
    "v_mfma_f32_32x32x16_bf16 v[112:127], a[212:215], v[212:215], v[112:127]\n"
    "buffer_load_dwordx4 v12, s[24:27], s71 offen offset:2048 lds\n"
    "s_waitcnt vmcnt(13)\n"
    "v_cmp_eq_u32 vcc, -1, v220\n"
    "v_mfma_f32_32x32x16_bf16 v[96:111], a[208:211], v[212:215], v[96:111]\n"
    "s_cbranch_vccnz Lagain_67\n"
    "Lback_68:\n"
    "buffer_load_dwordx4 v13, s[24:27], s71 offen offset:3072 lds\n"
    "v_mfma_f32_32x32x16_bf16 v[112:127], a[220:223], v[220:223], v[112:127]\n"
    "s_add_u32 m0, s68, 0x1000\n"
    "s_mov_b32 s71, 0x100000\n"
    "buffer_load_dwordx4 v10, s[24:27], s71 offen lds\n"
    "s_waitcnt vmcnt(14)\n"
    "v_cmp_eq_u32 vcc, -1, v228\n"
    "v_mfma_f32_32x32x16_bf16 v[96:111], a[216:219], v[220:223], v[96:111]\n"
    "s_cbranch_vccnz Lagain_69\n"
    "Lback_70:\n"
    "buffer_load_dwordx4 v11, s[24:27], s71 offen offset:1024 lds\n"
    "v_mfma_f32_32x32x16_bf16 v[112:127], a[228:231], v[228:231], v[112:127]\n"
    "buffer_load_dwordx4 v12, s[24:27], s71 offen offset:2048 lds\n"
    "s_waitcnt vmcnt(15)\n"
    "v_cmp_eq_u32 vcc, -1, v236\n"
    "v_mfma_f32_32x32x16_bf16 v[96:111], a[224:227], v[228:231], v[96:111]\n"
    "s_cbranch_vccnz Lagain_71\n"
    "Lback_72:\n"
    "buffer_load_dwordx4 v13, s[24:27], s71 offen offset:3072 lds\n"
    "v_mfma_f32_32x32x16_bf16 v[112:127], a[236:239], v[236:239], v[112:127]\n"
    "s_add_u32 m0, s68, 0x2000\n"
    "s_mov_b32 s71, 0x120000\n"
    "buffer_load_dwordx4 v10, s[24:27], s71 offen lds\n"
    "s_waitcnt vmcnt(16)\n"
    "v_cmp_eq_u32 vcc, -1, v244\n"
    "v_mfma_f32_32x32x16_bf16 v[96:111], a[232:235], v[236:239], v[96:111]\n"
    "s_cbranch_vccnz Lagain_73\n"
    "Lback_74:\n"
    "buffer_load_dwordx4 v11, s[24:27], s71 offen offset:1024 lds\n"
    "v_mfma_f32_32x32x16_bf16 v[112:127], a[244:247], v[244:247], v[112:127]\n"
    "buffer_load_dwordx4 v12, s[24:27], s71 offen offset:2048 lds\n"
    "s_waitcnt vmcnt(17)\n"
    "v_cmp_eq_u32 vcc, -1, v252\n"
    "v_mfma_f32_32x32x16_bf16 v[96:111], a[240:243], v[244:247], v[96:111]\n"
    "s_cbranch_vccnz Lagain_75\n"
    "Lback_76:\n"
    "buffer_load_dwordx4 v13, s[24:27], s71 offen offset:3072 lds\n"
    "v_mfma_f32_32x32x16_bf16 v[112:127], a[252:255], v[252:255], v[112:127]\n"
    "s_waitcnt vmcnt(17)\n"
    "v_cmp_eq_u32 vcc, -1, v208\n"
    "v_mfma_f32_32x32x16_bf16 v[96:111], a[248:251], v[252:255], v[96:111]\n"
    "s_cbranch_vccnz Lagain_77\n"
    "Lback_78:\n"
    "s_nop 2\n"
    "v_mfma_f32_32x32x16_bf16 v[80:95], a[212:215], v[208:211], v[80:95]\n"
    "s_nop 3\n"
    "ds_write_b128 v8, v[112:115]\n"
    "s_waitcnt vmcnt(16)\n"
    "v_cmp_eq_u32 vcc, -1, v216\n"
    "v_mfma_f32_32x32x16_bf16 v[64:79], a[208:211], v[208:211], v[64:79]\n"
    "s_cbranch_vccnz Lagain_79\n"
    "Lback_80:\n"
    "ds_write_b128 v8, v[116:119] offset:1024\n"
    "v_mfma_f32_32x32x16_bf16 v[80:95], a[220:223], v[216:219], v[80:95]\n"
    "ds_write_b128 v8, v[120:123] offset:2048\n"
    "s_waitcnt vmcnt(15)\n"
    "v_cmp_eq_u32 vcc, -1, v224\n"
    "v_mfma_f32_32x32x16_bf16 v[64:79], a[216:219], v[216:219], v[64:79]\n"
    "s_cbranch_vccnz Lagain_81\n"
    "Lback_82:\n"
    "ds_write_b128 v8, v[124:127] offset:3072\n"
    "v_mfma_f32_32x32x16_bf16 v[80:95], a[228:231], v[224:227], v[80:95]\n"
    "s_waitcnt vmcnt(14)\n"
    "v_cmp_eq_u32 vcc, -1, v232\n"
    "v_mfma_f32_32x32x16_bf16 v[64:79], a[224:227], v[224:227], v[64:79]\n"
    "s_cbranch_vccnz Lagain_83\n"
    "Lback_84:\n"
    "ds_write_b128 v7, v[96:99]\n"
    "v_mfma_f32_32x32x16_bf16 v[80:95], a[236:239], v[232:235], v[80:95]\n"
    "ds_write_b128 v7, v[100:103] offset:1024\n"
    "s_waitcnt vmcnt(13)\n"
    "v_cmp_eq_u32 vcc, -1, v240\n"
    "v_mfma_f32_32x32x16_bf16 v[64:79], a[232:235], v[232:235], v[64:79]\n"
    "s_cbranch_vccnz Lagain_85\n"
    "Lback_86:\n"
    "ds_write_b128 v7, v[104:107] offset:2048\n"
    "s_waitcnt vmcnt(12)\n"
    "v_cmp_eq_u32 vcc, -1, v248\n"
    "v_mfma_f32_32x32x16_bf16 v[80:95], a[244:247], v[240:243], v[80:95]\n"
    "s_cbranch_vccnz Lagain_87\n"
    "Lback_88:\n"
    "ds_write_b128 v7, v[108:111] offset:3072\n"
    "v_mfma_f32_32x32x16_bf16 v[80:95], a[252:255], v[248:251], v[80:95]\n"
    "s_nop 4\n"
    "v_mfma_f32_32x32x16_bf16 v[64:79], a[240:243], v[240:243], v[64:79]\n"
    "s_nop 4\n"
    "v_mfma_f32_32x32x16_bf16 v[64:79], a[248:251], v[248:251], v[64:79]\n"
    "ds_write_b128 v6, v[80:83]\n"
    "ds_write_b128 v6, v[84:87] offset:1024\n"
    "ds_write_b128 v6, v[88:91] offset:2048\n"
    "ds_write_b128 v6, v[92:95] offset:3072\n"
    "s_waitcnt lgkmcnt(0)\n"
    "s_barrier\n"
    "ds_read_b128 v[128:131], v9\n"
    "ds_read_b128 v[144:147], v9 offset:4096\n"
    "ds_read_b128 v[160:163], v9 offset:8192\n"
    "ds_read_b128 v[132:135], v9 offset:1024\n"
    "ds_read_b128 v[148:151], v9 offset:5120\n"
    "ds_read_b128 v[164:167], v9 offset:9216\n"
    "ds_read_b128 v[136:139], v9 offset:2048\n"
    "ds_read_b128 v[152:155], v9 offset:6144\n"
    "ds_read_b128 v[168:171], v9 offset:10240\n"
    "ds_read_b128 v[140:143], v9 offset:3072\n"
    "ds_read_b128 v[156:159], v9 offset:7168\n"
    "ds_read_b128 v[172:175], v9 offset:11264\n"
    "s_add_u32 m0, s68, 0x3000\n"
    "s_mov_b32 s71, 0x140000\n"
    "buffer_load_dwordx4 v10, s[24:27], s71 offen lds\n"
    "buffer_load_dwordx4 v11, s[24:27], s71 offen offset:1024 lds\n"
    "buffer_load_dwordx4 v12, s[24:27], s71 offen offset:2048 lds\n"
    "buffer_load_dwordx4 v13, s[24:27], s71 offen offset:3072 lds\n"
    "s_add_u32 m0, s68, 0x4000\n"
    "s_mov_b32 s71, 0x160000\n"
    "buffer_load_dwordx4 v10, s[24:27], s71 offen lds\n"
    "buffer_load_dwordx4 v11, s[24:27], s71 offen offset:1024 lds\n"
    "buffer_load_dwordx4 v12, s[24:27], s71 offen offset:2048 lds\n"
    "buffer_load_dwordx4 v13, s[24:27], s71 offen offset:3072 lds\n"
    "s_waitcnt lgkmcnt(10)\n"
    "v_pk_add_f32 v[128:129], v[128:129], v[144:145]\n"
    "v_pk_add_f32 v[130:131], v[130:131], v[146:147]\n"
    "s_waitcnt lgkmcnt(9)\n"
    "v_pk_add_f32 v[64:65], v[64:65], v[160:161]\n"
    "v_pk_add_f32 v[66:67], v[66:67], v[162:163]\n"
    "v_pk_add_f32 v[64:65], v[64:65], v[128:129]\n"
    "v_pk_add_f32 v[66:67], v[66:67], v[130:131]\n"
    "s_waitcnt lgkmcnt(7)\n"
    "v_pk_add_f32 v[132:133], v[132:133], v[148:149]\n"
    "v_pk_add_f32 v[134:135], v[134:135], v[150:151]\n"
    "s_waitcnt lgkmcnt(6)\n"
    "v_pk_add_f32 v[68:69], v[68:69], v[164:165]\n"
    "v_pk_add_f32 v[70:71], v[70:71], v[166:167]\n"
    "v_pk_add_f32 v[68:69], v[68:69], v[132:133]\n"
    "v_pk_add_f32 v[70:71], v[70:71], v[134:135]\n"
    "v_pk_mul_f32 v[176:177], v[64:65], v[64:65]\n"
    "v_pk_mul_f32 v[178:179], v[66:67], v[66:67]\n"
    "v_pk_mul_f32 v[180:181], v[68:69], v[68:69]\n"
    "v_pk_mul_f32 v[182:183], v[70:71], v[70:71]\n"
    "v_pk_fma_f32 v[176:177], v[176:177], v[16:17], v[16:17] op_sel:[0,1,0] op_sel_hi:[1,1,0]\n"
    "v_pk_fma_f32 v[178:179], v[178:179], v[16:17], v[16:17] op_sel:[0,1,0] op_sel_hi:[1,1,0]\n"
    "v_pk_fma_f32 v[180:181], v[180:181], v[16:17], v[16:17] op_sel:[0,1,0] op_sel_hi:[1,1,0]\n"
    "v_pk_fma_f32 v[182:183], v[182:183], v[16:17], v[16:17] op_sel:[0,1,0] op_sel_hi:[1,1,0]\n"
    "v_pk_mul_f32 v[176:177], v[176:177], v[64:65]\n"
    "v_pk_mul_f32 v[178:179], v[178:179], v[66:67]\n"
    "v_pk_mul_f32 v[180:181], v[180:181], v[68:69]\n"
    "v_pk_mul_f32 v[182:183], v[182:183], v[70:71]\n"
    "v_exp_f32 v176, v176\n"
    "v_exp_f32 v177, v177\n"
    "v_exp_f32 v178, v178\n"
    "v_exp_f32 v179, v179\n"
    "v_exp_f32 v180, v180\n"
    "v_exp_f32 v181, v181\n"
    "v_exp_f32 v182, v182\n"
    "v_exp_f32 v183, v183\n"
    "v_pk_add_f32 v[176:177], v[176:177], 1.0 op_sel_hi:[1,0]\n"
    "v_pk_add_f32 v[178:179], v[178:179], 1.0 op_sel_hi:[1,0]\n"
    "v_pk_add_f32 v[180:181], v[180:181], 1.0 op_sel_hi:[1,0]\n"
    "v_pk_add_f32 v[182:183], v[182:183], 1.0 op_sel_hi:[1,0]\n"
    "v_rcp_f32 v176, v176\n"
    "v_rcp_f32 v177, v177\n"
    "v_rcp_f32 v178, v178\n"
    "v_rcp_f32 v179, v179\n"
    "v_rcp_f32 v180, v180\n"
    "v_rcp_f32 v181, v181\n"
    "v_rcp_f32 v182, v182\n"
    "v_rcp_f32 v183, v183\n"
    "v_pk_mul_f32 v[64:65], v[64:65], v[176:177]\n"
    "v_pk_mul_f32 v[66:67], v[66:67], v[178:179]\n"
    "v_pk_mul_f32 v[68:69], v[68:69], v[180:181]\n"
    "v_pk_mul_f32 v[70:71], v[70:71], v[182:183]\n"
    "v_cvt_pk_bf16_f32 v192, v64, v65\n"
    "v_cvt_pk_bf16_f32 v193, v66, v67\n"
    "v_cvt_pk_bf16_f32 v194, v68, v69\n"
    "v_cvt_pk_bf16_f32 v195, v70, v71\n"
    "buffer_store_dwordx4 v[192:195], v1, s[12:15], s72 offen sc1\n"
    "s_waitcnt lgkmcnt(4)\n"
    "v_pk_add_f32 v[136:137], v[136:137], v[152:153]\n"
    "v_pk_add_f32 v[138:139], v[138:139], v[154:155]\n"
    "s_waitcnt lgkmcnt(3)\n"
    "v_pk_add_f32 v[72:73], v[72:73], v[168:169]\n"
    "v_pk_add_f32 v[74:75], v[74:75], v[170:171]\n"
    "v_pk_add_f32 v[72:73], v[72:73], v[136:137]\n"
    "v_pk_add_f32 v[74:75], v[74:75], v[138:139]\n"
    "s_waitcnt lgkmcnt(1)\n"
    "v_pk_add_f32 v[140:141], v[140:141], v[156:157]\n"
    "v_pk_add_f32 v[142:143], v[142:143], v[158:159]\n"
    "s_waitcnt lgkmcnt(0)\n"
    "v_pk_add_f32 v[76:77], v[76:77], v[172:173]\n"
    "v_pk_add_f32 v[78:79], v[78:79], v[174:175]\n"
    "v_pk_add_f32 v[76:77], v[76:77], v[140:141]\n"
    "v_pk_add_f32 v[78:79], v[78:79], v[142:143]\n"
    "v_pk_mul_f32 v[184:185], v[72:73], v[72:73]\n"
    "v_pk_mul_f32 v[186:187], v[74:75], v[74:75]\n"
    "v_pk_mul_f32 v[188:189], v[76:77], v[76:77]\n"
    "v_pk_mul_f32 v[190:191], v[78:79], v[78:79]\n"
    "v_pk_fma_f32 v[184:185], v[184:185], v[16:17], v[16:17] op_sel:[0,1,0] op_sel_hi:[1,1,0]\n"
    "v_pk_fma_f32 v[186:187], v[186:187], v[16:17], v[16:17] op_sel:[0,1,0] op_sel_hi:[1,1,0]\n"
    "v_pk_fma_f32 v[188:189], v[188:189], v[16:17], v[16:17] op_sel:[0,1,0] op_sel_hi:[1,1,0]\n"
    "v_pk_fma_f32 v[190:191], v[190:191], v[16:17], v[16:17] op_sel:[0,1,0] op_sel_hi:[1,1,0]\n"
    "v_pk_mul_f32 v[184:185], v[184:185], v[72:73]\n"
    "v_pk_mul_f32 v[186:187], v[186:187], v[74:75]\n"
    "v_pk_mul_f32 v[188:189], v[188:189], v[76:77]\n"
    "v_pk_mul_f32 v[190:191], v[190:191], v[78:79]\n"
    "v_exp_f32 v184, v184\n"
    "v_exp_f32 v185, v185\n"
    "v_exp_f32 v186, v186\n"
    "v_exp_f32 v187, v187\n"
    "v_exp_f32 v188, v188\n"
    "v_exp_f32 v189, v189\n"
    "v_exp_f32 v190, v190\n"
    "v_exp_f32 v191, v191\n"
    "v_pk_add_f32 v[184:185], v[184:185], 1.0 op_sel_hi:[1,0]\n"
    "v_pk_add_f32 v[186:187], v[186:187], 1.0 op_sel_hi:[1,0]\n"
    "v_pk_add_f32 v[188:189], v[188:189], 1.0 op_sel_hi:[1,0]\n"
    "v_pk_add_f32 v[190:191], v[190:191], 1.0 op_sel_hi:[1,0]\n"
    "v_rcp_f32 v184, v184\n"
    "v_rcp_f32 v185, v185\n"
    "v_rcp_f32 v186, v186\n"
    "v_rcp_f32 v187, v187\n"
    "v_rcp_f32 v188, v188\n"
    "v_rcp_f32 v189, v189\n"
    "v_rcp_f32 v190, v190\n"
    "v_rcp_f32 v191, v191\n"
    "v_pk_mul_f32 v[72:73], v[72:73], v[184:185]\n"
    "v_pk_mul_f32 v[74:75], v[74:75], v[186:187]\n"
    "v_pk_mul_f32 v[76:77], v[76:77], v[188:189]\n"
    "v_pk_mul_f32 v[78:79], v[78:79], v[190:191]\n"
    "v_cvt_pk_bf16_f32 v196, v72, v73\n"
    "v_cvt_pk_bf16_f32 v197, v74, v75\n"
    "v_cvt_pk_bf16_f32 v198, v76, v77\n"
    "v_cvt_pk_bf16_f32 v199, v78, v79\n"
    "buffer_store_dwordx4 v[196:199], v1, s[12:15], s72 offen offset:1024 sc1\n"
    "v_min_u32 v24, 6, v0\n"
    "v_lshl_add_u32 v25, v24, 1, 57\n"
    "s_mul_i32 s62, s53, 0x70\n"
    "s_mul_i32 s60, s44, 14\n"
    "s_add_u32 s62, s62, s60\n"
    "s_lshl_b32 s62, s62, 11\n"
    "s_add_u32 s62, s62, 0xb007f0\n"
    "v_lshl_add_u32 v14, v25, 11, s62\n"
    "s_lshl2_add_u32 s62, s46, s44\n"
    "s_mul_i32 s62, s62, 0x7000\n"
    "s_add_u32 s62, s62, 0xb00000\n"
    "v_add_u32 v20, s62, v1\n"
    "s_lshl2_add_u32 s62, s47, s44\n"
    "s_mul_i32 s62, s62, 0x7000\n"
    "s_add_u32 s62, s62, 0xb00000\n"
    "v_add_u32 v21, s62, v1\n"
    "s_lshl1_add_u32 s60, s55, s65\n"
    "s_lshl_b32 s63, s60, 6\n"
    "s_mul_i32 s66, s60, 0x7000\n"
    "s_lshl_b32 s62, s46, 6\n"
    "s_add_u32 s66, s66, s62\n"
    "v_mul_u32_u24 v57, 0xe00, v60\n"
    "v_lshl_add_u32 v57, v61, 1, v57\n"
    "s_lshl_b32 s61, s46, 6\n"
    "s_add_u32 s61, s61, s60\n"
    "s_lshl_b32 s61, s61, 11\n"
    "s_add_u32 s61, s61, 0x900000\n"
    "s_cmp_eq_u32 s77, 1\n"
    "s_cbranch_scc0 L_u1\n"
    "buffer_load_dword v27, v14, s[12:15], 0 offen sc1\n"
    "L_u1:\n"
    "s_add_u32 m0, s68, 0x5000\n"
    "s_mov_b32 s71, 0x180000\n"
    "buffer_load_dwordx4 v10, s[24:27], s71 offen lds\n"
    "buffer_load_dwordx4 v11, s[24:27], s71 offen offset:1024 lds\n"
    "buffer_load_dwordx4 v12, s[24:27], s71 offen offset:2048 lds\n"
    "buffer_load_dwordx4 v13, s[24:27], s71 offen offset:3072 lds\n"
    "s_add_u32 m0, s68, 0x6000\n"
    "s_mov_b32 s71, 0x1a0000\n"
    "buffer_load_dwordx4 v10, s[24:27], s71 offen lds\n"
    "buffer_load_dwordx4 v11, s[24:27], s71 offen offset:1024 lds\n"
    "buffer_load_dwordx4 v12, s[24:27], s71 offen offset:2048 lds\n"
    "buffer_load_dwordx4 v13, s[24:27], s71 offen offset:3072 lds\n"
    "buffer_load_dwordx2 v[28:29], v15, s[32:35], s63 offen\n"
    "buffer_load_dwordx2 v[30:31], v15, s[32:35], s63 offen offset:16\n"
    "buffer_load_dwordx2 v[32:33], v15, s[32:35], s63 offen offset:32\n"
    "buffer_load_dwordx2 v[34:35], v15, s[32:35], s63 offen offset:48\n"
    "buffer_load_dwordx4 v[112:115], v1, s[12:15], s61 offen sc1\n"
    "buffer_load_dwordx4 v[116:119], v1, s[12:15], s61 offen offset:1024 sc1\n"
    "s_barrier\n"
    "s_cmp_lt_u32 s56, 7\n"
    "s_cbranch_scc1 L_d7\n"
    "s_sleep 32\n"
    "L_d7:\n"
    "s_cmp_eq_u32 s77, 1\n"
    "s_cbranch_scc0 L_u2\n"
    "s_waitcnt vmcnt(14)\n"
    "v_cmp_eq_u32 vcc, -1, v27\n"
    "s_nop 0\n"
    "s_cbranch_vccz L_u3\n"
    "L_u2:\n"
    "s_mov_b32 s62, 0x100000\n"
    "Lpoll_89:\n"
    "buffer_load_dword v24, v14, s[12:15], 0 offen sc1\n"
    "s_waitcnt vmcnt(0)\n"
    "v_cmp_eq_u32 vcc, -1, v24\n"
    "s_nop 0\n"
    "s_cbranch_vccz Ldone_90\n"
    "s_sub_u32 s62, s62, 1\n"
    "s_sleep 1\n"
    "s_cbranch_scc0 Lpoll_89\n"
    "Ldone_90:\n"
    "L_u3:\n"
    "s_mov_b32 s70, 0\n"
    "buffer_load_dwordx4 v[128:131], v20, s[12:15], s70 offen sc1\n"
    "buffer_load_dwordx4 v[132:135], v21, s[12:15], s70 offen sc1\n"
    "buffer_load_dwordx4 v[136:139], v20, s[12:15], s70 offen offset:1024 sc1\n"
    "buffer_load_dwordx4 v[140:143], v21, s[12:15], s70 offen offset:1024 sc1\n"
    "buffer_load_dwordx4 v[144:147], v20, s[12:15], s70 offen offset:2048 sc1\n"
    "buffer_load_dwordx4 v[148:151], v21, s[12:15], s70 offen offset:2048 sc1\n"
    "buffer_load_dwordx4 v[152:155], v20, s[12:15], s70 offen offset:3072 sc1\n"
    "buffer_load_dwordx4 v[156:159], v21, s[12:15], s70 offen offset:3072 sc1\n"
    "s_mov_b32 s70, 0x1000\n"
    "buffer_load_dwordx4 v[160:163], v20, s[12:15], s70 offen sc1\n"
    "buffer_load_dwordx4 v[164:167], v21, s[12:15], s70 offen sc1\n"
    "buffer_load_dwordx4 v[168:171], v20, s[12:15], s70 offen offset:1024 sc1\n"
    "buffer_load_dwordx4 v[172:175], v21, s[12:15], s70 offen offset:1024 sc1\n"
    "buffer_load_dwordx4 v[176:179], v20, s[12:15], s70 offen offset:2048 sc1\n"
    "buffer_load_dwordx4 v[180:183], v21, s[12:15], s70 offen offset:2048 sc1\n"
    "s_waitcnt vmcnt(14)\n"
    "v_max_u32 v26, v112, v116\n"
    "v_cmp_eq_u32 vcc, -1, v26\n"
    "s_nop 0\n"
    "s_cbranch_vccnz Lagain_92\n"
    "Lback_93:\n"
    "v_lshlrev_b32 v24, 16, v112\n"
    "v_and_b32 v25, 0xffff0000, v112\n"
    "v_lshlrev_b32 v58, 16, v28\n"
    "v_and_b32 v59, 0xffff0000, v28\n"
    "v_pk_add_f32 v[64:65], v[24:25], v[58:59]\n"
    "v_lshlrev_b32 v24, 16, v113\n"
    "v_and_b32 v25, 0xffff0000, v113\n"
    "v_lshlrev_b32 v58, 16, v29\n"
    "v_and_b32 v59, 0xffff0000, v29\n"
    "v_pk_add_f32 v[66:67], v[24:25], v[58:59]\n"
    "v_lshlrev_b32 v24, 16, v114\n"
    "v_and_b32 v25, 0xffff0000, v114\n"
    "v_lshlrev_b32 v58, 16, v30\n"
    "v_and_b32 v59, 0xffff0000, v30\n"
    "v_pk_add_f32 v[68:69], v[24:25], v[58:59]\n"
    "v_lshlrev_b32 v24, 16, v115\n"
    "v_and_b32 v25, 0xffff0000, v115\n"
    "v_lshlrev_b32 v58, 16, v31\n"
    "v_and_b32 v59, 0xffff0000, v31\n"
    "v_pk_add_f32 v[70:71], v[24:25], v[58:59]\n"
    "v_lshlrev_b32 v24, 16, v116\n"
    "v_and_b32 v25, 0xffff0000, v116\n"
    "v_lshlrev_b32 v58, 16, v32\n"
    "v_and_b32 v59, 0xffff0000, v32\n"
    "v_pk_add_f32 v[72:73], v[24:25], v[58:59]\n"
    "v_lshlrev_b32 v24, 16, v117\n"
    "v_and_b32 v25, 0xffff0000, v117\n"
    "v_lshlrev_b32 v58, 16, v33\n"
    "v_and_b32 v59, 0xffff0000, v33\n"
    "v_pk_add_f32 v[74:75], v[24:25], v[58:59]\n"
    "v_lshlrev_b32 v24, 16, v118\n"
    "v_and_b32 v25, 0xffff0000, v118\n"
    "v_lshlrev_b32 v58, 16, v34\n"
    "v_and_b32 v59, 0xffff0000, v34\n"
    "v_pk_add_f32 v[76:77], v[24:25], v[58:59]\n"
    "v_lshlrev_b32 v24, 16, v119\n"
    "v_and_b32 v25, 0xffff0000, v119\n"
    "v_lshlrev_b32 v58, 16, v35\n"
    "v_and_b32 v59, 0xffff0000, v35\n"
    "v_pk_add_f32 v[78:79], v[24:25], v[58:59]\n"
    "ds_read_b64_tr_b16 a[112:113], v18\n"
    "ds_read_b64_tr_b16 a[114:115], v18 offset:1024\n"
    "ds_read_b64_tr_b16 a[116:117], v19\n"
    "ds_read_b64_tr_b16 a[118:119], v19 offset:1024\n"
    "ds_read_b64_tr_b16 a[120:121], v18 offset:2048\n"
    "ds_read_b64_tr_b16 a[122:123], v18 offset:3072\n"
    "ds_read_b64_tr_b16 a[124:125], v19 offset:2048\n"
    "ds_read_b64_tr_b16 a[126:127], v19 offset:3072\n"
    "ds_read_b64_tr_b16 a[128:129], v18 offset:4096\n"
    "ds_read_b64_tr_b16 a[130:131], v18 offset:5120\n"
    "ds_read_b64_tr_b16 a[132:133], v19 offset:4096\n"
    "ds_read_b64_tr_b16 a[134:135], v19 offset:5120\n"
    "ds_read_b64_tr_b16 a[136:137], v18 offset:6144\n"
    "ds_read_b64_tr_b16 a[138:139], v18 offset:7168\n"
    "ds_read_b64_tr_b16 a[140:141], v19 offset:6144\n"
    "ds_read_b64_tr_b16 a[142:143], v19 offset:7168\n"
    "s_waitcnt vmcnt(12)\n"
    "v_max_u32 v26, v128, v132\n"
    "v_cmp_eq_u32 vcc, -1, v26\n"
    "s_nop 0\n"
    "s_cbranch_vccnz Lagain_94\n"
    "Lback_95:\n"
    "v_mfma_f32_32x32x16_bf16 v[64:79], a[0:3], v[128:131], v[64:79]\n"
    "buffer_load_dwordx4 v[184:187], v20, s[12:15], s70 offen offset:3072 sc1\n"
    "v_mfma_f32_32x32x16_bf16 v[80:95], a[4:7], v[128:131], 0\n"
    "buffer_load_dwordx4 v[188:191], v21, s[12:15], s70 offen offset:3072 sc1\n"
    "v_mfma_f32_32x32x16_bf16 v[96:111], a[0:3], v[132:135], 0\n"
    "ds_read_b64_tr_b16 a[144:145], v18 offset:8192\n"
    "ds_read_b64_tr_b16 a[146:147], v18 offset:9216\n"
    "ds_read_b64_tr_b16 a[148:149], v19 offset:8192\n"
    "ds_read_b64_tr_b16 a[150:151], v19 offset:9216\n"
    "s_waitcnt vmcnt(12)\n"
    "v_max_u32 v26, v136, v140\n"
    "v_cmp_eq_u32 vcc, -1, v26\n"
    "v_mfma_f32_32x32x16_bf16 v[112:127], a[4:7], v[132:135], 0\n"
    "s_cbranch_vccnz Lagain_96\n"
    "Lback_97:\n"
    "v_mfma_f32_32x32x16_bf16 v[64:79], a[8:11], v[136:139], v[64:79]\n"
    "s_mov_b32 s70, 0x2000\n"
    "buffer_load_dwordx4 v[192:195], v20, s[12:15], s70 offen sc1\n"
    "v_mfma_f32_32x32x16_bf16 v[80:95], a[12:15], v[136:139], v[80:95]\n"
    "buffer_load_dwordx4 v[196:199], v21, s[12:15], s70 offen sc1\n"
    "v_mfma_f32_32x32x16_bf16 v[96:111], a[8:11], v[140:143], v[96:111]\n"
    "ds_read_b64_tr_b16 a[152:153], v18 offset:10240\n"
    "ds_read_b64_tr_b16 a[154:155], v18 offset:11264\n"
    "ds_read_b64_tr_b16 a[156:157], v19 offset:10240\n"
    "ds_read_b64_tr_b16 a[158:159], v19 offset:11264\n"
    "s_waitcnt vmcnt(12)\n"
    "v_max_u32 v26, v144, v148\n"
    "v_cmp_eq_u32 vcc, -1, v26\n"
    "v_mfma_f32_32x32x16_bf16 v[112:127], a[12:15], v[140:143], v[112:127]\n"
    "s_cbranch_vccnz Lagain_98\n"
    "Lback_99:\n"
    "v_mfma_f32_32x32x16_bf16 v[64:79], a[16:19], v[144:147], v[64:79]\n"
    "buffer_load_dwordx4 v[200:203], v20, s[12:15], s70 offen offset:1024 sc1\n"
    "v_mfma_f32_32x32x16_bf16 v[80:95], a[20:23], v[144:147], v[80:95]\n"
    "buffer_load_dwordx4 v[204:207], v21, s[12:15], s70 offen offset:1024 sc1\n"
    "v_mfma_f32_32x32x16_bf16 v[96:111], a[16:19], v[148:151], v[96:111]\n"
    "ds_read_b64_tr_b16 a[160:161], v18 offset:12288\n"
    "ds_read_b64_tr_b16 a[162:163], v18 offset:13312\n"
    "ds_read_b64_tr_b16 a[164:165], v19 offset:12288\n"
    "ds_read_b64_tr_b16 a[166:167], v19 offset:13312\n"
    "s_waitcnt vmcnt(12)\n"
    "v_max_u32 v26, v152, v156\n"
    "v_cmp_eq_u32 vcc, -1, v26\n"
    "v_mfma_f32_32x32x16_bf16 v[112:127], a[20:23], v[148:151], v[112:127]\n"
    "s_cbranch_vccnz Lagain_100\n"
    "Lback_101:\n"
    "v_mfma_f32_32x32x16_bf16 v[64:79], a[24:27], v[152:155], v[64:79]\n"
    "buffer_load_dwordx4 v[208:211], v20, s[12:15], s70 offen offset:2048 sc1\n"
    "v_mfma_f32_32x32x16_bf16 v[80:95], a[28:31], v[152:155], v[80:95]\n"
    "buffer_load_dwordx4 v[212:215], v21, s[12:15], s70 offen offset:2048 sc1\n"
    "v_mfma_f32_32x32x16_bf16 v[96:111], a[24:27], v[156:159], v[96:111]\n"
    "ds_read_b64_tr_b16 a[168:169], v18 offset:14336\n"
    "ds_read_b64_tr_b16 a[170:171], v18 offset:15360\n"
    "ds_read_b64_tr_b16 a[172:173], v19 offset:14336\n"
    "ds_read_b64_tr_b16 a[174:175], v19 offset:15360\n"
    "s_waitcnt vmcnt(12)\n"
    "v_max_u32 v26, v160, v164\n"
    "v_cmp_eq_u32 vcc, -1, v26\n"
    "v_mfma_f32_32x32x16_bf16 v[112:127], a[28:31], v[156:159], v[112:127]\n"
    "s_cbranch_vccnz Lagain_102\n"
    "Lback_103:\n"
    "v_mfma_f32_32x32x16_bf16 v[64:79], a[32:35], v[160:163], v[64:79]\n"
    "buffer_load_dwordx4 v[216:219], v20, s[12:15], s70 offen offset:3072 sc1\n"
    "v_mfma_f32_32x32x16_bf16 v[80:95], a[36:39], v[160:163], v[80:95]\n"
    "buffer_load_dwordx4 v[220:223], v21, s[12:15], s70 offen offset:3072 sc1\n"
    "v_mfma_f32_32x32x16_bf16 v[96:111], a[32:35], v[164:167], v[96:111]\n"
    "ds_read_b64_tr_b16 a[176:177], v18 offset:16384\n"
    "ds_read_b64_tr_b16 a[178:179], v18 offset:17408\n"
    "ds_read_b64_tr_b16 a[180:181], v19 offset:16384\n"
    "ds_read_b64_tr_b16 a[182:183], v19 offset:17408\n"
    "s_waitcnt vmcnt(12)\n"
    "v_max_u32 v26, v168, v172\n"
    "v_cmp_eq_u32 vcc, -1, v26\n"
    "v_mfma_f32_32x32x16_bf16 v[112:127], a[36:39], v[164:167], v[112:127]\n"
    "s_cbranch_vccnz Lagain_104\n"
    "Lback_105:\n"
    "v_mfma_f32_32x32x16_bf16 v[64:79], a[40:43], v[168:171], v[64:79]\n"
    "s_mov_b32 s70, 0x3000\n"
    "buffer_load_dwordx4 v[224:227], v20, s[12:15], s70 offen sc1\n"
    "v_mfma_f32_32x32x16_bf16 v[80:95], a[44:47], v[168:171], v[80:95]\n"
    "buffer_load_dwordx4 v[228:231], v21, s[12:15], s70 offen sc1\n"
    "v_mfma_f32_32x32x16_bf16 v[96:111], a[40:43], v[172:175], v[96:111]\n"
    "ds_read_b64_tr_b16 a[184:185], v18 offset:18432\n"
    "ds_read_b64_tr_b16 a[186:187], v18 offset:19456\n"
    "ds_read_b64_tr_b16 a[188:189], v19 offset:18432\n"
    "ds_read_b64_tr_b16 a[190:191], v19 offset:19456\n"
    "s_waitcnt vmcnt(12)\n"
    "v_max_u32 v26, v176, v180\n"
    "v_cmp_eq_u32 vcc, -1, v26\n"
    "v_mfma_f32_32x32x16_bf16 v[112:127], a[44:47], v[172:175], v[112:127]\n"
    "s_cbranch_vccnz Lagain_106\n"
    "Lback_107:\n"
    "v_mfma_f32_32x32x16_bf16 v[64:79], a[48:51], v[176:179], v[64:79]\n"
    "buffer_load_dwordx4 v[232:235], v20, s[12:15], s70 offen offset:1024 sc1\n"
    "v_mfma_f32_32x32x16_bf16 v[80:95], a[52:55], v[176:179], v[80:95]\n"
    "buffer_load_dwordx4 v[236:239], v21, s[12:15], s70 offen offset:1024 sc1\n"
    "v_mfma_f32_32x32x16_bf16 v[96:111], a[48:51], v[180:183], v[96:111]\n"
    "ds_read_b64_tr_b16 a[192:193], v18 offset:20480\n"
    "ds_read_b64_tr_b16 a[194:195], v18 offset:21504\n"
    "ds_read_b64_tr_b16 a[196:197], v19 offset:20480\n"
    "ds_read_b64_tr_b16 a[198:199], v19 offset:21504\n"
    "s_waitcnt vmcnt(12)\n"
    "v_max_u32 v26, v184, v188\n"
    "v_cmp_eq_u32 vcc, -1, v26\n"
    "v_mfma_f32_32x32x16_bf16 v[112:127], a[52:55], v[180:183], v[112:127]\n"
    "s_cbranch_vccnz Lagain_108\n"
    "Lback_109:\n"
    "v_mfma_f32_32x32x16_bf16 v[64:79], a[56:59], v[184:187], v[64:79]\n"
    "buffer_load_dwordx4 v[240:243], v20, s[12:15], s70 offen offset:2048 sc1\n"
    "v_mfma_f32_32x32x16_bf16 v[80:95], a[60:63], v[184:187], v[80:95]\n"
    "buffer_load_dwordx4 v[244:247], v21, s[12:15], s70 offen offset:2048 sc1\n"
    "v_mfma_f32_32x32x16_bf16 v[96:111], a[56:59], v[188:191], v[96:111]\n"
    "ds_read_b64_tr_b16 a[200:201], v18 offset:22528\n"
    "ds_read_b64_tr_b16 a[202:203], v18 offset:23552\n"
    "ds_read_b64_tr_b16 a[204:205], v19 offset:22528\n"
    "ds_read_b64_tr_b16 a[206:207], v19 offset:23552\n"
    "s_waitcnt vmcnt(12)\n"
    "v_max_u32 v26, v192, v196\n"
    "v_cmp_eq_u32 vcc, -1, v26\n"
    "v_mfma_f32_32x32x16_bf16 v[112:127], a[60:63], v[188:191], v[112:127]\n"
    "s_cbranch_vccnz Lagain_110\n"
    "Lback_111:\n"
    "v_mfma_f32_32x32x16_bf16 v[64:79], a[64:67], v[192:195], v[64:79]\n"
    "buffer_load_dwordx4 v[248:251], v20, s[12:15], s70 offen offset:3072 sc1\n"
    "v_mfma_f32_32x32x16_bf16 v[80:95], a[68:71], v[192:195], v[80:95]\n"
    "buffer_load_dwordx4 v[252:255], v21, s[12:15], s70 offen offset:3072 sc1\n"
    "v_mfma_f32_32x32x16_bf16 v[96:111], a[64:67], v[196:199], v[96:111]\n"
    "ds_read_b64_tr_b16 a[208:209], v18 offset:24576\n"
    "ds_read_b64_tr_b16 a[210:211], v18 offset:25600\n"
    "ds_read_b64_tr_b16 a[212:213], v19 offset:24576\n"
    "ds_read_b64_tr_b16 a[214:215], v19 offset:25600\n"
    "s_waitcnt vmcnt(12)\n"
    "v_max_u32 v26, v200, v204\n"
    "v_cmp_eq_u32 vcc, -1, v26\n"
    "v_mfma_f32_32x32x16_bf16 v[112:127], a[68:71], v[196:199], v[112:127]\n"
    "s_cbranch_vccnz Lagain_112\n"
    "Lback_113:\n"
    "v_mfma_f32_32x32x16_bf16 v[64:79], a[72:75], v[200:203], v[64:79]\n"
    "s_mov_b32 s70, 0x4000\n"
    "buffer_load_dwordx4 v[128:131], v20, s[12:15], s70 offen sc1\n"
    "v_mfma_f32_32x32x16_bf16 v[80:95], a[76:79], v[200:203], v[80:95]\n"
    "buffer_load_dwordx4 v[132:135], v21, s[12:15], s70 offen sc1\n"
    "v_mfma_f32_32x32x16_bf16 v[96:111], a[72:75], v[204:207], v[96:111]\n"
    "ds_read_b64_tr_b16 a[216:217], v18 offset:26624\n"
    "ds_read_b64_tr_b16 a[218:219], v18 offset:27648\n"
    "ds_read_b64_tr_b16 a[220:221], v19 offset:26624\n"
    "ds_read_b64_tr_b16 a[222:223], v19 offset:27648\n"
    "s_waitcnt vmcnt(12)\n"
    "v_max_u32 v26, v208, v212\n"
    "v_cmp_eq_u32 vcc, -1, v26\n"
    "v_mfma_f32_32x32x16_bf16 v[112:127], a[76:79], v[204:207], v[112:127]\n"
    "s_cbranch_vccnz Lagain_114\n"
    "Lback_115:\n"
    "v_mfma_f32_32x32x16_bf16 v[64:79], a[80:83], v[208:211], v[64:79]\n"
    "buffer_load_dwordx4 v[136:139], v20, s[12:15], s70 offen offset:1024 sc1\n"
    "v_mfma_f32_32x32x16_bf16 v[80:95], a[84:87], v[208:211], v[80:95]\n"
    "buffer_load_dwordx4 v[140:143], v21, s[12:15], s70 offen offset:1024 sc1\n"
    "v_mfma_f32_32x32x16_bf16 v[96:111], a[80:83], v[212:215], v[96:111]\n"
    "s_waitcnt vmcnt(12)\n"
    "v_max_u32 v26, v216, v220\n"
    "v_cmp_eq_u32 vcc, -1, v26\n"
    "v_mfma_f32_32x32x16_bf16 v[112:127], a[84:87], v[212:215], v[112:127]\n"
    "s_cbranch_vccnz Lagain_116\n"
    "Lback_117:\n"
    "v_mfma_f32_32x32x16_bf16 v[64:79], a[88:91], v[216:219], v[64:79]\n"
    "buffer_load_dwordx4 v[144:147], v20, s[12:15], s70 offen offset:2048 sc1\n"
    "v_mfma_f32_32x32x16_bf16 v[80:95], a[92:95], v[216:219], v[80:95]\n"
    "buffer_load_dwordx4 v[148:151], v21, s[12:15], s70 offen offset:2048 sc1\n"
    "v_mfma_f32_32x32x16_bf16 v[96:111], a[88:91], v[220:223], v[96:111]\n"
    "s_waitcnt vmcnt(12)\n"
    "v_max_u32 v26, v224, v228\n"
    "v_cmp_eq_u32 vcc, -1, v26\n"
    "v_mfma_f32_32x32x16_bf16 v[112:127], a[92:95], v[220:223], v[112:127]\n"
    "s_cbranch_vccnz Lagain_118\n"
    "Lback_119:\n"
    "v_mfma_f32_32x32x16_bf16 v[64:79], a[96:99], v[224:227], v[64:79]\n"
    "buffer_load_dwordx4 v[152:155], v20, s[12:15], s70 offen offset:3072 sc1\n"
    "v_mfma_f32_32x32x16_bf16 v[80:95], a[100:103], v[224:227], v[80:95]\n"
    "buffer_load_dwordx4 v[156:159], v21, s[12:15], s70 offen offset:3072 sc1\n"
    "v_mfma_f32_32x32x16_bf16 v[96:111], a[96:99], v[228:231], v[96:111]\n"
    "s_waitcnt lgkmcnt(0)\n"
    "s_waitcnt vmcnt(12)\n"
    "v_max_u32 v26, v232, v236\n"
    "v_cmp_eq_u32 vcc, -1, v26\n"
    "v_mfma_f32_32x32x16_bf16 v[112:127], a[100:103], v[228:231], v[112:127]\n"
    "s_cbranch_vccnz Lagain_120\n"
    "Lback_121:\n"
    "v_mfma_f32_32x32x16_bf16 v[64:79], a[104:107], v[232:235], v[64:79]\n"
    "s_mov_b32 s70, 0x5000\n"
    "buffer_load_dwordx4 v[160:163], v20, s[12:15], s70 offen sc1\n"
    "v_mfma_f32_32x32x16_bf16 v[80:95], a[108:111], v[232:235], v[80:95]\n"
    "buffer_load_dwordx4 v[164:167], v21, s[12:15], s70 offen sc1\n"
    "v_mfma_f32_32x32x16_bf16 v[96:111], a[104:107], v[236:239], v[96:111]\n"
    "s_waitcnt vmcnt(12)\n"
    "v_max_u32 v26, v240, v244\n"
    "v_cmp_eq_u32 vcc, -1, v26\n"
    "v_mfma_f32_32x32x16_bf16 v[112:127], a[108:111], v[236:239], v[112:127]\n"
    "s_cbranch_vccnz Lagain_122\n"
    "Lback_123:\n"
    "v_mfma_f32_32x32x16_bf16 v[64:79], a[112:115], v[240:243], v[64:79]\n"
    "buffer_load_dwordx4 v[168:171], v20, s[12:15], s70 offen offset:1024 sc1\n"
    "v_mfma_f32_32x32x16_bf16 v[80:95], a[116:119], v[240:243], v[80:95]\n"
    "buffer_load_dwordx4 v[172:175], v21, s[12:15], s70 offen offset:1024 sc1\n"
    "v_mfma_f32_32x32x16_bf16 v[96:111], a[112:115], v[244:247], v[96:111]\n"
    "s_waitcnt vmcnt(12)\n"
    "v_max_u32 v26, v248, v252\n"
    "v_cmp_eq_u32 vcc, -1, v26\n"
    "v_mfma_f32_32x32x16_bf16 v[112:127], a[116:119], v[244:247], v[112:127]\n"
    "s_cbranch_vccnz Lagain_124\n"
    "Lback_125:\n"
    "v_mfma_f32_32x32x16_bf16 v[64:79], a[120:123], v[248:251], v[64:79]\n"
    "buffer_load_dwordx4 v[180:183], v21, s[12:15], s70 offen offset:2048 sc1\n"
    "v_mfma_f32_32x32x16_bf16 v[80:95], a[124:127], v[248:251], v[80:95]\n"
    "buffer_load_dwordx4 v[188:191], v21, s[12:15], s70 offen offset:3072 sc1\n"
    "v_mfma_f32_32x32x16_bf16 v[96:111], a[120:123], v[252:255], v[96:111]\n"
    "s_waitcnt vmcnt(12)\n"
    "v_max_u32 v26, v128, v132\n"
    "v_cmp_eq_u32 vcc, -1, v26\n"
    "v_mfma_f32_32x32x16_bf16 v[112:127], a[124:127], v[252:255], v[112:127]\n"
    "s_cbranch_vccnz Lagain_126\n"
    "Lback_127:\n"
    "v_mfma_f32_32x32x16_bf16 v[64:79], a[128:131], v[128:131], v[64:79]\n"
    "s_mov_b32 s70, 0x6000\n"
    "buffer_load_dwordx4 v[196:199], v21, s[12:15], s70 offen sc1\n"
    "v_mfma_f32_32x32x16_bf16 v[80:95], a[132:135], v[128:131], v[80:95]\n"
    "buffer_load_dwordx4 v[204:207], v21, s[12:15], s70 offen offset:1024 sc1\n"
    "v_mfma_f32_32x32x16_bf16 v[96:111], a[128:131], v[132:135], v[96:111]\n"
    "s_waitcnt vmcnt(12)\n"
    "v_max_u32 v26, v136, v140\n"
    "v_cmp_eq_u32 vcc, -1, v26\n"
    "v_mfma_f32_32x32x16_bf16 v[112:127], a[132:135], v[132:135], v[112:127]\n"
    "s_cbranch_vccnz Lagain_128\n"
    "Lback_129:\n"
    "v_mfma_f32_32x32x16_bf16 v[64:79], a[136:139], v[136:139], v[64:79]\n"
    "buffer_load_dwordx4 v[212:215], v21, s[12:15], s70 offen offset:2048 sc1\n"
    "v_mfma_f32_32x32x16_bf16 v[80:95], a[140:143], v[136:139], v[80:95]\n"
    "buffer_load_dwordx4 v[220:223], v21, s[12:15], s70 offen offset:3072 sc1\n"
    "v_mfma_f32_32x32x16_bf16 v[96:111], a[136:139], v[140:143], v[96:111]\n"
    "s_waitcnt vmcnt(12)\n"
    "v_max_u32 v26, v144, v148\n"
    "v_cmp_eq_u32 vcc, -1, v26\n"
    "v_mfma_f32_32x32x16_bf16 v[112:127], a[140:143], v[140:143], v[112:127]\n"
    "s_cbranch_vccnz Lagain_130\n"
    "Lback_131:\n"
    "v_mfma_f32_32x32x16_bf16 v[64:79], a[144:147], v[144:147], v[64:79]\n"
    "s_mov_b32 s70, 0x5000\n"
    "buffer_load_dwordx4 v[176:179], v20, s[12:15], s70 offen offset:2048 sc1\n"
    "v_mfma_f32_32x32x16_bf16 v[80:95], a[148:151], v[144:147], v[80:95]\n"
    "buffer_load_dwordx4 v[184:187], v20, s[12:15], s70 offen offset:3072 sc1\n"
    "v_mfma_f32_32x32x16_bf16 v[96:111], a[144:147], v[148:151], v[96:111]\n"
    "s_waitcnt vmcnt(12)\n"
    "v_max_u32 v26, v152, v156\n"
    "v_cmp_eq_u32 vcc, -1, v26\n"
    "v_mfma_f32_32x32x16_bf16 v[112:127], a[148:151], v[148:151], v[112:127]\n"
    "s_cbranch_vccnz Lagain_132\n"
    "Lback_133:\n"
    "v_mfma_f32_32x32x16_bf16 v[64:79], a[152:155], v[152:155], v[64:79]\n"
    "s_mov_b32 s70, 0x6000\n"
    "buffer_load_dwordx4 v[192:195], v20, s[12:15], s70 offen sc1\n"
    "v_mfma_f32_32x32x16_bf16 v[80:95], a[156:159], v[152:155], v[80:95]\n"
    "buffer_load_dwordx4 v[200:203], v20, s[12:15], s70 offen offset:1024 sc1\n"
    "v_mfma_f32_32x32x16_bf16 v[96:111], a[152:155], v[156:159], v[96:111]\n"
    "s_waitcnt vmcnt(12)\n"
    "v_max_u32 v26, v160, v164\n"
    "v_cmp_eq_u32 vcc, -1, v26\n"
    "v_mfma_f32_32x32x16_bf16 v[112:127], a[156:159], v[156:159], v[112:127]\n"
    "s_cbranch_vccnz Lagain_134\n"
    "Lback_135:\n"
    "v_mfma_f32_32x32x16_bf16 v[64:79], a[160:163], v[160:163], v[64:79]\n"
    "buffer_load_dwordx4 v[208:211], v20, s[12:15], s70 offen offset:2048 sc1\n"
    "v_mfma_f32_32x32x16_bf16 v[80:95], a[164:167], v[160:163], v[80:95]\n"
    "buffer_load_dwordx4 v[216:219], v20, s[12:15], s70 offen offset:3072 sc1\n"
    "v_mfma_f32_32x32x16_bf16 v[96:111], a[160:163], v[164:167], v[96:111]\n"
    "s_waitcnt vmcnt(12)\n"
    "v_max_u32 v26, v168, v172\n"
    "v_cmp_eq_u32 vcc, -1, v26\n"
    "v_mfma_f32_32x32x16_bf16 v[112:127], a[164:167], v[164:167], v[112:127]\n"
    "s_cbranch_vccnz Lagain_136\n"
    "Lback_137:\n"
    "v_mfma_f32_32x32x16_bf16 v[64:79], a[168:171], v[168:171], v[64:79]\n"
    "v_mfma_f32_32x32x16_bf16 v[80:95], a[172:175], v[168:171], v[80:95]\n"
    "v_mfma_f32_32x32x16_bf16 v[96:111], a[168:171], v[172:175], v[96:111]\n"
    "s_waitcnt vmcnt(11)\n"
    "v_cmp_eq_u32 vcc, -1, v180\n"
    "v_mfma_f32_32x32x16_bf16 v[112:127], a[172:175], v[172:175], v[112:127]\n"
    "s_cbranch_vccnz Lagain_138\n"
    "Lback_139:\n"
    "v_mfma_f32_32x32x16_bf16 v[112:127], a[180:183], v[180:183], v[112:127]\n"
    "s_waitcnt vmcnt(10)\n"
    "v_cmp_eq_u32 vcc, -1, v188\n"
    "v_mfma_f32_32x32x16_bf16 v[96:111], a[176:179], v[180:183], v[96:111]\n"
    "s_cbranch_vccnz Lagain_140\n"
    "Lback_141:\n"
    "v_mfma_f32_32x32x16_bf16 v[112:127], a[188:191], v[188:191], v[112:127]\n"
    "s_waitcnt vmcnt(9)\n"
    "v_cmp_eq_u32 vcc, -1, v196\n"
    "v_mfma_f32_32x32x16_bf16 v[96:111], a[184:187], v[188:191], v[96:111]\n"
    "s_cbranch_vccnz Lagain_142\n"
    "Lback_143:\n"
    "v_mfma_f32_32x32x16_bf16 v[112:127], a[196:199], v[196:199], v[112:127]\n"
    "s_waitcnt vmcnt(8)\n"
    "v_cmp_eq_u32 vcc, -1, v204\n"
    "v_mfma_f32_32x32x16_bf16 v[96:111], a[192:195], v[196:199], v[96:111]\n"
    "s_cbranch_vccnz Lagain_144\n"
    "Lback_145:\n"
    "v_mfma_f32_32x32x16_bf16 v[112:127], a[204:207], v[204:207], v[112:127]\n"
    "s_waitcnt vmcnt(7)\n"
    "v_cmp_eq_u32 vcc, -1, v212\n"
    "v_mfma_f32_32x32x16_bf16 v[96:111], a[200:203], v[204:207], v[96:111]\n"
    "s_cbranch_vccnz Lagain_146\n"
    "Lback_147:\n"
    "v_mfma_f32_32x32x16_bf16 v[112:127], a[212:215], v[212:215], v[112:127]\n"
    "s_waitcnt vmcnt(6)\n"
    "v_cmp_eq_u32 vcc, -1, v220\n"
    "v_mfma_f32_32x32x16_bf16 v[96:111], a[208:211], v[212:215], v[96:111]\n"
    "s_cbranch_vccnz Lagain_148\n"
    "Lback_149:\n"
    "v_mfma_f32_32x32x16_bf16 v[112:127], a[220:223], v[220:223], v[112:127]\n"
    "s_waitcnt vmcnt(5)\n"
    "v_cmp_eq_u32 vcc, -1, v176\n"
    "v_mfma_f32_32x32x16_bf16 v[96:111], a[216:219], v[220:223], v[96:111]\n"
    "s_cbranch_vccnz Lagain_150\n"
    "Lback_151:\n"
    "s_nop 2\n"
    "v_mfma_f32_32x32x16_bf16 v[80:95], a[180:183], v[176:179], v[80:95]\n"
    "s_nop 3\n"
    "ds_write_b128 v8, v[112:115]\n"
    "s_waitcnt vmcnt(4)\n"
    "v_cmp_eq_u32 vcc, -1, v184\n"
    "v_mfma_f32_32x32x16_bf16 v[64:79], a[176:179], v[176:179], v[64:79]\n"
    "s_cbranch_vccnz Lagain_152\n"
    "Lback_153:\n"
    "ds_write_b128 v8, v[116:119] offset:1024\n"
    "v_mfma_f32_32x32x16_bf16 v[80:95], a[188:191], v[184:187], v[80:95]\n"
    "ds_write_b128 v8, v[120:123] offset:2048\n"
    "s_waitcnt vmcnt(3)\n"
    "v_cmp_eq_u32 vcc, -1, v192\n"
    "v_mfma_f32_32x32x16_bf16 v[64:79], a[184:187], v[184:187], v[64:79]\n"
    "s_cbranch_vccnz Lagain_154\n"
    "Lback_155:\n"
    "ds_write_b128 v8, v[124:127] offset:3072\n"
    "v_mfma_f32_32x32x16_bf16 v[80:95], a[196:199], v[192:195], v[80:95]\n"
    "s_waitcnt vmcnt(2)\n"
    "v_cmp_eq_u32 vcc, -1, v200\n"
    "v_mfma_f32_32x32x16_bf16 v[64:79], a[192:195], v[192:195], v[64:79]\n"
    "s_cbranch_vccnz Lagain_156\n"
    "Lback_157:\n"
    "ds_write_b128 v7, v[96:99]\n"
    "v_mfma_f32_32x32x16_bf16 v[80:95], a[204:207], v[200:203], v[80:95]\n"
    "ds_write_b128 v7, v[100:103] offset:1024\n"
    "s_waitcnt vmcnt(1)\n"
    "v_cmp_eq_u32 vcc, -1, v208\n"
    "v_mfma_f32_32x32x16_bf16 v[64:79], a[200:203], v[200:203], v[64:79]\n"
    "s_cbranch_vccnz Lagain_158\n"
    "Lback_159:\n"
    "ds_write_b128 v7, v[104:107] offset:2048\n"
    "s_waitcnt vmcnt(0)\n"
    "v_cmp_eq_u32 vcc, -1, v216\n"
    "v_mfma_f32_32x32x16_bf16 v[80:95], a[212:215], v[208:211], v[80:95]\n"
    "s_cbranch_vccnz Lagain_160\n"
    "Lback_161:\n"
    "ds_write_b128 v7, v[108:111] offset:3072\n"
    "v_mfma_f32_32x32x16_bf16 v[80:95], a[220:223], v[216:219], v[80:95]\n"
    "s_nop 4\n"
    "v_mfma_f32_32x32x16_bf16 v[64:79], a[208:211], v[208:211], v[64:79]\n"
    "s_nop 4\n"
    "v_mfma_f32_32x32x16_bf16 v[64:79], a[216:219], v[216:219], v[64:79]\n"
    "ds_write_b128 v6, v[80:83]\n"
    "ds_write_b128 v6, v[84:87] offset:1024\n"
    "ds_write_b128 v6, v[88:91] offset:2048\n"
    "ds_write_b128 v6, v[92:95] offset:3072\n"
    "s_waitcnt lgkmcnt(0)\n"
    "s_barrier\n"
    "ds_read_b128 v[128:131], v9\n"
    "ds_read_b128 v[144:147], v9 offset:4096\n"
    "ds_read_b128 v[160:163], v9 offset:8192\n"
    "ds_read_b128 v[132:135], v9 offset:1024\n"
    "ds_read_b128 v[148:151], v9 offset:5120\n"
    "ds_read_b128 v[164:167], v9 offset:9216\n"
    "ds_read_b128 v[136:139], v9 offset:2048\n"
    "ds_read_b128 v[152:155], v9 offset:6144\n"
    "ds_read_b128 v[168:171], v9 offset:10240\n"
    "ds_read_b128 v[140:143], v9 offset:3072\n"
    "ds_read_b128 v[156:159], v9 offset:7168\n"
    "ds_read_b128 v[172:175], v9 offset:11264\n"
    "s_waitcnt lgkmcnt(10)\n"
    "v_pk_add_f32 v[128:129], v[128:129], v[144:145]\n"
    "v_pk_add_f32 v[130:131], v[130:131], v[146:147]\n"
    "s_waitcnt lgkmcnt(9)\n"
    "v_pk_add_f32 v[64:65], v[64:65], v[160:161]\n"
    "v_pk_add_f32 v[66:67], v[66:67], v[162:163]\n"
    "v_pk_add_f32 v[64:65], v[64:65], v[128:129]\n"
    "v_pk_add_f32 v[66:67], v[66:67], v[130:131]\n"
    "v_cvt_pk_bf16_f32 v176, v64, v65\n"
    "v_cvt_pk_bf16_f32 v177, v66, v67\n"
    "buffer_store_short v176, v57, s[36:39], s66 offen sc1\n"
    "buffer_store_short_d16_hi v176, v57, s[36:39], s66 offen offset:896 sc1\n"
    "buffer_store_short v177, v57, s[36:39], s66 offen offset:1792 sc1\n"
    "buffer_store_short_d16_hi v177, v57, s[36:39], s66 offen offset:2688 sc1\n"
    "s_add_u32 s66, s66, 0x1c00\n"
    "s_waitcnt lgkmcnt(7)\n"
    "v_pk_add_f32 v[132:133], v[132:133], v[148:149]\n"
    "v_pk_add_f32 v[134:135], v[134:135], v[150:151]\n"
    "s_waitcnt lgkmcnt(6)\n"
    "v_pk_add_f32 v[68:69], v[68:69], v[164:165]\n"
    "v_pk_add_f32 v[70:71], v[70:71], v[166:167]\n"
    "v_pk_add_f32 v[68:69], v[68:69], v[132:133]\n"
    "v_pk_add_f32 v[70:71], v[70:71], v[134:135]\n"
    "v_cvt_pk_bf16_f32 v178, v68, v69\n"
    "v_cvt_pk_bf16_f32 v179, v70, v71\n"
    "buffer_store_short v178, v57, s[36:39], s66 offen sc1\n"
    "buffer_store_short_d16_hi v178, v57, s[36:39], s66 offen offset:896 sc1\n"
    "buffer_store_short v179, v57, s[36:39], s66 offen offset:1792 sc1\n"
    "buffer_store_short_d16_hi v179, v57, s[36:39], s66 offen offset:2688 sc1\n"
    "s_add_u32 s66, s66, 0x1c00\n"
    "s_waitcnt lgkmcnt(4)\n"
    "v_pk_add_f32 v[136:137], v[136:137], v[152:153]\n"
    "v_pk_add_f32 v[138:139], v[138:139], v[154:155]\n"
    "s_waitcnt lgkmcnt(3)\n"
    "v_pk_add_f32 v[72:73], v[72:73], v[168:169]\n"
    "v_pk_add_f32 v[74:75], v[74:75], v[170:171]\n"
    "v_pk_add_f32 v[72:73], v[72:73], v[136:137]\n"
    "v_pk_add_f32 v[74:75], v[74:75], v[138:139]\n"
    "v_cvt_pk_bf16_f32 v180, v72, v73\n"
    "v_cvt_pk_bf16_f32 v181, v74, v75\n"
    "buffer_store_short v180, v57, s[36:39], s66 offen sc1\n"
    "buffer_store_short_d16_hi v180, v57, s[36:39], s66 offen offset:896 sc1\n"
    "buffer_store_short v181, v57, s[36:39], s66 offen offset:1792 sc1\n"
    "buffer_store_short_d16_hi v181, v57, s[36:39], s66 offen offset:2688 sc1\n"
    "s_add_u32 s66, s66, 0x1c00\n"
    "s_waitcnt lgkmcnt(1)\n"
    "v_pk_add_f32 v[140:141], v[140:141], v[156:157]\n"
    "v_pk_add_f32 v[142:143], v[142:143], v[158:159]\n"
    "s_waitcnt lgkmcnt(0)\n"
    "v_pk_add_f32 v[76:77], v[76:77], v[172:173]\n"
    "v_pk_add_f32 v[78:79], v[78:79], v[174:175]\n"
    "v_pk_add_f32 v[76:77], v[76:77], v[140:141]\n"
    "v_pk_add_f32 v[78:79], v[78:79], v[142:143]\n"
    "v_cvt_pk_bf16_f32 v182, v76, v77\n"
    "v_cvt_pk_bf16_f32 v183, v78, v79\n"
    "buffer_store_short v182, v57, s[36:39], s66 offen sc1\n"
    "buffer_store_short_d16_hi v182, v57, s[36:39], s66 offen offset:896 sc1\n"
    "buffer_store_short v183, v57, s[36:39], s66 offen offset:1792 sc1\n"
    "buffer_store_short_d16_hi v183, v57, s[36:39], s66 offen offset:2688 sc1\n"
    "s_lshl2_add_u32 s62, s45, s44\n"
    "s_cmp_eq_u32 s62, 0\n"
    "s_cbranch_scc0 L_end\n"
    "s_mov_b64 exec, 1\n"
    "buffer_store_dword v22, off, s[12:15], 0 sc1\n"
    "s_mov_b64 exec, -1\n"
    "L_end:\n"
    "s_endpgm\n"
    "L_idle:\n"
    "s_lshr_b32 s60, s45, 3\n"
    "s_sub_u32 s60, s60, 24\n"
    "s_lshl2_add_u32 s60, s60, s44\n"
    "s_mul_i32 s60, s60, 7\n"
    "s_bfe_u32 s61, s45, 0x20001\n"
    "s_mul_i32 s61, s61, 0x1c0\n"
    "s_add_u32 s60, s60, s61\n"
    "s_add_u32 s60, s60, 0xe0\n"
    "s_lshl_b32 s60, s60, 12\n"
    "v_lshlrev_b32 v24, 7, v0\n"
    "s_sleep 20\n"
    "buffer_load_dword v28, v24, s[24:27], s60 offen\n"
    "s_add_u32 s60, s60, 0x2000\n"
    "buffer_load_dword v29, v24, s[24:27], s60 offen\n"
    "s_add_u32 s60, s60, 0x2000\n"
    "buffer_load_dword v30, v24, s[24:27], s60 offen\n"
    "s_add_u32 s60, s60, 0x2000\n"
    "buffer_load_dword v31, v24, s[24:27], s60 offen\n"
    "s_add_u32 s60, s60, 0x2000\n"
    "s_waitcnt vmcnt(0)\n"
    "s_branch L_end\n"
    "L_f1:\n"
    "s_mov_b32 exec_lo, 0xff00ff\n"
    "s_mov_b32 exec_hi, 0\n"
    "s_mov_b32 s60, 0x100000\n"
    "Lpoll_162:\n"
    "buffer_load_dwordx2 v[32:33], v11, s[12:15], 0 offen sc1\n"
    "s_waitcnt vmcnt(0)\n"
    "v_cmp_ne_u64 vcc, v[32:33], v[22:23]\n"
    "s_nop 0\n"
    "s_cbranch_vccz Ldone_163\n"
    "s_sub_u32 s60, s60, 1\n"
    "s_sleep 1\n"
    "s_cbranch_scc0 Lpoll_162\n"
    "Ldone_163:\n"
    "s_mov_b64 exec, -1\n"
    "s_nop 7\n"
    "s_branch L_f1_back\n"
    "L_f3:\n"
    "s_mov_b32 s60, 0x100000\n"
    "Lpoll_165:\n"
    "buffer_load_dwordx2 v[32:33], v11, s[12:15], 0 offen sc1\n"
    "s_waitcnt vmcnt(0)\n"
    "v_cmp_ne_u64 vcc, v[32:33], v[22:23]\n"
    "s_nop 0\n"
    "s_cbranch_vccz Ldone_166\n"
    "s_sub_u32 s60, s60, 1\n"
    "s_sleep 1\n"
    "s_cbranch_scc0 Lpoll_165\n"
    "Ldone_166:\n"
    "s_nop 7\n"
    "s_branch L_f3_back\n"
    "L_fx:\n"
    "s_mov_b32 s60, 0x100000\n"
    "Lpoll_168:\n"
    "buffer_load_dwordx2 v[42:43], v41, s[12:15], 0 offen sc1\n"
    "s_waitcnt vmcnt(0)\n"
    "v_cmp_ne_u64 vcc, v[42:43], v[22:23]\n"
    "s_nop 0\n"
    "s_cbranch_vccz Ldone_169\n"
    "s_sub_u32 s60, s60, 1\n"
    "s_sleep 1\n"
    "s_cbranch_scc0 Lpoll_168\n"
    "Ldone_169:\n"
    "s_nop 7\n"
    "s_branch L_fx_back\n"
    "Lagain_4:\n"
    "s_waitcnt vmcnt(0) lgkmcnt(0)\n"
    "s_mov_b32 s85, 0x10000\n"
    "Llp_171:\n"
    "s_mov_b32 m0, s76\n"
    "s_nop 0\n"
    "buffer_load_dwordx4 v1, s[12:15], s74 offen sc1 lds\n"
    "buffer_load_dwordx4 v1, s[12:15], s74 offen offset:1024 sc1 lds\n"
    "buffer_load_dwordx4 v1, s[12:15], s74 offen offset:2048 sc1 lds\n"
    "buffer_load_dwordx4 v1, s[12:15], s74 offen offset:3072 sc1 lds\n"
    "s_add_u32 m0, s76, 0x1000\n"
    "s_nop 0\n"
    "buffer_load_dwordx4 v1, s[12:15], s75 offen sc1 lds\n"
    "buffer_load_dwordx4 v1, s[12:15], s75 offen offset:1024 sc1 lds\n"
    "buffer_load_dwordx4 v1, s[12:15], s75 offen offset:2048 sc1 lds\n"
    "buffer_load_dwordx4 v1, s[12:15], s75 offen offset:3072 sc1 lds\n"
    "s_waitcnt vmcnt(0)\n"
    "ds_read_b128 v[24:27], v56\n"
    "ds_read_b128 v[28:31], v56 offset:1024\n"
    "ds_read_b128 v[32:35], v56 offset:2048\n"
    "ds_read_b128 v[36:39], v56 offset:3072\n"
    "ds_read_b128 v[40:43], v56 offset:4096\n"
    "ds_read_b128 v[44:47], v56 offset:5120\n"
    "ds_read_b128 v[48:51], v56 offset:6144\n"
    "ds_read_b128 v[52:55], v56 offset:7168\n"
    "s_waitcnt lgkmcnt(0)\n"
    "v_mov_b32 v101, v24\n"
    "v_max_u32 v101, v101, v28\n"
    "v_max_u32 v101, v101, v32\n"
    "v_max_u32 v101, v101, v36\n"
    "v_max_u32 v101, v101, v40\n"
    "v_max_u32 v101, v101, v44\n"
    "v_max_u32 v101, v101, v48\n"
    "v_max_u32 v101, v101, v52\n"
    "v_cmp_eq_u32 vcc, -1, v101\n"
    "s_sub_u32 s85, s85, 1\n"
    "s_cselect_b64 vcc, 0, vcc\n"
    "s_cbranch_vccnz Llp_171\n"
    "s_nop 3\n"
    "s_branch Lback_5\n"
    "Lagain_11:\n"
    "s_waitcnt vmcnt(0) lgkmcnt(0)\n"
    "s_mov_b32 s85, 0x10000\n"
    "Llp_172:\n"
    "s_xor_b32 s84, s2, 0\n"
    "buffer_load_dwordx4 v[128:131], v20, s[12:15], s84 offen sc1\n"
    "s_xor_b32 s84, s2, 0\n"
    "buffer_load_dwordx4 v[132:135], v21, s[12:15], s84 offen sc1\n"
    "s_waitcnt vmcnt(0)\n"
    "v_mov_b32 v26, v128\n"
    "v_max_u32 v26, v26, v132\n"
    "v_cmp_eq_u32 vcc, -1, v26\n"
    "s_sub_u32 s85, s85, 1\n"
    "s_cselect_b64 vcc, 0, vcc\n"
    "s_cbranch_vccnz Llp_172\n"
    "s_nop 3\n"
    "s_branch Lback_12\n"
    "Lagain_13:\n"
    "s_waitcnt vmcnt(0) lgkmcnt(0)\n"
    "s_mov_b32 s85, 0x10000\n"
    "Llp_173:\n"
    "s_xor_b32 s84, s2, 0\n"
    "buffer_load_dwordx4 v[136:139], v20, s[12:15], s84 offen offset:1024 sc1\n"
    "s_xor_b32 s84, s2, 0\n"
    "buffer_load_dwordx4 v[140:143], v21, s[12:15], s84 offen offset:1024 sc1\n"
    "s_waitcnt vmcnt(0)\n"
    "v_mov_b32 v26, v136\n"
    "v_max_u32 v26, v26, v140\n"
    "v_cmp_eq_u32 vcc, -1, v26\n"
    "s_sub_u32 s85, s85, 1\n"
    "s_cselect_b64 vcc, 0, vcc\n"
    "s_cbranch_vccnz Llp_173\n"
    "s_nop 3\n"
    "s_branch Lback_14\n"
    "Lagain_15:\n"
    "s_waitcnt vmcnt(0) lgkmcnt(0)\n"
    "s_mov_b32 s85, 0x10000\n"
    "Llp_174:\n"
    "s_xor_b32 s84, s2, 0x800\n"
    "buffer_load_dwordx4 v[144:147], v20, s[12:15], s84 offen sc1\n"
    "s_xor_b32 s84, s2, 0x800\n"
    "buffer_load_dwordx4 v[148:151], v21, s[12:15], s84 offen sc1\n"
    "s_waitcnt vmcnt(0)\n"
    "v_mov_b32 v26, v144\n"
    "v_max_u32 v26, v26, v148\n"
    "v_cmp_eq_u32 vcc, -1, v26\n"
    "s_sub_u32 s85, s85, 1\n"
    "s_cselect_b64 vcc, 0, vcc\n"
    "s_cbranch_vccnz Llp_174\n"
    "s_nop 3\n"
    "s_branch Lback_16\n"
    "Lagain_17:\n"
    "s_waitcnt vmcnt(0) lgkmcnt(0)\n"
    "s_mov_b32 s85, 0x10000\n"
    "Llp_175:\n"
    "s_xor_b32 s84, s2, 0x800\n"
    "buffer_load_dwordx4 v[152:155], v20, s[12:15], s84 offen offset:1024 sc1\n"
    "s_xor_b32 s84, s2, 0x800\n"
    "buffer_load_dwordx4 v[156:159], v21, s[12:15], s84 offen offset:1024 sc1\n"
    "s_waitcnt vmcnt(0)\n"
    "v_mov_b32 v26, v152\n"
    "v_max_u32 v26, v26, v156\n"
    "v_cmp_eq_u32 vcc, -1, v26\n"
    "s_sub_u32 s85, s85, 1\n"
    "s_cselect_b64 vcc, 0, vcc\n"
    "s_cbranch_vccnz Llp_175\n"
    "s_nop 3\n"
    "s_branch Lback_18\n"
    "Lagain_19:\n"
    "s_waitcnt vmcnt(0) lgkmcnt(0)\n"
    "s_mov_b32 s85, 0x10000\n"
    "Llp_176:\n"
    "s_xor_b32 s84, s2, 0x1000\n"
    "buffer_load_dwordx4 v[160:163], v20, s[12:15], s84 offen sc1\n"
    "s_xor_b32 s84, s2, 0x1000\n"
    "buffer_load_dwordx4 v[164:167], v21, s[12:15], s84 offen sc1\n"
    "s_waitcnt vmcnt(0)\n"
    "v_mov_b32 v26, v160\n"
    "v_max_u32 v26, v26, v164\n"
    "v_cmp_eq_u32 vcc, -1, v26\n"
    "s_sub_u32 s85, s85, 1\n"
    "s_cselect_b64 vcc, 0, vcc\n"
    "s_cbranch_vccnz Llp_176\n"
    "s_nop 3\n"
    "s_branch Lback_20\n"
    "Lagain_21:\n"
    "s_waitcnt vmcnt(0) lgkmcnt(0)\n"
    "s_mov_b32 s85, 0x10000\n"
    "Llp_177:\n"
    "s_xor_b32 s84, s2, 0x1000\n"
    "buffer_load_dwordx4 v[168:171], v20, s[12:15], s84 offen offset:1024 sc1\n"
    "s_xor_b32 s84, s2, 0x1000\n"
    "buffer_load_dwordx4 v[172:175], v21, s[12:15], s84 offen offset:1024 sc1\n"
    "s_waitcnt vmcnt(0)\n"
    "v_mov_b32 v26, v168\n"
    "v_max_u32 v26, v26, v172\n"
    "v_cmp_eq_u32 vcc, -1, v26\n"
    "s_sub_u32 s85, s85, 1\n"
    "s_cselect_b64 vcc, 0, vcc\n"
    "s_cbranch_vccnz Llp_177\n"
    "s_nop 3\n"
    "s_branch Lback_22\n"
    "Lagain_23:\n"
    "s_waitcnt vmcnt(0) lgkmcnt(0)\n"
    "s_mov_b32 s85, 0x10000\n"
    "Llp_178:\n"
    "s_xor_b32 s84, s2, 0x1800\n"
    "buffer_load_dwordx4 v[176:179], v20, s[12:15], s84 offen sc1\n"
    "s_xor_b32 s84, s2, 0x1800\n"
    "buffer_load_dwordx4 v[180:183], v21, s[12:15], s84 offen sc1\n"
    "s_waitcnt vmcnt(0)\n"
    "v_mov_b32 v26, v176\n"
    "v_max_u32 v26, v26, v180\n"
    "v_cmp_eq_u32 vcc, -1, v26\n"
    "s_sub_u32 s85, s85, 1\n"
    "s_cselect_b64 vcc, 0, vcc\n"
    "s_cbranch_vccnz Llp_178\n"
    "s_nop 3\n"
    "s_branch Lback_24\n"
    "Lagain_25:\n"
    "s_waitcnt vmcnt(0) lgkmcnt(0)\n"
    "s_mov_b32 s85, 0x10000\n"
    "Llp_179:\n"
    "s_xor_b32 s84, s2, 0x1800\n"
    "buffer_load_dwordx4 v[184:187], v20, s[12:15], s84 offen offset:1024 sc1\n"
    "s_xor_b32 s84, s2, 0x1800\n"
    "buffer_load_dwordx4 v[188:191], v21, s[12:15], s84 offen offset:1024 sc1\n"
    "s_waitcnt vmcnt(0)\n"
    "v_mov_b32 v26, v184\n"
    "v_max_u32 v26, v26, v188\n"
    "v_cmp_eq_u32 vcc, -1, v26\n"
    "s_sub_u32 s85, s85, 1\n"
    "s_cselect_b64 vcc, 0, vcc\n"
    "s_cbranch_vccnz Llp_179\n"
    "s_nop 3\n"
    "s_branch Lback_26\n"
    "Lagain_27:\n"
    "s_waitcnt vmcnt(0) lgkmcnt(0)\n"
    "s_mov_b32 s85, 0x10000\n"
    "Llp_180:\n"
    "s_xor_b32 s84, s2, 0x2000\n"
    "buffer_load_dwordx4 v[192:195], v20, s[12:15], s84 offen sc1\n"
    "s_xor_b32 s84, s2, 0x2000\n"
    "buffer_load_dwordx4 v[196:199], v21, s[12:15], s84 offen sc1\n"
    "s_waitcnt vmcnt(0)\n"
    "v_mov_b32 v26, v192\n"
    "v_max_u32 v26, v26, v196\n"
    "v_cmp_eq_u32 vcc, -1, v26\n"
    "s_sub_u32 s85, s85, 1\n"
    "s_cselect_b64 vcc, 0, vcc\n"
    "s_cbranch_vccnz Llp_180\n"
    "s_nop 3\n"
    "s_branch Lback_28\n"
    "Lagain_29:\n"
    "s_waitcnt vmcnt(0) lgkmcnt(0)\n"
    "s_mov_b32 s85, 0x10000\n"
    "Llp_181:\n"
    "buffer_load_dword v25, v14, s[12:15], s74 offen sc1\n"
    "s_waitcnt vmcnt(0)\n"
    "v_mov_b32 v26, v25\n"
    "v_cmp_eq_u32 vcc, -1, v26\n"
    "s_sub_u32 s85, s85, 1\n"
    "s_cselect_b64 vcc, 0, vcc\n"
    "s_cbranch_vccnz Llp_181\n"
    "s_nop 3\n"
    "s_branch Lback_30\n"
    "Lagain_31:\n"
    "s_waitcnt vmcnt(0) lgkmcnt(0)\n"
    "s_mov_b32 s85, 0x10000\n"
    "Llp_182:\n"
    "s_xor_b32 s84, s2, 0x2000\n"
    "buffer_load_dwordx4 v[200:203], v20, s[12:15], s84 offen offset:1024 sc1\n"
    "s_xor_b32 s84, s2, 0x2000\n"
    "buffer_load_dwordx4 v[204:207], v21, s[12:15], s84 offen offset:1024 sc1\n"
    "s_waitcnt vmcnt(0)\n"
    "v_mov_b32 v26, v200\n"
    "v_max_u32 v26, v26, v204\n"
    "v_cmp_eq_u32 vcc, -1, v26\n"
    "s_sub_u32 s85, s85, 1\n"
    "s_cselect_b64 vcc, 0, vcc\n"
    "s_cbranch_vccnz Llp_182\n"
    "s_nop 3\n"
    "s_branch Lback_32\n"
    "Lagain_33:\n"
    "s_waitcnt vmcnt(0) lgkmcnt(0)\n"
    "s_mov_b32 s85, 0x10000\n"
    "Llp_183:\n"
    "s_xor_b32 s84, s2, 0x2800\n"
    "buffer_load_dwordx4 v[208:211], v20, s[12:15], s84 offen sc1\n"
    "s_xor_b32 s84, s2, 0x2800\n"
    "buffer_load_dwordx4 v[212:215], v21, s[12:15], s84 offen sc1\n"
    "s_waitcnt vmcnt(0)\n"
    "v_mov_b32 v26, v208\n"
    "v_max_u32 v26, v26, v212\n"
    "v_cmp_eq_u32 vcc, -1, v26\n"
    "s_sub_u32 s85, s85, 1\n"
    "s_cselect_b64 vcc, 0, vcc\n"
    "s_cbranch_vccnz Llp_183\n"
    "s_nop 3\n"
    "s_branch Lback_34\n"
    "Lagain_35:\n"
    "s_waitcnt vmcnt(0) lgkmcnt(0)\n"
    "s_mov_b32 s85, 0x10000\n"
    "Llp_184:\n"
    "s_xor_b32 s84, s2, 0x2800\n"
    "buffer_load_dwordx4 v[216:219], v20, s[12:15], s84 offen offset:1024 sc1\n"
    "s_xor_b32 s84, s2, 0x2800\n"
    "buffer_load_dwordx4 v[220:223], v21, s[12:15], s84 offen offset:1024 sc1\n"
    "s_waitcnt vmcnt(0)\n"
    "v_mov_b32 v26, v216\n"
    "v_max_u32 v26, v26, v220\n"
    "v_cmp_eq_u32 vcc, -1, v26\n"
    "s_sub_u32 s85, s85, 1\n"
    "s_cselect_b64 vcc, 0, vcc\n"
    "s_cbranch_vccnz Llp_184\n"
    "s_nop 3\n"
    "s_branch Lback_36\n"
    "Lagain_37:\n"
    "s_waitcnt vmcnt(0) lgkmcnt(0)\n"
    "s_mov_b32 s85, 0x10000\n"
    "Llp_185:\n"
    "s_xor_b32 s84, s2, 0x3000\n"
    "buffer_load_dwordx4 v[224:227], v20, s[12:15], s84 offen sc1\n"
    "s_xor_b32 s84, s2, 0x3000\n"
    "buffer_load_dwordx4 v[228:231], v21, s[12:15], s84 offen sc1\n"
    "s_waitcnt vmcnt(0)\n"
    "v_mov_b32 v26, v224\n"
    "v_max_u32 v26, v26, v228\n"
    "v_cmp_eq_u32 vcc, -1, v26\n"
    "s_sub_u32 s85, s85, 1\n"
    "s_cselect_b64 vcc, 0, vcc\n"
    "s_cbranch_vccnz Llp_185\n"
    "s_nop 3\n"
    "s_branch Lback_38\n"
    "Lagain_39:\n"
    "s_waitcnt vmcnt(0) lgkmcnt(0)\n"
    "s_mov_b32 s85, 0x10000\n"
    "Llp_186:\n"
    "s_xor_b32 s84, s2, 0x3000\n"
    "buffer_load_dwordx4 v[232:235], v20, s[12:15], s84 offen offset:1024 sc1\n"
    "s_xor_b32 s84, s2, 0x3000\n"
    "buffer_load_dwordx4 v[236:239], v21, s[12:15], s84 offen offset:1024 sc1\n"
    "s_waitcnt vmcnt(0)\n"
    "v_mov_b32 v26, v232\n"
    "v_max_u32 v26, v26, v236\n"
    "v_cmp_eq_u32 vcc, -1, v26\n"
    "s_sub_u32 s85, s85, 1\n"
    "s_cselect_b64 vcc, 0, vcc\n"
    "s_cbranch_vccnz Llp_186\n"
    "s_nop 3\n"
    "s_branch Lback_40\n"
    "Lagain_41:\n"
    "s_waitcnt vmcnt(0) lgkmcnt(0)\n"
    "s_mov_b32 s85, 0x10000\n"
    "Llp_187:\n"
    "s_xor_b32 s84, s2, 0x3800\n"
    "buffer_load_dwordx4 v[240:243], v20, s[12:15], s84 offen sc1\n"
    "s_xor_b32 s84, s2, 0x3800\n"
    "buffer_load_dwordx4 v[244:247], v21, s[12:15], s84 offen sc1\n"
    "s_waitcnt vmcnt(0)\n"
    "v_mov_b32 v26, v240\n"
    "v_max_u32 v26, v26, v244\n"
    "v_cmp_eq_u32 vcc, -1, v26\n"
    "s_sub_u32 s85, s85, 1\n"
    "s_cselect_b64 vcc, 0, vcc\n"
    "s_cbranch_vccnz Llp_187\n"
    "s_nop 3\n"
    "s_branch Lback_42\n"
    "Lagain_43:\n"
    "s_waitcnt vmcnt(0) lgkmcnt(0)\n"
    "s_mov_b32 s85, 0x10000\n"
    "Llp_188:\n"
    "s_xor_b32 s84, s2, 0x3800\n"
    "buffer_load_dwordx4 v[248:251], v20, s[12:15], s84 offen offset:1024 sc1\n"
    "s_xor_b32 s84, s2, 0x3800\n"
    "buffer_load_dwordx4 v[252:255], v21, s[12:15], s84 offen offset:1024 sc1\n"
    "s_waitcnt vmcnt(0)\n"
    "v_mov_b32 v26, v248\n"
    "v_max_u32 v26, v26, v252\n"
    "v_cmp_eq_u32 vcc, -1, v26\n"
    "s_sub_u32 s85, s85, 1\n"
    "s_cselect_b64 vcc, 0, vcc\n"
    "s_cbranch_vccnz Llp_188\n"
    "s_nop 3\n"
    "s_branch Lback_44\n"
    "Lagain_45:\n"
    "s_waitcnt vmcnt(0) lgkmcnt(0)\n"
    "s_mov_b32 s85, 0x10000\n"
    "Llp_189:\n"
    "s_xor_b32 s84, s2, 0x10000\n"
    "buffer_load_dwordx4 v[128:131], v20, s[12:15], s84 offen sc1\n"
    "s_xor_b32 s84, s2, 0x10000\n"
    "buffer_load_dwordx4 v[132:135], v21, s[12:15], s84 offen sc1\n"
    "s_waitcnt vmcnt(0)\n"
    "v_mov_b32 v26, v128\n"
    "v_max_u32 v26, v26, v132\n"
    "v_cmp_eq_u32 vcc, -1, v26\n"
    "s_sub_u32 s85, s85, 1\n"
    "s_cselect_b64 vcc, 0, vcc\n"
    "s_cbranch_vccnz Llp_189\n"
    "s_nop 3\n"
    "s_branch Lback_46\n"
    "Lagain_47:\n"
    "s_waitcnt vmcnt(0) lgkmcnt(0)\n"
    "s_mov_b32 s85, 0x10000\n"
    "Llp_190:\n"
    "s_xor_b32 s84, s2, 0x10000\n"
    "buffer_load_dwordx4 v[136:139], v20, s[12:15], s84 offen offset:1024 sc1\n"
    "s_xor_b32 s84, s2, 0x10000\n"
    "buffer_load_dwordx4 v[140:143], v21, s[12:15], s84 offen offset:1024 sc1\n"
    "s_waitcnt vmcnt(0)\n"
    "v_mov_b32 v26, v136\n"
    "v_max_u32 v26, v26, v140\n"
    "v_cmp_eq_u32 vcc, -1, v26\n"
    "s_sub_u32 s85, s85, 1\n"
    "s_cselect_b64 vcc, 0, vcc\n"
    "s_cbranch_vccnz Llp_190\n"
    "s_nop 3\n"
    "s_branch Lback_48\n"
    "Lagain_49:\n"
    "s_waitcnt vmcnt(0) lgkmcnt(0)\n"
    "s_mov_b32 s85, 0x10000\n"
    "Llp_191:\n"
    "s_xor_b32 s84, s2, 0x10800\n"
    "buffer_load_dwordx4 v[144:147], v20, s[12:15], s84 offen sc1\n"
    "s_xor_b32 s84, s2, 0x10800\n"
    "buffer_load_dwordx4 v[148:151], v21, s[12:15], s84 offen sc1\n"
    "s_waitcnt vmcnt(0)\n"
    "v_mov_b32 v26, v144\n"
    "v_max_u32 v26, v26, v148\n"
    "v_cmp_eq_u32 vcc, -1, v26\n"
    "s_sub_u32 s85, s85, 1\n"
    "s_cselect_b64 vcc, 0, vcc\n"
    "s_cbranch_vccnz Llp_191\n"
    "s_nop 3\n"
    "s_branch Lback_50\n"
    "Lagain_51:\n"
    "s_waitcnt vmcnt(0) lgkmcnt(0)\n"
    "s_mov_b32 s85, 0x10000\n"
    "Llp_192:\n"
    "s_xor_b32 s84, s2, 0x10800\n"
    "buffer_load_dwordx4 v[152:155], v20, s[12:15], s84 offen offset:1024 sc1\n"
    "s_xor_b32 s84, s2, 0x10800\n"
    "buffer_load_dwordx4 v[156:159], v21, s[12:15], s84 offen offset:1024 sc1\n"
    "s_waitcnt vmcnt(0)\n"
    "v_mov_b32 v26, v152\n"
    "v_max_u32 v26, v26, v156\n"
    "v_cmp_eq_u32 vcc, -1, v26\n"
    "s_sub_u32 s85, s85, 1\n"
    "s_cselect_b64 vcc, 0, vcc\n"
    "s_cbranch_vccnz Llp_192\n"
    "s_nop 3\n"
    "s_branch Lback_52\n"
    "Lagain_53:\n"
    "s_waitcnt vmcnt(0) lgkmcnt(0)\n"
    "s_mov_b32 s85, 0x10000\n"
    "Llp_193:\n"
    "s_xor_b32 s84, s2, 0x11000\n"
    "buffer_load_dwordx4 v[160:163], v20, s[12:15], s84 offen sc1\n"
    "s_xor_b32 s84, s2, 0x11000\n"
    "buffer_load_dwordx4 v[164:167], v21, s[12:15], s84 offen sc1\n"
    "s_waitcnt vmcnt(0)\n"
    "v_mov_b32 v26, v160\n"
    "v_max_u32 v26, v26, v164\n"
    "v_cmp_eq_u32 vcc, -1, v26\n"
    "s_sub_u32 s85, s85, 1\n"
    "s_cselect_b64 vcc, 0, vcc\n"
    "s_cbranch_vccnz Llp_193\n"
    "s_nop 3\n"
    "s_branch Lback_54\n"
    "Lagain_55:\n"
    "s_waitcnt vmcnt(0) lgkmcnt(0)\n"
    "s_mov_b32 s85, 0x10000\n"
    "Llp_194:\n"
    "s_xor_b32 s84, s2, 0x11000\n"
    "buffer_load_dwordx4 v[168:171], v20, s[12:15], s84 offen offset:1024 sc1\n"
    "s_xor_b32 s84, s2, 0x11000\n"
    "buffer_load_dwordx4 v[172:175], v21, s[12:15], s84 offen offset:1024 sc1\n"
    "s_waitcnt vmcnt(0)\n"
    "v_mov_b32 v26, v168\n"
    "v_max_u32 v26, v26, v172\n"
    "v_cmp_eq_u32 vcc, -1, v26\n"
    "s_sub_u32 s85, s85, 1\n"
    "s_cselect_b64 vcc, 0, vcc\n"
    "s_cbranch_vccnz Llp_194\n"
    "s_nop 3\n"
    "s_branch Lback_56\n"
    "Lagain_57:\n"
    "s_waitcnt vmcnt(0) lgkmcnt(0)\n"
    "s_mov_b32 s85, 0x10000\n"
    "Llp_195:\n"
    "s_xor_b32 s84, s2, 0x11800\n"
    "buffer_load_dwordx4 v[176:179], v20, s[12:15], s84 offen sc1\n"
    "s_xor_b32 s84, s2, 0x11800\n"
    "buffer_load_dwordx4 v[180:183], v21, s[12:15], s84 offen sc1\n"
    "s_waitcnt vmcnt(0)\n"
    "v_mov_b32 v26, v176\n"
    "v_max_u32 v26, v26, v180\n"
    "v_cmp_eq_u32 vcc, -1, v26\n"
    "s_sub_u32 s85, s85, 1\n"
    "s_cselect_b64 vcc, 0, vcc\n"
    "s_cbranch_vccnz Llp_195\n"
    "s_nop 3\n"
    "s_branch Lback_58\n"
    "Lagain_59:\n"
    "s_waitcnt vmcnt(0) lgkmcnt(0)\n"
    "s_mov_b32 s85, 0x10000\n"
    "Llp_196:\n"
    "s_xor_b32 s84, s2, 0x11800\n"
    "buffer_load_dwordx4 v[184:187], v20, s[12:15], s84 offen offset:1024 sc1\n"
    "s_xor_b32 s84, s2, 0x11800\n"
    "buffer_load_dwordx4 v[188:191], v21, s[12:15], s84 offen offset:1024 sc1\n"
    "s_waitcnt vmcnt(0)\n"
    "v_mov_b32 v26, v184\n"
    "v_max_u32 v26, v26, v188\n"
    "v_cmp_eq_u32 vcc, -1, v26\n"
    "s_sub_u32 s85, s85, 1\n"
    "s_cselect_b64 vcc, 0, vcc\n"
    "s_cbranch_vccnz Llp_196\n"
    "s_nop 3\n"
    "s_branch Lback_60\n"
    "Lagain_61:\n"
    "s_waitcnt vmcnt(0) lgkmcnt(0)\n"
    "s_mov_b32 s85, 0x10000\n"
    "Llp_197:\n"
    "s_xor_b32 s84, s2, 0x12000\n"
    "buffer_load_dwordx4 v[192:195], v20, s[12:15], s84 offen sc1\n"
    "s_xor_b32 s84, s2, 0x12000\n"
    "buffer_load_dwordx4 v[196:199], v21, s[12:15], s84 offen sc1\n"
    "s_waitcnt vmcnt(0)\n"
    "v_mov_b32 v26, v192\n"
    "v_max_u32 v26, v26, v196\n"
    "v_cmp_eq_u32 vcc, -1, v26\n"
    "s_sub_u32 s85, s85, 1\n"
    "s_cselect_b64 vcc, 0, vcc\n"
    "s_cbranch_vccnz Llp_197\n"
    "s_nop 3\n"
    "s_branch Lback_62\n"
    "Lagain_63:\n"
    "s_waitcnt vmcnt(0) lgkmcnt(0)\n"
    "s_mov_b32 s85, 0x10000\n"
    "Llp_198:\n"
    "s_xor_b32 s84, s2, 0x12000\n"
    "buffer_load_dwordx4 v[200:203], v20, s[12:15], s84 offen offset:1024 sc1\n"
    "s_xor_b32 s84, s2, 0x12000\n"
    "buffer_load_dwordx4 v[204:207], v21, s[12:15], s84 offen offset:1024 sc1\n"
    "s_waitcnt vmcnt(0)\n"
    "v_mov_b32 v26, v200\n"
    "v_max_u32 v26, v26, v204\n"
    "v_cmp_eq_u32 vcc, -1, v26\n"
    "s_sub_u32 s85, s85, 1\n"
    "s_cselect_b64 vcc, 0, vcc\n"
    "s_cbranch_vccnz Llp_198\n"
    "s_nop 3\n"
    "s_branch Lback_64\n"
    "Lagain_65:\n"
    "s_waitcnt vmcnt(0) lgkmcnt(0)\n"
    "s_mov_b32 s85, 0x10000\n"
    "Llp_199:\n"
    "s_xor_b32 s84, s2, 0x12800\n"
    "buffer_load_dwordx4 v[212:215], v21, s[12:15], s84 offen sc1\n"
    "s_waitcnt vmcnt(0)\n"
    "v_mov_b32 v26, v212\n"
    "v_cmp_eq_u32 vcc, -1, v26\n"
    "s_sub_u32 s85, s85, 1\n"
    "s_cselect_b64 vcc, 0, vcc\n"
    "s_cbranch_vccnz Llp_199\n"
    "s_nop 3\n"
    "s_branch Lback_66\n"
    "Lagain_67:\n"
    "s_waitcnt vmcnt(0) lgkmcnt(0)\n"
    "s_mov_b32 s85, 0x10000\n"
    "Llp_200:\n"
    "s_xor_b32 s84, s2, 0x12800\n"
    "buffer_load_dwordx4 v[220:223], v21, s[12:15], s84 offen offset:1024 sc1\n"
    "s_waitcnt vmcnt(0)\n"
    "v_mov_b32 v26, v220\n"
    "v_cmp_eq_u32 vcc, -1, v26\n"
    "s_sub_u32 s85, s85, 1\n"
    "s_cselect_b64 vcc, 0, vcc\n"
    "s_cbranch_vccnz Llp_200\n"
    "s_nop 3\n"
    "s_branch Lback_68\n"
    "Lagain_69:\n"
    "s_waitcnt vmcnt(0) lgkmcnt(0)\n"
    "s_mov_b32 s85, 0x10000\n"
    "Llp_201:\n"
    "s_xor_b32 s84, s2, 0x13000\n"
    "buffer_load_dwordx4 v[228:231], v21, s[12:15], s84 offen sc1\n"
    "s_waitcnt vmcnt(0)\n"
    "v_mov_b32 v26, v228\n"
    "v_cmp_eq_u32 vcc, -1, v26\n"
    "s_sub_u32 s85, s85, 1\n"
    "s_cselect_b64 vcc, 0, vcc\n"
    "s_cbranch_vccnz Llp_201\n"
    "s_nop 3\n"
    "s_branch Lback_70\n"
    "Lagain_71:\n"
    "s_waitcnt vmcnt(0) lgkmcnt(0)\n"
    "s_mov_b32 s85, 0x10000\n"
    "Llp_202:\n"
    "s_xor_b32 s84, s2, 0x13000\n"
    "buffer_load_dwordx4 v[236:239], v21, s[12:15], s84 offen offset:1024 sc1\n"
    "s_waitcnt vmcnt(0)\n"
    "v_mov_b32 v26, v236\n"
    "v_cmp_eq_u32 vcc, -1, v26\n"
    "s_sub_u32 s85, s85, 1\n"
    "s_cselect_b64 vcc, 0, vcc\n"
    "s_cbranch_vccnz Llp_202\n"
    "s_nop 3\n"
    "s_branch Lback_72\n"
    "Lagain_73:\n"
    "s_waitcnt vmcnt(0) lgkmcnt(0)\n"
    "s_mov_b32 s85, 0x10000\n"
    "Llp_203:\n"
    "s_xor_b32 s84, s2, 0x13800\n"
    "buffer_load_dwordx4 v[244:247], v21, s[12:15], s84 offen sc1\n"
    "s_waitcnt vmcnt(0)\n"
    "v_mov_b32 v26, v244\n"
    "v_cmp_eq_u32 vcc, -1, v26\n"
    "s_sub_u32 s85, s85, 1\n"
    "s_cselect_b64 vcc, 0, vcc\n"
    "s_cbranch_vccnz Llp_203\n"
    "s_nop 3\n"
    "s_branch Lback_74\n"
    "Lagain_75:\n"
    "s_waitcnt vmcnt(0) lgkmcnt(0)\n"
    "s_mov_b32 s85, 0x10000\n"
    "Llp_204:\n"
    "s_xor_b32 s84, s2, 0x13800\n"
    "buffer_load_dwordx4 v[252:255], v21, s[12:15], s84 offen offset:1024 sc1\n"
    "s_waitcnt vmcnt(0)\n"
    "v_mov_b32 v26, v252\n"
    "v_cmp_eq_u32 vcc, -1, v26\n"
    "s_sub_u32 s85, s85, 1\n"
    "s_cselect_b64 vcc, 0, vcc\n"
    "s_cbranch_vccnz Llp_204\n"
    "s_nop 3\n"
    "s_branch Lback_76\n"
    "Lagain_77:\n"
    "s_waitcnt vmcnt(0) lgkmcnt(0)\n"
    "s_mov_b32 s85, 0x10000\n"
    "Llp_205:\n"
    "s_xor_b32 s84, s2, 0x12800\n"
    "buffer_load_dwordx4 v[208:211], v20, s[12:15], s84 offen sc1\n"
    "s_waitcnt vmcnt(0)\n"
    "v_mov_b32 v26, v208\n"
    "v_cmp_eq_u32 vcc, -1, v26\n"
    "s_sub_u32 s85, s85, 1\n"
    "s_cselect_b64 vcc, 0, vcc\n"
    "s_cbranch_vccnz Llp_205\n"
    "s_nop 3\n"
    "s_branch Lback_78\n"
    "Lagain_79:\n"
    "s_waitcnt vmcnt(0) lgkmcnt(0)\n"
    "s_mov_b32 s85, 0x10000\n"
    "Llp_206:\n"
    "s_xor_b32 s84, s2, 0x12800\n"
    "buffer_load_dwordx4 v[216:219], v20, s[12:15], s84 offen offset:1024 sc1\n"
    "s_waitcnt vmcnt(0)\n"
    "v_mov_b32 v26, v216\n"
    "v_cmp_eq_u32 vcc, -1, v26\n"
    "s_sub_u32 s85, s85, 1\n"
    "s_cselect_b64 vcc, 0, vcc\n"
    "s_cbranch_vccnz Llp_206\n"
    "s_nop 3\n"
    "s_branch Lback_80\n"
    "Lagain_81:\n"
    "s_waitcnt vmcnt(0) lgkmcnt(0)\n"
    "s_mov_b32 s85, 0x10000\n"
    "Llp_207:\n"
    "s_xor_b32 s84, s2, 0x13000\n"
    "buffer_load_dwordx4 v[224:227], v20, s[12:15], s84 offen sc1\n"
    "s_waitcnt vmcnt(0)\n"
    "v_mov_b32 v26, v224\n"
    "v_cmp_eq_u32 vcc, -1, v26\n"
    "s_sub_u32 s85, s85, 1\n"
    "s_cselect_b64 vcc, 0, vcc\n"
    "s_cbranch_vccnz Llp_207\n"
    "s_nop 3\n"
    "s_branch Lback_82\n"
    "Lagain_83:\n"
    "s_waitcnt vmcnt(0) lgkmcnt(0)\n"
    "s_mov_b32 s85, 0x10000\n"
    "Llp_208:\n"
    "s_xor_b32 s84, s2, 0x13000\n"
    "buffer_load_dwordx4 v[232:235], v20, s[12:15], s84 offen offset:1024 sc1\n"
    "s_waitcnt vmcnt(0)\n"
    "v_mov_b32 v26, v232\n"
    "v_cmp_eq_u32 vcc, -1, v26\n"
    "s_sub_u32 s85, s85, 1\n"
    "s_cselect_b64 vcc, 0, vcc\n"
    "s_cbranch_vccnz Llp_208\n"
    "s_nop 3\n"
    "s_branch Lback_84\n"
    "Lagain_85:\n"
    "s_waitcnt vmcnt(0) lgkmcnt(0)\n"
    "s_mov_b32 s85, 0x10000\n"
    "Llp_209:\n"
    "s_xor_b32 s84, s2, 0x13800\n"
    "buffer_load_dwordx4 v[240:243], v20, s[12:15], s84 offen sc1\n"
    "s_waitcnt vmcnt(0)\n"
    "v_mov_b32 v26, v240\n"
    "v_cmp_eq_u32 vcc, -1, v26\n"
    "s_sub_u32 s85, s85, 1\n"
    "s_cselect_b64 vcc, 0, vcc\n"
    "s_cbranch_vccnz Llp_209\n"
    "s_nop 3\n"
    "s_branch Lback_86\n"
    "Lagain_87:\n"
    "s_waitcnt vmcnt(0) lgkmcnt(0)\n"
    "s_mov_b32 s85, 0x10000\n"
    "Llp_210:\n"
    "s_xor_b32 s84, s2, 0x13800\n"
    "buffer_load_dwordx4 v[248:251], v20, s[12:15], s84 offen offset:1024 sc1\n"
    "s_waitcnt vmcnt(0)\n"
    "v_mov_b32 v26, v248\n"
    "v_cmp_eq_u32 vcc, -1, v26\n"
    "s_sub_u32 s85, s85, 1\n"
    "s_cselect_b64 vcc, 0, vcc\n"
    "s_cbranch_vccnz Llp_210\n"
    "s_nop 3\n"
    "s_branch Lback_88\n"
    "Lagain_92:\n"
    "s_waitcnt vmcnt(0) lgkmcnt(0)\n"
    "s_mov_b32 s85, 0x10000\n"
    "Llp_211:\n"
    "buffer_load_dwordx4 v[112:115], v1, s[12:15], s61 offen sc1\n"
    "buffer_load_dwordx4 v[116:119], v1, s[12:15], s61 offen offset:1024 sc1\n"
    "s_waitcnt vmcnt(0)\n"
    "v_mov_b32 v26, v112\n"
    "v_max_u32 v26, v26, v116\n"
    "v_cmp_eq_u32 vcc, -1, v26\n"
    "s_sub_u32 s85, s85, 1\n"
    "s_cselect_b64 vcc, 0, vcc\n"
    "s_cbranch_vccnz Llp_211\n"
    "s_nop 3\n"
    "s_branch Lback_93\n"
    "Lagain_94:\n"
    "s_waitcnt vmcnt(0) lgkmcnt(0)\n"
    "s_mov_b32 s85, 0x10000\n"
    "Llp_212:\n"
    "s_mov_b32 s84, 0\n"
    "buffer_load_dwordx4 v[128:131], v20, s[12:15], s84 offen sc1\n"
    "s_mov_b32 s84, 0\n"
    "buffer_load_dwordx4 v[132:135], v21, s[12:15], s84 offen sc1\n"
    "s_waitcnt vmcnt(0)\n"
    "v_mov_b32 v26, v128\n"
    "v_max_u32 v26, v26, v132\n"
    "v_cmp_eq_u32 vcc, -1, v26\n"
    "s_sub_u32 s85, s85, 1\n"
    "s_cselect_b64 vcc, 0, vcc\n"
    "s_cbranch_vccnz Llp_212\n"
    "s_nop 3\n"
    "s_branch Lback_95\n"
    "Lagain_96:\n"
    "s_waitcnt vmcnt(0) lgkmcnt(0)\n"
    "s_mov_b32 s85, 0x10000\n"
    "Llp_213:\n"
    "s_mov_b32 s84, 0\n"
    "buffer_load_dwordx4 v[136:139], v20, s[12:15], s84 offen offset:1024 sc1\n"
    "s_mov_b32 s84, 0\n"
    "buffer_load_dwordx4 v[140:143], v21, s[12:15], s84 offen offset:1024 sc1\n"
    "s_waitcnt vmcnt(0)\n"
    "v_mov_b32 v26, v136\n"
    "v_max_u32 v26, v26, v140\n"
    "v_cmp_eq_u32 vcc, -1, v26\n"
    "s_sub_u32 s85, s85, 1\n"
    "s_cselect_b64 vcc, 0, vcc\n"
    "s_cbranch_vccnz Llp_213\n"
    "s_nop 3\n"
    "s_branch Lback_97\n"
    "Lagain_98:\n"
    "s_waitcnt vmcnt(0) lgkmcnt(0)\n"
    "s_mov_b32 s85, 0x10000\n"
    "Llp_214:\n"
    "s_mov_b32 s84, 0\n"
    "buffer_load_dwordx4 v[144:147], v20, s[12:15], s84 offen offset:2048 sc1\n"
    "s_mov_b32 s84, 0\n"
    "buffer_load_dwordx4 v[148:151], v21, s[12:15], s84 offen offset:2048 sc1\n"
    "s_waitcnt vmcnt(0)\n"
    "v_mov_b32 v26, v144\n"
    "v_max_u32 v26, v26, v148\n"
    "v_cmp_eq_u32 vcc, -1, v26\n"
    "s_sub_u32 s85, s85, 1\n"
    "s_cselect_b64 vcc, 0, vcc\n"
    "s_cbranch_vccnz Llp_214\n"
    "s_nop 3\n"
    "s_branch Lback_99\n"
    "Lagain_100:\n"
    "s_waitcnt vmcnt(0) lgkmcnt(0)\n"
    "s_mov_b32 s85, 0x10000\n"
    "Llp_215:\n"
    "s_mov_b32 s84, 0\n"
    "buffer_load_dwordx4 v[152:155], v20, s[12:15], s84 offen offset:3072 sc1\n"
    "s_mov_b32 s84, 0\n"
    "buffer_load_dwordx4 v[156:159], v21, s[12:15], s84 offen offset:3072 sc1\n"
    "s_waitcnt vmcnt(0)\n"
    "v_mov_b32 v26, v152\n"
    "v_max_u32 v26, v26, v156\n"
    "v_cmp_eq_u32 vcc, -1, v26\n"
    "s_sub_u32 s85, s85, 1\n"
    "s_cselect_b64 vcc, 0, vcc\n"
    "s_cbranch_vccnz Llp_215\n"
    "s_nop 3\n"
    "s_branch Lback_101\n"
    "Lagain_102:\n"
    "s_waitcnt vmcnt(0) lgkmcnt(0)\n"
    "s_mov_b32 s85, 0x10000\n"
    "Llp_216:\n"
    "s_mov_b32 s84, 0x1000\n"
    "buffer_load_dwordx4 v[160:163], v20, s[12:15], s84 offen sc1\n"
    "s_mov_b32 s84, 0x1000\n"
    "buffer_load_dwordx4 v[164:167], v21, s[12:15], s84 offen sc1\n"
    "s_waitcnt vmcnt(0)\n"
    "v_mov_b32 v26, v160\n"
    "v_max_u32 v26, v26, v164\n"
    "v_cmp_eq_u32 vcc, -1, v26\n"
    "s_sub_u32 s85, s85, 1\n"
    "s_cselect_b64 vcc, 0, vcc\n"
    "s_cbranch_vccnz Llp_216\n"
    "s_nop 3\n"
    "s_branch Lback_103\n"
    "Lagain_104:\n"
    "s_waitcnt vmcnt(0) lgkmcnt(0)\n"
    "s_mov_b32 s85, 0x10000\n"
    "Llp_217:\n"
    "s_mov_b32 s84, 0x1000\n"
    "buffer_load_dwordx4 v[168:171], v20, s[12:15], s84 offen offset:1024 sc1\n"
    "s_mov_b32 s84, 0x1000\n"
    "buffer_load_dwordx4 v[172:175], v21, s[12:15], s84 offen offset:1024 sc1\n"
    "s_waitcnt vmcnt(0)\n"
    "v_mov_b32 v26, v168\n"
    "v_max_u32 v26, v26, v172\n"
    "v_cmp_eq_u32 vcc, -1, v26\n"
    "s_sub_u32 s85, s85, 1\n"
    "s_cselect_b64 vcc, 0, vcc\n"
    "s_cbranch_vccnz Llp_217\n"
    "s_nop 3\n"
    "s_branch Lback_105\n"
    "Lagain_106:\n"
    "s_waitcnt vmcnt(0) lgkmcnt(0)\n"
    "s_mov_b32 s85, 0x10000\n"
    "Llp_218:\n"
    "s_mov_b32 s84, 0x1000\n"
    "buffer_load_dwordx4 v[176:179], v20, s[12:15], s84 offen offset:2048 sc1\n"
    "s_mov_b32 s84, 0x1000\n"
    "buffer_load_dwordx4 v[180:183], v21, s[12:15], s84 offen offset:2048 sc1\n"
    "s_waitcnt vmcnt(0)\n"
    "v_mov_b32 v26, v176\n"
    "v_max_u32 v26, v26, v180\n"
    "v_cmp_eq_u32 vcc, -1, v26\n"
    "s_sub_u32 s85, s85, 1\n"
    "s_cselect_b64 vcc, 0, vcc\n"
    "s_cbranch_vccnz Llp_218\n"
    "s_nop 3\n"
    "s_branch Lback_107\n"
    "Lagain_108:\n"
    "s_waitcnt vmcnt(0) lgkmcnt(0)\n"
    "s_mov_b32 s85, 0x10000\n"
    "Llp_219:\n"
    "s_mov_b32 s84, 0x1000\n"
    "buffer_load_dwordx4 v[184:187], v20, s[12:15], s84 offen offset:3072 sc1\n"
    "s_mov_b32 s84, 0x1000\n"
    "buffer_load_dwordx4 v[188:191], v21, s[12:15], s84 offen offset:3072 sc1\n"
    "s_waitcnt vmcnt(0)\n"
    "v_mov_b32 v26, v184\n"
    "v_max_u32 v26, v26, v188\n"
    "v_cmp_eq_u32 vcc, -1, v26\n"
    "s_sub_u32 s85, s85, 1\n"
    "s_cselect_b64 vcc, 0, vcc\n"
    "s_cbranch_vccnz Llp_219\n"
    "s_nop 3\n"
    "s_branch Lback_109\n"
    "Lagain_110:\n"
    "s_waitcnt vmcnt(0) lgkmcnt(0)\n"
    "s_mov_b32 s85, 0x10000\n"
    "Llp_220:\n"
    "s_mov_b32 s84, 0x2000\n"
    "buffer_load_dwordx4 v[192:195], v20, s[12:15], s84 offen sc1\n"
    "s_mov_b32 s84, 0x2000\n"
    "buffer_load_dwordx4 v[196:199], v21, s[12:15], s84 offen sc1\n"
    "s_waitcnt vmcnt(0)\n"
    "v_mov_b32 v26, v192\n"
    "v_max_u32 v26, v26, v196\n"
    "v_cmp_eq_u32 vcc, -1, v26\n"
    "s_sub_u32 s85, s85, 1\n"
    "s_cselect_b64 vcc, 0, vcc\n"
    "s_cbranch_vccnz Llp_220\n"
    "s_nop 3\n"
    "s_branch Lback_111\n"
    "Lagain_112:\n"
    "s_waitcnt vmcnt(0) lgkmcnt(0)\n"
    "s_mov_b32 s85, 0x10000\n"
    "Llp_221:\n"
    "s_mov_b32 s84, 0x2000\n"
    "buffer_load_dwordx4 v[200:203], v20, s[12:15], s84 offen offset:1024 sc1\n"
    "s_mov_b32 s84, 0x2000\n"
    "buffer_load_dwordx4 v[204:207], v21, s[12:15], s84 offen offset:1024 sc1\n"
    "s_waitcnt vmcnt(0)\n"
    "v_mov_b32 v26, v200\n"
    "v_max_u32 v26, v26, v204\n"
    "v_cmp_eq_u32 vcc, -1, v26\n"
    "s_sub_u32 s85, s85, 1\n"
    "s_cselect_b64 vcc, 0, vcc\n"
    "s_cbranch_vccnz Llp_221\n"
    "s_nop 3\n"
    "s_branch Lback_113\n"
    "Lagain_114:\n"
    "s_waitcnt vmcnt(0) lgkmcnt(0)\n"
    "s_mov_b32 s85, 0x10000\n"
    "Llp_222:\n"
    "s_mov_b32 s84, 0x2000\n"
    "buffer_load_dwordx4 v[208:211], v20, s[12:15], s84 offen offset:2048 sc1\n"
    "s_mov_b32 s84, 0x2000\n"
    "buffer_load_dwordx4 v[212:215], v21, s[12:15], s84 offen offset:2048 sc1\n"
    "s_waitcnt vmcnt(0)\n"
    "v_mov_b32 v26, v208\n"
    "v_max_u32 v26, v26, v212\n"
    "v_cmp_eq_u32 vcc, -1, v26\n"
    "s_sub_u32 s85, s85, 1\n"
    "s_cselect_b64 vcc, 0, vcc\n"
    "s_cbranch_vccnz Llp_222\n"
    "s_nop 3\n"
    "s_branch Lback_115\n"
    "Lagain_116:\n"
    "s_waitcnt vmcnt(0) lgkmcnt(0)\n"
    "s_mov_b32 s85, 0x10000\n"
    "Llp_223:\n"
    "s_mov_b32 s84, 0x2000\n"
    "buffer_load_dwordx4 v[216:219], v20, s[12:15], s84 offen offset:3072 sc1\n"
    "s_mov_b32 s84, 0x2000\n"
    "buffer_load_dwordx4 v[220:223], v21, s[12:15], s84 offen offset:3072 sc1\n"
    "s_waitcnt vmcnt(0)\n"
    "v_mov_b32 v26, v216\n"
    "v_max_u32 v26, v26, v220\n"
    "v_cmp_eq_u32 vcc, -1, v26\n"
    "s_sub_u32 s85, s85, 1\n"
    "s_cselect_b64 vcc, 0, vcc\n"
    "s_cbranch_vccnz Llp_223\n"
    "s_nop 3\n"
    "s_branch Lback_117\n"
    "Lagain_118:\n"
    "s_waitcnt vmcnt(0) lgkmcnt(0)\n"
    "s_mov_b32 s85, 0x10000\n"
    "Llp_224:\n"
    "s_mov_b32 s84, 0x3000\n"
    "buffer_load_dwordx4 v[224:227], v20, s[12:15], s84 offen sc1\n"
    "s_mov_b32 s84, 0x3000\n"
    "buffer_load_dwordx4 v[228:231], v21, s[12:15], s84 offen sc1\n"
    "s_waitcnt vmcnt(0)\n"
    "v_mov_b32 v26, v224\n"
    "v_max_u32 v26, v26, v228\n"
    "v_cmp_eq_u32 vcc, -1, v26\n"
    "s_sub_u32 s85, s85, 1\n"
    "s_cselect_b64 vcc, 0, vcc\n"
    "s_cbranch_vccnz Llp_224\n"
    "s_nop 3\n"
    "s_branch Lback_119\n"
    "Lagain_120:\n"
    "s_waitcnt vmcnt(0) lgkmcnt(0)\n"
    "s_mov_b32 s85, 0x10000\n"
    "Llp_225:\n"
    "s_mov_b32 s84, 0x3000\n"
    "buffer_load_dwordx4 v[232:235], v20, s[12:15], s84 offen offset:1024 sc1\n"
    "s_mov_b32 s84, 0x3000\n"
    "buffer_load_dwordx4 v[236:239], v21, s[12:15], s84 offen offset:1024 sc1\n"
    "s_waitcnt vmcnt(0)\n"
    "v_mov_b32 v26, v232\n"
    "v_max_u32 v26, v26, v236\n"
    "v_cmp_eq_u32 vcc, -1, v26\n"
    "s_sub_u32 s85, s85, 1\n"
    "s_cselect_b64 vcc, 0, vcc\n"
    "s_cbranch_vccnz Llp_225\n"
    "s_nop 3\n"
    "s_branch Lback_121\n"
    "Lagain_122:\n"
    "s_waitcnt vmcnt(0) lgkmcnt(0)\n"
    "s_mov_b32 s85, 0x10000\n"
    "Llp_226:\n"
    "s_mov_b32 s84, 0x3000\n"
    "buffer_load_dwordx4 v[240:243], v20, s[12:15], s84 offen offset:2048 sc1\n"
    "s_mov_b32 s84, 0x3000\n"
    "buffer_load_dwordx4 v[244:247], v21, s[12:15], s84 offen offset:2048 sc1\n"
    "s_waitcnt vmcnt(0)\n"
    "v_mov_b32 v26, v240\n"
    "v_max_u32 v26, v26, v244\n"
    "v_cmp_eq_u32 vcc, -1, v26\n"
    "s_sub_u32 s85, s85, 1\n"
    "s_cselect_b64 vcc, 0, vcc\n"
    "s_cbranch_vccnz Llp_226\n"
    "s_nop 3\n"
    "s_branch Lback_123\n"
    "Lagain_124:\n"
    "s_waitcnt vmcnt(0) lgkmcnt(0)\n"
    "s_mov_b32 s85, 0x10000\n"
    "Llp_227:\n"
    "s_mov_b32 s84, 0x3000\n"
    "buffer_load_dwordx4 v[248:251], v20, s[12:15], s84 offen offset:3072 sc1\n"
    "s_mov_b32 s84, 0x3000\n"
    "buffer_load_dwordx4 v[252:255], v21, s[12:15], s84 offen offset:3072 sc1\n"
    "s_waitcnt vmcnt(0)\n"
    "v_mov_b32 v26, v248\n"
    "v_max_u32 v26, v26, v252\n"
    "v_cmp_eq_u32 vcc, -1, v26\n"
    "s_sub_u32 s85, s85, 1\n"
    "s_cselect_b64 vcc, 0, vcc\n"
    "s_cbranch_vccnz Llp_227\n"
    "s_nop 3\n"
    "s_branch Lback_125\n"
    "Lagain_126:\n"
    "s_waitcnt vmcnt(0) lgkmcnt(0)\n"
    "s_mov_b32 s85, 0x10000\n"
    "Llp_228:\n"
    "s_mov_b32 s84, 0x4000\n"
    "buffer_load_dwordx4 v[128:131], v20, s[12:15], s84 offen sc1\n"
    "s_mov_b32 s84, 0x4000\n"
    "buffer_load_dwordx4 v[132:135], v21, s[12:15], s84 offen sc1\n"
    "s_waitcnt vmcnt(0)\n"
    "v_mov_b32 v26, v128\n"
    "v_max_u32 v26, v26, v132\n"
    "v_cmp_eq_u32 vcc, -1, v26\n"
    "s_sub_u32 s85, s85, 1\n"
    "s_cselect_b64 vcc, 0, vcc\n"
    "s_cbranch_vccnz Llp_228\n"
    "s_nop 3\n"
    "s_branch Lback_127\n"
    "Lagain_128:\n"
    "s_waitcnt vmcnt(0) lgkmcnt(0)\n"
    "s_mov_b32 s85, 0x10000\n"
    "Llp_229:\n"
    "s_mov_b32 s84, 0x4000\n"
    "buffer_load_dwordx4 v[136:139], v20, s[12:15], s84 offen offset:1024 sc1\n"
    "s_mov_b32 s84, 0x4000\n"
    "buffer_load_dwordx4 v[140:143], v21, s[12:15], s84 offen offset:1024 sc1\n"
    "s_waitcnt vmcnt(0)\n"
    "v_mov_b32 v26, v136\n"
    "v_max_u32 v26, v26, v140\n"
    "v_cmp_eq_u32 vcc, -1, v26\n"
    "s_sub_u32 s85, s85, 1\n"
    "s_cselect_b64 vcc, 0, vcc\n"
    "s_cbranch_vccnz Llp_229\n"
    "s_nop 3\n"
    "s_branch Lback_129\n"
    "Lagain_130:\n"
    "s_waitcnt vmcnt(0) lgkmcnt(0)\n"
    "s_mov_b32 s85, 0x10000\n"
    "Llp_230:\n"
    "s_mov_b32 s84, 0x4000\n"
    "buffer_load_dwordx4 v[144:147], v20, s[12:15], s84 offen offset:2048 sc1\n"
    "s_mov_b32 s84, 0x4000\n"
    "buffer_load_dwordx4 v[148:151], v21, s[12:15], s84 offen offset:2048 sc1\n"
    "s_waitcnt vmcnt(0)\n"
    "v_mov_b32 v26, v144\n"
    "v_max_u32 v26, v26, v148\n"
    "v_cmp_eq_u32 vcc, -1, v26\n"
    "s_sub_u32 s85, s85, 1\n"
    "s_cselect_b64 vcc, 0, vcc\n"
    "s_cbranch_vccnz Llp_230\n"
    "s_nop 3\n"
    "s_branch Lback_131\n"
    "Lagain_132:\n"
    "s_waitcnt vmcnt(0) lgkmcnt(0)\n"
    "s_mov_b32 s85, 0x10000\n"
    "Llp_231:\n"
    "s_mov_b32 s84, 0x4000\n"
    "buffer_load_dwordx4 v[152:155], v20, s[12:15], s84 offen offset:3072 sc1\n"
    "s_mov_b32 s84, 0x4000\n"
    "buffer_load_dwordx4 v[156:159], v21, s[12:15], s84 offen offset:3072 sc1\n"
    "s_waitcnt vmcnt(0)\n"
    "v_mov_b32 v26, v152\n"
    "v_max_u32 v26, v26, v156\n"
    "v_cmp_eq_u32 vcc, -1, v26\n"
    "s_sub_u32 s85, s85, 1\n"
    "s_cselect_b64 vcc, 0, vcc\n"
    "s_cbranch_vccnz Llp_231\n"
    "s_nop 3\n"
    "s_branch Lback_133\n"
    "Lagain_134:\n"
    "s_waitcnt vmcnt(0) lgkmcnt(0)\n"
    "s_mov_b32 s85, 0x10000\n"
    "Llp_232:\n"
    "s_mov_b32 s84, 0x5000\n"
    "buffer_load_dwordx4 v[160:163], v20, s[12:15], s84 offen sc1\n"
    "s_mov_b32 s84, 0x5000\n"
    "buffer_load_dwordx4 v[164:167], v21, s[12:15], s84 offen sc1\n"
    "s_waitcnt vmcnt(0)\n"
    "v_mov_b32 v26, v160\n"
    "v_max_u32 v26, v26, v164\n"
    "v_cmp_eq_u32 vcc, -1, v26\n"
    "s_sub_u32 s85, s85, 1\n"
    "s_cselect_b64 vcc, 0, vcc\n"
    "s_cbranch_vccnz Llp_232\n"
    "s_nop 3\n"
    "s_branch Lback_135\n"
    "Lagain_136:\n"
    "s_waitcnt vmcnt(0) lgkmcnt(0)\n"
    "s_mov_b32 s85, 0x10000\n"
    "Llp_233:\n"
    "s_mov_b32 s84, 0x5000\n"
    "buffer_load_dwordx4 v[168:171], v20, s[12:15], s84 offen offset:1024 sc1\n"
    "s_mov_b32 s84, 0x5000\n"
    "buffer_load_dwordx4 v[172:175], v21, s[12:15], s84 offen offset:1024 sc1\n"
    "s_waitcnt vmcnt(0)\n"
    "v_mov_b32 v26, v168\n"
    "v_max_u32 v26, v26, v172\n"
    "v_cmp_eq_u32 vcc, -1, v26\n"
    "s_sub_u32 s85, s85, 1\n"
    "s_cselect_b64 vcc, 0, vcc\n"
    "s_cbranch_vccnz Llp_233\n"
    "s_nop 3\n"
    "s_branch Lback_137\n"
    "Lagain_138:\n"
    "s_waitcnt vmcnt(0) lgkmcnt(0)\n"
    "s_mov_b32 s85, 0x10000\n"
    "Llp_234:\n"
    "s_mov_b32 s84, 0x5000\n"
    "buffer_load_dwordx4 v[180:183], v21, s[12:15], s84 offen offset:2048 sc1\n"
    "s_waitcnt vmcnt(0)\n"
    "v_mov_b32 v26, v180\n"
    "v_cmp_eq_u32 vcc, -1, v26\n"
    "s_sub_u32 s85, s85, 1\n"
    "s_cselect_b64 vcc, 0, vcc\n"
    "s_cbranch_vccnz Llp_234\n"
    "s_nop 3\n"
    "s_branch Lback_139\n"
    "Lagain_140:\n"
    "s_waitcnt vmcnt(0) lgkmcnt(0)\n"
    "s_mov_b32 s85, 0x10000\n"
    "Llp_235:\n"
    "s_mov_b32 s84, 0x5000\n"
    "buffer_load_dwordx4 v[188:191], v21, s[12:15], s84 offen offset:3072 sc1\n"
    "s_waitcnt vmcnt(0)\n"
    "v_mov_b32 v26, v188\n"
    "v_cmp_eq_u32 vcc, -1, v26\n"
    "s_sub_u32 s85, s85, 1\n"
    "s_cselect_b64 vcc, 0, vcc\n"
    "s_cbranch_vccnz Llp_235\n"
    "s_nop 3\n"
    "s_branch Lback_141\n"
    "Lagain_142:\n"
    "s_waitcnt vmcnt(0) lgkmcnt(0)\n"
    "s_mov_b32 s85, 0x10000\n"
    "Llp_236:\n"
    "s_mov_b32 s84, 0x6000\n"
    "buffer_load_dwordx4 v[196:199], v21, s[12:15], s84 offen sc1\n"
    "s_waitcnt vmcnt(0)\n"
    "v_mov_b32 v26, v196\n"
    "v_cmp_eq_u32 vcc, -1, v26\n"
    "s_sub_u32 s85, s85, 1\n"
    "s_cselect_b64 vcc, 0, vcc\n"
    "s_cbranch_vccnz Llp_236\n"
    "s_nop 3\n"
    "s_branch Lback_143\n"
    "Lagain_144:\n"
    "s_waitcnt vmcnt(0) lgkmcnt(0)\n"
    "s_mov_b32 s85, 0x10000\n"
    "Llp_237:\n"
    "s_mov_b32 s84, 0x6000\n"
    "buffer_load_dwordx4 v[204:207], v21, s[12:15], s84 offen offset:1024 sc1\n"
    "s_waitcnt vmcnt(0)\n"
    "v_mov_b32 v26, v204\n"
    "v_cmp_eq_u32 vcc, -1, v26\n"
    "s_sub_u32 s85, s85, 1\n"
    "s_cselect_b64 vcc, 0, vcc\n"
    "s_cbranch_vccnz Llp_237\n"
    "s_nop 3\n"
    "s_branch Lback_145\n"
    "Lagain_146:\n"
    "s_waitcnt vmcnt(0) lgkmcnt(0)\n"
    "s_mov_b32 s85, 0x10000\n"
    "Llp_238:\n"
    "s_mov_b32 s84, 0x6000\n"
    "buffer_load_dwordx4 v[212:215], v21, s[12:15], s84 offen offset:2048 sc1\n"
    "s_waitcnt vmcnt(0)\n"
    "v_mov_b32 v26, v212\n"
    "v_cmp_eq_u32 vcc, -1, v26\n"
    "s_sub_u32 s85, s85, 1\n"
    "s_cselect_b64 vcc, 0, vcc\n"
    "s_cbranch_vccnz Llp_238\n"
    "s_nop 3\n"
    "s_branch Lback_147\n"
    "Lagain_148:\n"
    "s_waitcnt vmcnt(0) lgkmcnt(0)\n"
    "s_mov_b32 s85, 0x10000\n"
    "Llp_239:\n"
    "s_mov_b32 s84, 0x6000\n"
    "buffer_load_dwordx4 v[220:223], v21, s[12:15], s84 offen offset:3072 sc1\n"
    "s_waitcnt vmcnt(0)\n"
    "v_mov_b32 v26, v220\n"
    "v_cmp_eq_u32 vcc, -1, v26\n"
    "s_sub_u32 s85, s85, 1\n"
    "s_cselect_b64 vcc, 0, vcc\n"
    "s_cbranch_vccnz Llp_239\n"
    "s_nop 3\n"
    "s_branch Lback_149\n"
    "Lagain_150:\n"
    "s_waitcnt vmcnt(0) lgkmcnt(0)\n"
    "s_mov_b32 s85, 0x10000\n"
    "Llp_240:\n"
    "s_mov_b32 s84, 0x5000\n"
    "buffer_load_dwordx4 v[176:179], v20, s[12:15], s84 offen offset:2048 sc1\n"
    "s_waitcnt vmcnt(0)\n"
    "v_mov_b32 v26, v176\n"
    "v_cmp_eq_u32 vcc, -1, v26\n"
    "s_sub_u32 s85, s85, 1\n"
    "s_cselect_b64 vcc, 0, vcc\n"
    "s_cbranch_vccnz Llp_240\n"
    "s_nop 3\n"
    "s_branch Lback_151\n"
    "Lagain_152:\n"
    "s_waitcnt vmcnt(0) lgkmcnt(0)\n"
    "s_mov_b32 s85, 0x10000\n"
    "Llp_241:\n"
    "s_mov_b32 s84, 0x5000\n"
    "buffer_load_dwordx4 v[184:187], v20, s[12:15], s84 offen offset:3072 sc1\n"
    "s_waitcnt vmcnt(0)\n"
    "v_mov_b32 v26, v184\n"
    "v_cmp_eq_u32 vcc, -1, v26\n"
    "s_sub_u32 s85, s85, 1\n"
    "s_cselect_b64 vcc, 0, vcc\n"
    "s_cbranch_vccnz Llp_241\n"
    "s_nop 3\n"
    "s_branch Lback_153\n"
    "Lagain_154:\n"
    "s_waitcnt vmcnt(0) lgkmcnt(0)\n"
    "s_mov_b32 s85, 0x10000\n"
    "Llp_242:\n"
    "s_mov_b32 s84, 0x6000\n"
    "buffer_load_dwordx4 v[192:195], v20, s[12:15], s84 offen sc1\n"
    "s_waitcnt vmcnt(0)\n"
    "v_mov_b32 v26, v192\n"
    "v_cmp_eq_u32 vcc, -1, v26\n"
    "s_sub_u32 s85, s85, 1\n"
    "s_cselect_b64 vcc, 0, vcc\n"
    "s_cbranch_vccnz Llp_242\n"
    "s_nop 3\n"
    "s_branch Lback_155\n"
    "Lagain_156:\n"
    "s_waitcnt vmcnt(0) lgkmcnt(0)\n"
    "s_mov_b32 s85, 0x10000\n"
    "Llp_243:\n"
    "s_mov_b32 s84, 0x6000\n"
    "buffer_load_dwordx4 v[200:203], v20, s[12:15], s84 offen offset:1024 sc1\n"
    "s_waitcnt vmcnt(0)\n"
    "v_mov_b32 v26, v200\n"
    "v_cmp_eq_u32 vcc, -1, v26\n"
    "s_sub_u32 s85, s85, 1\n"
    "s_cselect_b64 vcc, 0, vcc\n"
    "s_cbranch_vccnz Llp_243\n"
    "s_nop 3\n"
    "s_branch Lback_157\n"
    "Lagain_158:\n"
    "s_waitcnt vmcnt(0) lgkmcnt(0)\n"
    "s_mov_b32 s85, 0x10000\n"
    "Llp_244:\n"
    "s_mov_b32 s84, 0x6000\n"
    "buffer_load_dwordx4 v[208:211], v20, s[12:15], s84 offen offset:2048 sc1\n"
    "s_waitcnt vmcnt(0)\n"
    "v_mov_b32 v26, v208\n"
    "v_cmp_eq_u32 vcc, -1, v26\n"
    "s_sub_u32 s85, s85, 1\n"
    "s_cselect_b64 vcc, 0, vcc\n"
    "s_cbranch_vccnz Llp_244\n"
    "s_nop 3\n"
    "s_branch Lback_159\n"
    "Lagain_160:\n"
    "s_waitcnt vmcnt(0) lgkmcnt(0)\n"
    "s_mov_b32 s85, 0x10000\n"
    "Llp_245:\n"
    "s_mov_b32 s84, 0x6000\n"
    "buffer_load_dwordx4 v[216:219], v20, s[12:15], s84 offen offset:3072 sc1\n"
    "s_waitcnt vmcnt(0)\n"
    "v_mov_b32 v26, v216\n"
    "v_cmp_eq_u32 vcc, -1, v26\n"
    "s_sub_u32 s85, s85, 1\n"
    "s_cselect_b64 vcc, 0, vcc\n"
    "s_cbranch_vccnz Llp_245\n"
    "s_nop 3\n"
    "s_branch Lback_161\n"
    "L_slow:\n"
    "s_nop 15\n"
    "s_lshl_b32 s60, s48, 18\n"
    "s_add_u32 s60, s60, 0x100000\n"
    "s_mov_b32 s61, s60\n"
    "s_mov_b32 s62, 64\n"
    "v_mov_b32 v26, 0xff7fffff\n"
    "L_slow1:\n"
    "buffer_load_dwordx4 v[192:195], v1, s[12:15], s61 offen sc1\n"
    "buffer_load_dwordx4 v[196:199], v1, s[12:15], s61 offen offset:1024 sc1\n"
    "s_waitcnt vmcnt(0)\n"
    "v_mfma_f32_32x32x16_bf16 v[160:175], v[192:195], v[128:131], 0\n"
    "v_mfma_f32_32x32x16_bf16 v[160:175], v[196:199], v[132:135], v[160:175]\n"
    "s_nop 11\n"
    "v_max3_f32 v26, v160, v161, v26\n"
    "v_max3_f32 v26, v162, v163, v26\n"
    "v_max3_f32 v26, v164, v165, v26\n"
    "v_max3_f32 v26, v166, v167, v26\n"
    "v_max3_f32 v26, v168, v169, v26\n"
    "v_max3_f32 v26, v170, v171, v26\n"
    "v_max3_f32 v26, v172, v173, v26\n"
    "v_max3_f32 v26, v174, v175, v26\n"
    "s_add_u32 s61, s61, 0x1000\n"
    "s_sub_u32 s62, s62, 1\n"
    "s_cmp_lg_u32 s62, 0\n"
    "s_nop 7\n"
    "s_cbranch_scc1 L_slow1\n"
    "v_mov_b32 v27, v26\n"
    "s_nop 1\n"
    "v_permlane32_swap_b32 v26, v27\n"
    "v_max_f32 v26, v26, v27\n"
    "v_sub_f32 v26, 0, v26\n"
    "v_cvt_pk_bf16_f32 v40, v26, v26\n"
    "v_and_b32 v41, 0xffff0000, v40\n"
    "v_sub_f32 v26, v26, v41\n"
    "v_cvt_pk_bf16_f32 v42, v26, v26\n"
    "v_and_b32 v41, 0xffff0000, v42\n"
    "v_sub_f32 v26, v26, v41\n"
    "v_cvt_pk_bf16_f32 v43, v26, 0\n"
    "v_mov_b32 v36, v132\n"
    "v_mov_b32 v37, v133\n"
    "v_mov_b32 v38, v134\n"
    "v_mov_b32 v39, v135\n"
    "s_mov_b32 exec_lo, 0\n"
    "s_mov_b32 s61, 0xffff\n"
    "v_bfi_b32 v38, s61, v40, v42\n"
    "v_mov_b32 v39, v43\n"
    "s_mov_b32 exec_lo, -1\n"
    "v_mov_b32 v64, 0\n"
    "v_mov_b32 v65, 0\n"
    "v_mov_b32 v66, 0\n"
    "v_mov_b32 v67, 0\n"
    "v_mov_b32 v68, 0\n"
    "v_mov_b32 v69, 0\n"
    "v_mov_b32 v70, 0\n"
    "v_mov_b32 v71, 0\n"
    "v_mov_b32 v72, 0\n"
    "v_mov_b32 v73, 0\n"
    "v_mov_b32 v74, 0\n"
    "v_mov_b32 v75, 0\n"
    "v_mov_b32 v76, 0\n"
    "v_mov_b32 v77, 0\n"
    "v_mov_b32 v78, 0\n"
    "v_mov_b32 v79, 0\n"
    "s_mov_b32 s61, s60\n"
    "s_mov_b32 s62, 64\n"
    "L_slow2:\n"
    "buffer_load_dwordx4 v[192:195], v1, s[12:15], s61 offen sc1\n"
    "buffer_load_dwordx4 v[196:199], v1, s[12:15], s61 offen offset:1024 sc1\n"
    "buffer_load_dwordx4 v[200:203], v1, s[12:15], s61 offen offset:2048 sc1\n"
    "buffer_load_dwordx4 v[204:207], v1, s[12:15], s61 offen offset:3072 sc1\n"
    "s_waitcnt vmcnt(0)\n"
    "v_mfma_f32_32x32x16_bf16 v[160:175], v[192:195], v[128:131], 0\n"
    "v_mfma_f32_32x32x16_bf16 v[160:175], v[196:199], v[36:39], v[160:175]\n"
    "s_nop 11\n"
    "v_exp_f32 v160, v160\n"
    "v_exp_f32 v161, v161\n"
    "v_exp_f32 v162, v162\n"
    "v_exp_f32 v163, v163\n"
    "v_exp_f32 v164, v164\n"
    "v_exp_f32 v165, v165\n"
    "v_exp_f32 v166, v166\n"
    "v_exp_f32 v167, v167\n"
    "v_exp_f32 v168, v168\n"
    "v_exp_f32 v169, v169\n"
    "v_exp_f32 v170, v170\n"
    "v_exp_f32 v171, v171\n"
    "v_exp_f32 v172, v172\n"
    "v_exp_f32 v173, v173\n"
    "v_exp_f32 v174, v174\n"
    "v_exp_f32 v175, v175\n"
    "v_cvt_pk_bf16_f32 v28, v160, v161\n"
    "v_cvt_pk_bf16_f32 v29, v162, v163\n"
    "v_cvt_pk_bf16_f32 v30, v164, v165\n"
    "v_cvt_pk_bf16_f32 v31, v166, v167\n"
    "v_cvt_pk_bf16_f32 v32, v168, v169\n"
    "v_cvt_pk_bf16_f32 v33, v170, v171\n"
    "v_cvt_pk_bf16_f32 v34, v172, v173\n"
    "v_cvt_pk_bf16_f32 v35, v174, v175\n"
    "v_mfma_f32_32x32x16_bf16 v[64:79], v[200:203], v[28:31], v[64:79]\n"
    "s_nop 0\n"
    "v_mfma_f32_32x32x16_bf16 v[64:79], v[204:207], v[32:35], v[64:79]\n"
    "s_add_u32 s61, s61, 0x1000\n"
    "s_sub_u32 s62, s62, 1\n"
    "s_cmp_lg_u32 s62, 0\n"
    "s_nop 15\n"
    "s_cbranch_scc1 L_slow2\n"
    "s_lshl2_add_u32 s60, s49, s44\n"
    "s_lshl4_add_u32 s60, s60, s48\n"
    "s_lshl_b32 s61, s60, 11\n"
    "s_add_u32 s61, s61, 0x500000\n"
    "s_mov_b32 s62, 0x1f800000\n"
    "s_mov_b32 s63, 0x5f800000\n"
    "v_mov_b32 v24, v76\n"
    "v_mov_b32 v25, v76\n"
    "s_nop 1\n"
    "v_permlane32_swap_b32 v24, v25\n"
    "v_rcp_f32 v24, v25\n"
    "s_nop 0\n"
    "v_pk_mul_f32 v[64:65], v[64:65], v[24:25] op_sel_hi:[1,0]\n"
    "v_pk_mul_f32 v[66:67], v[66:67], v[24:25] op_sel_hi:[1,0]\n"
    "v_pk_mul_f32 v[68:69], v[68:69], v[24:25] op_sel_hi:[1,0]\n"
    "v_pk_mul_f32 v[70:71], v[70:71], v[24:25] op_sel_hi:[1,0]\n"
    "v_pk_mul_f32 v[72:73], v[72:73], v[24:25] op_sel_hi:[1,0]\n"
    "v_pk_mul_f32 v[74:75], v[74:75], v[24:25] op_sel_hi:[1,0]\n"
    "v_pk_mul_f32 v[76:77], v[76:77], v[24:25] op_sel_hi:[1,0]\n"
    "v_pk_mul_f32 v[78:79], v[78:79], v[24:25] op_sel_hi:[1,0]\n"
    "v_cvt_pk_bf16_f32 v28, v64, v65\n"
    "v_cvt_pk_bf16_f32 v29, v66, v67\n"
    "v_cvt_pk_bf16_f32 v30, v68, v69\n"
    "v_cvt_pk_bf16_f32 v31, v70, v71\n"
    "v_cvt_pk_bf16_f32 v32, v72, v73\n"
    "v_cvt_pk_bf16_f32 v33, v74, v75\n"
    "v_cvt_pk_bf16_f32 v34, v76, v77\n"
    "v_cvt_pk_bf16_f32 v35, v78, v79\n"
    "s_mov_b32 exec_lo, 0\n"
    "v_mov_b32 v34, 0\n"
    "v_mov_b32 v35, 0\n"
    "s_mov_b32 exec_lo, -1\n"
    "s_nop 3\n"
    "s_branch L_slow_back\n"
    "mk_end:\n"
    : "+{v0}"(tid)
    : "s"(__builtin_amdgcn_kernarg_segment_ptr()), "s"(blockIdx.x)
    : "v1", "v2", "v3", "v4", "v5", "v6", "v7", "v8", "v9", "v10", "v11", "v12", "v13", "v14", "v15", "v16", "v17", "v18", "v19", "v20", "v21", "v22", "v23", "v24",
      "v25", "v26", "v27", "v28", "v29", "v30", "v31", "v32", "v33", "v34", "v35", "v36", "v37", "v38", "v39", "v40", "v41", "v42", "v43", "v44", "v45", "v46", "v47", "v48",
      "v49", "v50", "v51", "v52", "v53", "v54", "v55", "v56", "v57", "v58", "v59", "v60", "v61", "v62", "v63", "v64", "v65", "v66", "v67", "v68", "v69", "v70", "v71", "v72",
      "v73", "v74", "v75", "v76", "v77", "v78", "v79", "v80", "v81", "v82", "v83", "v84", "v85", "v86", "v87", "v88", "v89", "v90", "v91", "v92", "v93", "v94", "v95", "v96",
      "v97", "v98", "v99", "v100", "v101", "v102", "v103", "v104", "v105", "v106", "v107", "v108", "v109", "v110", "v111", "v112", "v113", "v114", "v115", "v116", "v117", "v118", "v119", "v120",
      "v121", "v122", "v123", "v124", "v125", "v126", "v127", "v128", "v129", "v130", "v131", "v132", "v133", "v134", "v135", "v136", "v137", "v138", "v139", "v140", "v141", "v142", "v143", "v144",
      "v145", "v146", "v147", "v148", "v149", "v150", "v151", "v152", "v153", "v154", "v155", "v156", "v157", "v158", "v159", "v160", "v161", "v162", "v163", "v164", "v165", "v166", "v167", "v168",
      "v169", "v170", "v171", "v172", "v173", "v174", "v175", "v176", "v177", "v178", "v179", "v180", "v181", "v182", "v183", "v184", "v185", "v186", "v187", "v188", "v189", "v190", "v191", "v192",
      "v193", "v194", "v195", "v196", "v197", "v198", "v199", "v200", "v201", "v202", "v203", "v204", "v205", "v206", "v207", "v208", "v209", "v210", "v211", "v212", "v213", "v214", "v215", "v216",
      "v217", "v218", "v219", "v220", "v221", "v222", "v223", "v224", "v225", "v226", "v227", "v228", "v229", "v230", "v231", "v232", "v233", "v234", "v235", "v236", "v237", "v238", "v239", "v240",
      "v241", "v242", "v243", "v244", "v245", "v246", "v247", "v248", "v249", "v250", "v251", "v252", "v253", "v254", "v255", "a0", "a1", "a2", "a3", "a4", "a5", "a6", "a7", "a8",
      "a9", "a10", "a11", "a12", "a13", "a14", "a15", "a16", "a17", "a18", "a19", "a20", "a21", "a22", "a23", "a24", "a25", "a26", "a27", "a28", "a29", "a30", "a31", "a32",
      "a33", "a34", "a35", "a36", "a37", "a38", "a39", "a40", "a41", "a42", "a43", "a44", "a45", "a46", "a47", "a48", "a49", "a50", "a51", "a52", "a53", "a54", "a55", "a56",
      "a57", "a58", "a59", "a60", "a61", "a62", "a63", "a64", "a65", "a66", "a67", "a68", "a69", "a70", "a71", "a72", "a73", "a74", "a75", "a76", "a77", "a78", "a79", "a80",
      "a81", "a82", "a83", "a84", "a85", "a86", "a87", "a88", "a89", "a90", "a91", "a92", "a93", "a94", "a95", "a96", "a97", "a98", "a99", "a100", "a101", "a102", "a103", "a104",
      "a105", "a106", "a107", "a108", "a109", "a110", "a111", "a112", "a113", "a114", "a115", "a116", "a117", "a118", "a119", "a120", "a121", "a122", "a123", "a124", "a125", "a126", "a127", "a128",
      "a129", "a130", "a131", "a132", "a133", "a134", "a135", "a136", "a137", "a138", "a139", "a140", "a141", "a142", "a143", "a144", "a145", "a146", "a147", "a148", "a149", "a150", "a151", "a152",
      "a153", "a154", "a155", "a156", "a157", "a158", "a159", "a160", "a161", "a162", "a163", "a164", "a165", "a166", "a167", "a168", "a169", "a170", "a171", "a172", "a173", "a174", "a175", "a176",
      "a177", "a178", "a179", "a180", "a181", "a182", "a183", "a184", "a185", "a186", "a187", "a188", "a189", "a190", "a191", "a192", "a193", "a194", "a195", "a196", "a197", "a198", "a199", "a200",
      "a201", "a202", "a203", "a204", "a205", "a206", "a207", "a208", "a209", "a210", "a211", "a212", "a213", "a214", "a215", "a216", "a217", "a218", "a219", "a220", "a221", "a222", "a223", "a224",
      "a225", "a226", "a227", "a228", "a229", "a230", "a231", "a232", "a233", "a234", "a235", "a236", "a237", "a238", "a239", "a240", "a241", "a242", "a243", "a244", "a245", "a246", "a247", "a248",
      "a249", "a250", "a251", "a252", "a253", "a254", "a255", "s4", "s5", "s6", "s7", "s8", "s9", "s10", "s11", "s12", "s13", "s14", "s15", "s16", "s17", "s18", "s19", "s20",
      "s21", "s22", "s23", "s24", "s25", "s26", "s27", "s28", "s29", "s30", "s31", "s32", "s33", "s34", "s35", "s36", "s37", "s38", "s39", "s40", "s41", "s42", "s43", "s44",
      "s45", "s46", "s47", "s48", "s49", "s50", "s51", "s52", "s53", "s54", "s55", "s56", "s57", "s58", "s59", "s60", "s61", "s62", "s63", "s64", "s65", "s66", "s67", "s68",
      "s69", "s70", "s71", "s72", "s73", "s74", "s75", "s76", "s77", "s78", "s79", "s80", "s81", "s82", "s83", "s84", "s85", "s86", "s87", "s88", "s89", "s90", "s91", "s92",
      "s93", "s94", "s95", "s96", "s97", "s98", "s99", "s100", "s101", "vcc", "scc", "memory");
}

extern "C" void kernel_launch(void* const* d_in, const int* in_sizes, int n_in, void* d_out, int out_size, void* d_ws, size_t ws_size, hipStream_t stream) {
  (void)hipFuncSetAttribute((const void*)fused_block, hipFuncAttributeMaxDynamicSharedMemorySize, LDS_BYTES);
  fused_block<<<256, 256, LDS_BYTES, stream>>>(d_in[0], d_in[1], d_ws, d_in[4], d_in[2], d_in[6], d_in[5], d_in[7], d_out);
}
```
